```python
import torch

def _softmax(x, axis=-1):
    return torch.softmax(x, dim=axis)
def _gelu(x):
    return torch.nn.functional.gelu(x, approximate="tanh")
def _silu(x):
    return torch.nn.functional.silu(x)
def _sigmoid(x):
    return torch.sigmoid(x)
def _layernorm(x, axis=-1, eps=1e-5):
    m = x.mean(dim=axis, keepdim=True)
    v = x.var(dim=axis, keepdim=True, unbiased=False)
    return (x - m) / torch.sqrt(v + eps)
def _scatter_add(tgt, idx, src, axis=0):
    return tgt.clone().index_add_(axis, idx, src)
def _segment_sum(src, seg, n):
    out = torch.zeros((n,) + src.shape[1:], dtype=src.dtype)
    return out.index_add_(0, seg, src)
def _pad(x, pw):
    flat = tuple(v for pair in reversed(pw) for v in pair)
    return torch.nn.functional.pad(x, flat)

kernel_name = 'sequential_preLN__grouped_kv_mqa__dense_activation_squared_relu__prompt'

def setup_inputs():

    _w = (lambda a: (lambda t: t.to(torch.bfloat16) if t.is_floating_point() else t)(torch.as_tensor(a)))
    def _A_setup():
        B, S, D, H = 1, 2048, 576, 32
        Dh = D // H
        return {
            "x": _w((lambda *s: torch.randn(*s, dtype=torch.bfloat16) * 0.05)(B, S, D)),
            "wq": _w((lambda *s: torch.randn(*s, dtype=torch.bfloat16) * 0.05)(D, H * Dh)),
            "wk": _w((lambda *s: torch.randn(*s, dtype=torch.bfloat16) * 0.05)(D, Dh)),
            "wv": _w((lambda *s: torch.randn(*s, dtype=torch.bfloat16) * 0.05)(D, Dh)),
            "wo": _w((lambda *s: torch.randn(*s, dtype=torch.bfloat16) * 0.05)(H * Dh, D)),
        }
    _A_in = _A_setup()

    def _B_setup():
        B, S, D, F = 1, 2048, 576, 4608
        return {
            "x": _w((lambda *s: torch.randn(*s, dtype=torch.bfloat16) * 0.05)(B, S, D)),
            "w1": _w((lambda *s: torch.randn(*s, dtype=torch.bfloat16) * 0.05)(D, F)),
            "b1": _w((lambda *s: torch.randn(*s, dtype=torch.bfloat16) * 0.4)(F)),
            "w2": _w((lambda *s: torch.randn(*s, dtype=torch.bfloat16) * 0.05)(F, D)),
            "b2": _w((lambda *s: torch.randn(*s, dtype=torch.bfloat16) * 3.2)(D)),
        }
    _B_in = _B_setup()

    return {
        "A_x": _A_in["x"],
        "A_wq": _A_in["wq"],
        "A_wk": _A_in["wk"],
        "A_wv": _A_in["wv"],
        "A_wo": _A_in["wo"],
        "B_x": _B_in["x"],
        "B_w1": _B_in["w1"],
        "B_b1": _B_in["b1"],
        "B_w2": _B_in["w2"],
        "B_b2": _B_in["b2"],
    }

def reference(A_x, A_wq, A_wk, A_wv, A_wo, B_x, B_w1, B_b1, B_w2, B_b2):
    def _A_ref(x, wq, wk, wv, wo):
        B, S, D = x.shape
        H = 32
        Dh = D // H
        q = (lambda x, *d: x.permute(*d))((x @ wq).reshape(B, S, H, Dh), 0, 2, 1, 3)
        k = x @ wk
        v = x @ wv
        scores = torch.einsum("bhqd,bkd->bhqk", q, k) * (Dh ** -0.5)
        p = _softmax(scores, axis=-1)
        o = torch.einsum("bhqk,bkd->bhqd", p, v)
        return (lambda x, *d: x.permute(*d))(o, 0, 2, 1, 3).reshape(B, S, H * Dh) @ wo

    def _B_ref(x, w1, b1, w2, b2):
        h = torch.einsum("bsd,df->bsf", x, w1) + b1
        zero = torch.zeros_like(h)
        r = torch.where(h > zero, h, zero)
        h = r * r
        out = torch.einsum("bsf,fd->bsd", h, w2) + b2
        return out

    x = A_x
    h = x + _A_ref(_layernorm(x, axis=-1), A_wq, A_wk, A_wv, A_wo)
    h2 = h + _B_ref(_layernorm(h, axis=-1), B_w1, B_b1, B_w2, B_b2)
    return h2
```

```cpp
#include <hip/hip_runtime.h>
#include <hip/hip_bf16.h>
#include <stdint.h>

#define GAS __attribute__((address_space(1)))
#define LAS __attribute__((address_space(3)))
typedef unsigned short u16;
typedef unsigned v4u __attribute__((ext_vector_type(4)));
typedef unsigned v2u __attribute__((ext_vector_type(2)));
typedef float f32x4 __attribute__((ext_vector_type(4)));
typedef float f32x8 __attribute__((ext_vector_type(8)));
typedef float f32x16 __attribute__((ext_vector_type(16)));
typedef __bf16 bf16x8 __attribute__((ext_vector_type(8)));
typedef __bf16 bf16x16 __attribute__((ext_vector_type(16)));
typedef __bf16 bf16x2 __attribute__((ext_vector_type(2)));
typedef short s16x4 __attribute__((ext_vector_type(4)));

constexpr int S = 2048, D = 576, DH = 18, F = 4608;
constexpr int KS = 18;
constexpr int KVT = 2496;
constexpr float EPS = 1e-5f;
constexpr float QSCALE = 0.23570226039551584f * 1.4426950408889634f;
constexpr float MSAFE = 48.f;

constexpr size_t WS_Q = 0;
constexpr size_t WS_KV = WS_Q + (size_t)S * D * 2;
constexpr size_t WS_KN = WS_KV + 163840;
constexpr size_t WS_O = WS_KN + 1024;
constexpr size_t WS_H = WS_O + (size_t)S * D * 2;
constexpr size_t WS_WO = WS_H + (size_t)S * D * 2;
constexpr size_t WS_W12 = WS_WO + (size_t)D * D * 2;
constexpr size_t WS_P = WS_W12 + (size_t)2 * D * F * 2 + 65536;

__device__ __forceinline__ float bflo(unsigned u) { return __builtin_bit_cast(float, u << 16); }
__device__ __forceinline__ float bfhi(unsigned u) { return __builtin_bit_cast(float, u & 0xffff0000u); }
__device__ __forceinline__ unsigned pk2(float lo, float hi) {
  bf16x2 r; r[0] = (__bf16)lo; r[1] = (__bf16)hi; return __builtin_bit_cast(unsigned, r);
}
__device__ __forceinline__ f32x4 mfma16(v4u a, v4u b, f32x4 c) {
  return __builtin_amdgcn_mfma_f32_16x16x32_bf16(__builtin_bit_cast(bf16x8, a), __builtin_bit_cast(bf16x8, b), c, 0, 0, 0);
}
__device__ __forceinline__ void glds16(const void* g, unsigned lds_base) {
  unsigned sv;
  asm volatile("s_mov_b32 %0, m0\n\ts_mov_b32 m0, %2\n\ts_nop 0\n\tglobal_load_lds_dwordx4 %1, off\n\ts_mov_b32 m0, %0"
               : "=&s"(sv) : "v"(g), "s"(lds_base) : "memory");
}
#define SBAR() __builtin_amdgcn_sched_barrier(0)

__device__ __forceinline__ void ln_frags(v4u (&x)[KS]) {
  float sum = 0.f;
#pragma unroll
  for (int s = 0; s < KS; ++s)
#pragma unroll
    for (int e = 0; e < 4; ++e) sum += bflo(x[s][e]) + bfhi(x[s][e]);
  sum += __shfl_xor(sum, 16); sum += __shfl_xor(sum, 32);
  const float mean = sum * (1.f / D);
  float sq = 0.f;
#pragma unroll
  for (int s = 0; s < KS; ++s)
#pragma unroll
    for (int e = 0; e < 4; ++e) {
      const float a = bflo(x[s][e]) - mean, b = bfhi(x[s][e]) - mean;
      sq = __builtin_fmaf(a, a, sq); sq = __builtin_fmaf(b, b, sq);
    }
  sq += __shfl_xor(sq, 16); sq += __shfl_xor(sq, 32);
  const float rstd = 1.f / __builtin_sqrtf(sq * (1.f / D) + EPS);
  const float nb = -mean * rstd;
#pragma unroll
  for (int s = 0; s < KS; ++s)
#pragma unroll
    for (int e = 0; e < 4; ++e)
      x[s][e] = pk2(__builtin_fmaf(bflo(x[s][e]), rstd, nb), __builtin_fmaf(bfhi(x[s][e]), rstd, nb));
}

constexpr int NJOB_W1 = 18 * 72, NJOB_W2 = 144 * 9, NJOB_WO = 18 * 9, NJOB = NJOB_W1 + NJOB_W2 + NJOB_WO;
__device__ __forceinline__ size_t w12_off(int j, int phase, int tl, int s) {
  const int w = tl / 9, t = tl - 9 * w;
  return ((size_t)(((j * 4 + w) * 2 + phase) * 9 + t) * KS + s) * 1024;
}
__device__ __forceinline__ void pretile_job(int job, int lane, const u16* w1, const u16* w2, const u16* wo, unsigned char* ws) {
  const int cs = lane & 15, g = lane >> 4;
  const int ul = 2 * (cs >> 3) + (cs & 1), c0 = 4 * ((cs >> 1) & 3);
  v2u r[8];
  unsigned char* dst;
  if (job < NJOB_W1) {
    const int s = job / 72, blk = job - 72 * s;
    const u16* src = w1 + (size_t)(32 * s + 8 * g) * F + blk * 64 + 4 * cs;
#pragma unroll
    for (int jj = 0; jj < 8; ++jj) r[jj] = *(const v2u*)(src + (size_t)jj * F);
    const int j = blk / 9, tl = (blk - 9 * j) * 4 + ul;
    dst = ws + WS_W12 + w12_off(j, 0, tl, s);
  } else if (job < NJOB_W1 + NJOB_W2) {
    const int q = job - NJOB_W1, sg = q / 9, blk = q - 9 * sg;
    const u16* src = w2 + (size_t)(32 * sg + 8 * g) * D + blk * 64 + 4 * cs;
#pragma unroll
    for (int jj = 0; jj < 8; ++jj) r[jj] = *(const v2u*)(src + (size_t)jj * D);
    const int j = sg / KS, s = sg - KS * j;
    dst = ws + WS_W12 + w12_off(j, 1, blk * 4 + ul, s);
  } else {
    const int q = job - NJOB_W1 - NJOB_W2, s = q / 9, blk = q - 9 * s;
    const int chunk = 4 * s + g, jq = chunk / 9, cc = chunk - 9 * jq;
#pragma unroll
    for (int jj = 0; jj < 8; ++jj) {
      const int row = cc < 8 ? (4 * jq + (cc >> 1)) * DH + 4 * (cc & 1) + (jj & 3) + 8 * (jj >> 2)
                             : (4 * jq + (jj >> 1)) * DH + 16 + (jj & 1);
      r[jj] = *(const v2u*)(wo + (size_t)row * D + blk * 64 + 4 * cs);
    }
    dst = ws + WS_WO + ((size_t)(blk * 4 + ul) * KS + s) * 1024;
  }
  dst += (g * 16 + c0) * 16;
#pragma unroll
  for (int i = 0; i < 4; ++i) {
    v4u o;
#pragma unroll
    for (int m = 0; m < 4; ++m)
      o[m] = __builtin_amdgcn_perm(r[2 * m + 1][i >> 1], r[2 * m][i >> 1], (i & 1) ? 0x07060302u : 0x05040100u);
    *(v4u*)(dst + i * 16) = o;
  }
}
__global__ void __launch_bounds__(256) k_pretile(const u16* w1, const u16* w2, const u16* wo, unsigned char* ws) {
  const int job = blockIdx.x * 4 + (threadIdx.x >> 6);
  if (job < NJOB) pretile_job(job, threadIdx.x & 63, w1, w2, wo, ws);
}

constexpr int K1_LDS = 576 * 160;
typedef short v4i16_t __attribute__((ext_vector_type(4)));
__device__ __forceinline__ v2u tr_read(const LAS unsigned char* p) {
  return __builtin_bit_cast(v2u, __builtin_amdgcn_ds_read_tr16_b64_v4i16((LAS v4i16_t*)p));
}
__device__ __forceinline__ int k1_row(int k) {
  const int kl = k & 31;
  return (k & ~31) + 16 * ((kl >> 2) & 1) + 4 * (kl >> 3) + (kl & 3);
}
__global__ void __launch_bounds__(256, 1) k_qkv(const u16* x, const u16* wq, const u16* wk, const u16* wv, unsigned char* ws) {
  extern __shared__ __attribute__((aligned(16))) unsigned char smem[];
  LAS unsigned char* L = (LAS unsigned char*)smem;
  const int tid = threadIdx.x, lane = tid & 63, w = __builtin_amdgcn_readfirstlane(tid >> 6);
  const int b = blockIdx.x, G = (b & 7) * 4 + (b >> 6), j = (b >> 3) & 7;
  const int tok = lane & 15, g = lane >> 4;
  const int token = 64 * G + 16 * w + tok;
  const unsigned lds0 = (unsigned)(uintptr_t)L;
  v4u xf[KS];
  {
    const u16* xp = x + (size_t)token * D + 8 * g;
#pragma unroll
    for (int s = 0; s < KS; ++s) xf[s] = *(const v4u*)(xp + 32 * s);
  }
  if (j < 7) {
    const int rin = lane / 10, ch = lane - 10 * rin;
    if (lane < 60) {
#pragma unroll
      for (int i = 0; i < 24; ++i) {
        const int rho = 6 * (4 * i + w) + rin, p = rho & 31;
        const int k = (rho & ~31) + 8 * ((p & 15) >> 2) + 4 * (p >> 4) + (p & 3);
        glds16(wq + (size_t)k * D + 80 * j + 8 * ch, lds0 + (4 * i + w) * 960);
      }
    }
  } else {
    for (int k = tid; k < D; k += 256) {
      const v4u q0 = *(const v4u*)(wq + (size_t)k * D + 560), q1 = *(const v4u*)(wq + (size_t)k * D + 568);
      const unsigned* kp = (const unsigned*)(wk + (size_t)k * DH);
      const unsigned* vp = (const unsigned*)(wv + (size_t)k * DH);
      unsigned kr[9], vr[9];
#pragma unroll
      for (int i = 0; i < 9; ++i) { kr[i] = kp[i]; vr[i] = vp[i]; }
      LAS unsigned char* row = L + k1_row(k) * 160;
      *(LAS v4u*)(row) = q0; *(LAS v4u*)(row + 16) = q1;
      *(LAS v4u*)(row + 32) = v4u{kr[0], kr[1], kr[2], kr[3]}; *(LAS v4u*)(row + 48) = v4u{kr[4], kr[5], kr[6], kr[7]};
      *(LAS v4u*)(row + 64) = v4u{kr[8], vr[8], 0u, 0u}; *(LAS v4u*)(row + 80) = v4u{0u, 0u, 0u, 0u};
      *(LAS v4u*)(row + 96) = v4u{vr[0], vr[1], vr[2], vr[3]}; *(LAS v4u*)(row + 112) = v4u{vr[4], vr[5], vr[6], vr[7]};
    }
  }
  ln_frags(xf);
  asm volatile("s_waitcnt vmcnt(0) lgkmcnt(0)\n\ts_barrier" ::: "memory");
  const LAS unsigned char* ap = L + (4 * g + ((lane & 15) >> 2)) * 160 + (lane & 3) * 8;
  f32x4 acc[5];
#pragma unroll
  for (int t = 0; t < 5; ++t) acc[t] = f32x4{0.f, 0.f, 0.f, 0.f};
  if (j < 7) {
#pragma unroll
    for (int s = 0; s < KS; ++s)
#pragma unroll
      for (int t = 0; t < 5; ++t) {
        const v2u a0 = tr_read(ap + s * 32 * 160 + t * 32), a1 = tr_read(ap + (s * 32 + 16) * 160 + t * 32);
        acc[t] = mfma16(v4u{a0[0], a0[1], a1[0], a1[1]}, xf[s], acc[t]);
      }
    u16* qp = (u16*)(ws + WS_Q) + (size_t)token * D + 80 * j + 4 * g;
#pragma unroll
    for (int t = 0; t < 5; ++t)
      *(v2u*)(qp + 16 * t) = v2u{pk2(acc[t][0] * QSCALE, acc[t][1] * QSCALE), pk2(acc[t][2] * QSCALE, acc[t][3] * QSCALE)};
  } else {
#pragma unroll
    for (int s = 0; s < KS; ++s)
#pragma unroll
      for (int t = 0; t < 4; ++t) {
        const v2u a0 = tr_read(ap + s * 32 * 160 + t * 32), a1 = tr_read(ap + (s * 32 + 16) * 160 + t * 32);
        const v4u a = v4u{a0[0], a0[1], a1[0], a1[1]};
        acc[t] = t < 3 ? mfma16(a, xf[s], acc[t]) : mfma16(xf[s], a, acc[t]);
      }
    u16* qp = (u16*)(ws + WS_Q) + (size_t)token * D + 560 + 4 * g;
    *(v2u*)qp = v2u{pk2(acc[0][0] * QSCALE, acc[0][1] * QSCALE), pk2(acc[0][2] * QSCALE, acc[0][3] * QSCALE)};
    unsigned char* kvt = ws + WS_KV + (size_t)(token >> 5) * KVT;
    const int key = token & 31;
    const v2u k0 = v2u{pk2(acc[1][0], acc[1][1]), pk2(acc[1][2], acc[1][3])};
    *(v2u*)(kvt + ((g >> 1) * 32 + key) * 16 + (g & 1) * 8) = k0;
    const unsigned k16 = pk2(acc[2][0], acc[2][1]), v16 = pk2(acc[2][2], acc[2][3]);
    float n2 = bflo(k0[0]) * bflo(k0[0]) + bfhi(k0[0]) * bfhi(k0[0]) + bflo(k0[1]) * bflo(k0[1]) + bfhi(k0[1]) * bfhi(k0[1]);
    unsigned char* vt = kvt + 1280 + (w & 1) * 608;
    if (g == 0) {
      n2 += bflo(k16) * bflo(k16) + bfhi(k16) * bfhi(k16);
      *(v2u*)(kvt + 1024 + key * 8) = v2u{k16, 0x3f803f80u};
      *(u16*)(vt + (tok >> 3) * 304 + 16 * 16 + (tok & 7) * 2) = (u16)(v16 & 0xffffu);
      *(u16*)(vt + (tok >> 3) * 304 + 17 * 16 + (tok & 7) * 2) = (u16)(v16 >> 16);
    }
    *(v2u*)(vt + (g >> 1) * 304 + tok * 16 + (g & 1) * 8) = v2u{pk2(acc[3][0], acc[3][1]), pk2(acc[3][2], acc[3][3])};
    if (lane < 2) *(v4u*)(vt + lane * 304 + 18 * 16) = v4u{0x3f803f80u, 0x3f803f80u, 0x3f803f80u, 0x3f803f80u};
    n2 += __shfl_xor(n2, 16); n2 += __shfl_xor(n2, 32);
#pragma unroll
    for (int m = 1; m < 16; m <<= 1) n2 = __builtin_fmaxf(n2, __shfl_xor(n2, m));
    if (lane == 0) ((float*)(ws + WS_KN))[4 * G + w] = n2;
  }
}

__device__ __forceinline__ f32x16 qk(v4u k0, v2u k1, v4u q0, v2u q1) {
  f32x16 z = {};
  f32x16 s = __builtin_amdgcn_mfma_f32_32x32x16_bf16(__builtin_bit_cast(bf16x8, k0), __builtin_bit_cast(bf16x8, q0), z, 0, 0, 0);
  return __builtin_amdgcn_mfma_f32_32x32x8bf16_1k(__builtin_bit_cast(s16x4, k1), __builtin_bit_cast(s16x4, q1), s, 0, 0, 0);
}
__device__ __forceinline__ f32x16 pv(v4u v, f32x8 p, f32x16 o) {
  return __builtin_amdgcn_smfmac_f32_32x32x32_bf16(__builtin_bit_cast(bf16x8, v), __builtin_bit_cast(bf16x16, p), o, 0xDDDD, 0, 0);
}
__device__ __forceinline__ unsigned bf_trunc(float f) { return __builtin_bit_cast(unsigned, f) >> 16; }

__global__ void __launch_bounds__(512, 2) k_attn(unsigned char* ws) {
  extern __shared__ __attribute__((aligned(16))) unsigned char smem[];
  LAS unsigned char* L = (LAS unsigned char*)smem;
  const int tid = threadIdx.x, lane = tid & 63, w = __builtin_amdgcn_readfirstlane(tid >> 6);
  const int b = blockIdx.x, G = b >> 3, j = b & 7;
  const int r32 = lane & 31, hi = lane >> 5;
  const int hl = w >> 1, head = 4 * j + hl, token = 64 * G + 32 * (w & 1) + r32;
  const unsigned lds0 = (unsigned)(uintptr_t)L;
  const unsigned* qp = (const unsigned*)((const u16*)(ws + WS_Q) + (size_t)token * D + head * DH);
  v4u q0; unsigned q16;
  q0[0] = qp[4 * hi]; q0[1] = qp[4 * hi + 1]; q0[2] = qp[4 * hi + 2]; q0[3] = qp[4 * hi + 3];
  q16 = qp[8];
  const float* knp = (const float*)(ws + WS_KN);
  float kn = __builtin_fmaxf(knp[lane], knp[64 + lane]);
  {
    const unsigned char* src = ws + WS_KV + w * 1024 + lane * 16;
#pragma unroll
    for (int i = 0; i < 20; ++i) glds16(src + i * 8192, lds0 + w * 1024 + i * 8192);
  }
#pragma unroll
  for (int m = 1; m < 64; m <<= 1) kn = __builtin_fmaxf(kn, __shfl_xor(kn, m));
  float qn = 0.f;
#pragma unroll
  for (int e = 0; e < 4; ++e) qn += bflo(q0[e]) * bflo(q0[e]) + bfhi(q0[e]) * bfhi(q0[e]);
  qn += __shfl_xor(qn, 32);
  qn += bflo(q16) * bflo(q16) + bfhi(q16) * bfhi(q16);
  float M = __builtin_sqrtf(qn * kn) * 1.001f;
  asm volatile("s_waitcnt vmcnt(0)\n\ts_barrier" ::: "memory");

  const LAS unsigned char* kp0 = L + lane * 16;
  const LAS unsigned char* kp1 = L + 1024 + r32 * 8;
  const int vrow = r32 < 19 ? r32 : ((r32 >= 20 && r32 < 28) ? 0 : 4);
  const LAS unsigned char* vp = L + 1280 + hi * 304 + vrow * 16;

  v2u q1;
  if (__any(!(M <= MSAFE))) {
    q1[0] = hi ? 0u : q16; q1[1] = 0u;
    float mx = -INFINITY;
    for (int t = 0; t < 64; ++t) {
      const f32x16 s = qk(*(const LAS v4u*)(kp0 + t * KVT), *(const LAS v2u*)(kp1 + t * KVT), q0, q1);
#pragma unroll
      for (int r = 0; r < 16; ++r) mx = __builtin_fmaxf(mx, s[r]);
    }
    M = __builtin_fmaxf(mx, __shfl_xor(mx, 32));
  }
  {
    const float nm = -M;
    const unsigned m1 = bf_trunc(nm); const float r1 = nm - bflo(m1);
    const unsigned m2 = bf_trunc(r1); const float r2 = r1 - bflo(m2);
    const unsigned m3 = bf_trunc(r2);
    q1[0] = hi ? 0u : q16;
    q1[1] = hi ? m3 : (m1 | (m2 << 16));
  }
  f32x16 o = {};
  f32x16 s = qk(*(const LAS v4u*)(kp0), *(const LAS v2u*)(kp1), q0, q1);
#pragma unroll 2
  for (int t = 0; t < 64; ++t) {
    const int tn = t + 1 < 64 ? t + 1 : t;
    const v4u v0 = *(const LAS v4u*)(vp + t * KVT), v1 = *(const LAS v4u*)(vp + t * KVT + 608);
    const f32x16 sn = qk(*(const LAS v4u*)(kp0 + tn * KVT), *(const LAS v2u*)(kp1 + tn * KVT), q0, q1);
    f32x8 p0, p1;
#pragma unroll
    for (int r = 0; r < 8; ++r) { p0[r] = __builtin_amdgcn_exp2f(s[r]); p1[r] = __builtin_amdgcn_exp2f(s[8 + r]); }
    o = pv(v0, p0, o);
    o = pv(v1, p1, o);
    s = sn;
  }
  const float l = __shfl(o[10], r32);
  const float rl = 1.f / l;
  unsigned char* op = ws + WS_O + ((size_t)(token >> 4) * 72 + 9 * j) * 256 + (token & 15) * 16;
  *(v4u*)(op + (2 * hl + hi) * 256) = v4u{pk2(o[0] * rl, o[1] * rl), pk2(o[2] * rl, o[3] * rl), pk2(o[4] * rl, o[5] * rl), pk2(o[6] * rl, o[7] * rl)};
  if (!hi) *(unsigned*)(op + 8 * 256 + hl * 4) = pk2(o[8] * rl, o[9] * rl);
}

__global__ void __launch_bounds__(256, 1) k_wo(const u16* x, unsigned char* ws) {
  extern __shared__ __attribute__((aligned(16))) unsigned char smem[];
  LAS unsigned char* L = (LAS unsigned char*)smem;
  const int tid = threadIdx.x, lane = tid & 63, w = __builtin_amdgcn_readfirstlane(tid >> 6);
  const int b = blockIdx.x, G = b >> 3, j = b & 7;
  const int tt0 = 4 * G + 2 * (j & 1), cq = j >> 1;
  const int tok = lane & 15, g = lane >> 4;
  const unsigned lds0 = (unsigned)(uintptr_t)L;
  {
    const unsigned char* src = ws + WS_O + (size_t)tt0 * 72 * 256 + w * 1024 + lane * 16;
#pragma unroll
    for (int i = 0; i < 9; ++i) glds16(src + i * 4096, lds0 + w * 1024 + i * 4096);
  }
  const int nt = w == 0 ? 3 : 2, u0 = 9 * cq + (w == 0 ? 0 : 2 * w + 1);
  v4u a[3][KS];
  v2u xr[3][2];
  const v4u* ap = (const v4u*)(ws + WS_WO + (size_t)u0 * KS * 1024) + lane;
#pragma unroll
  for (int t = 0; t < 3; ++t)
    if (t < nt) {
#pragma unroll
      for (int s = 0; s < KS; ++s) a[t][s] = ap[(t * KS + s) * 64];
      const int U = u0 + t;
#pragma unroll
      for (int e = 0; e < 2; ++e) xr[t][e] = *(const v2u*)(x + (size_t)(16 * (tt0 + e) + tok) * D + 32 * (U >> 1) + 8 * g + 4 * (U & 1));
    }
  asm volatile("s_waitcnt vmcnt(0)\n\ts_barrier" ::: "memory");
  f32x4 acc[3][2];
#pragma unroll
  for (int t = 0; t < 3; ++t) { acc[t][0] = f32x4{0.f, 0.f, 0.f, 0.f}; acc[t][1] = acc[t][0]; }
#pragma unroll
  for (int s = 0; s < KS; ++s) {
    const v4u b0 = *(const LAS v4u*)(L + s * 1024 + lane * 16), b1 = *(const LAS v4u*)(L + (KS + s) * 1024 + lane * 16);
#pragma unroll
    for (int t = 0; t < 3; ++t)
      if (t < nt) { acc[t][0] = mfma16(a[t][s], b0, acc[t][0]); acc[t][1] = mfma16(a[t][s], b1, acc[t][1]); }
  }
#pragma unroll
  for (int t = 0; t < 3; ++t)
    if (t < nt) {
      const int U = u0 + t;
#pragma unroll
      for (int e = 0; e < 2; ++e) {
        const v2u hv = v2u{pk2(bflo(xr[t][e][0]) + acc[t][e][0], bfhi(xr[t][e][0]) + acc[t][e][1]),
                           pk2(bflo(xr[t][e][1]) + acc[t][e][2], bfhi(xr[t][e][1]) + acc[t][e][3])};
        *(v2u*)(ws + WS_H + ((size_t)(tt0 + e) * KS + (U >> 1)) * 1024 + lane * 16 + (U & 1) * 8) = hv;
      }
    }
}

constexpr int FFN_LDS = 2 * 73728 + 576 * 4;
constexpr int WIN = 18;
__global__ void __launch_bounds__(256, 1) k_ffn(const u16* b1, unsigned char* ws) {
  extern __shared__ __attribute__((aligned(16))) unsigned char smem[];
  LAS unsigned char* L = (LAS unsigned char*)smem;
  const int tid = threadIdx.x, lane = tid & 63, w = __builtin_amdgcn_readfirstlane(tid >> 6);
  const int b = blockIdx.x, G = b >> 3, j = b & 7;
  const int g = lane >> 4;
  v4u bf[4][KS];
  {
    const v4u* hp = (const v4u*)(ws + WS_H + (size_t)(4 * G + w) * KS * 1024) + lane;
#pragma unroll
    for (int s = 0; s < KS; ++s) bf[0][s] = hp[s * 64];
  }
  const v4u* ap = (const v4u*)(ws + WS_W12 + (size_t)(j * 4 + w) * 2 * 9 * KS * 1024) + lane;
  v4u a[WIN];
  for (int i = tid; i < D; i += 256) ((LAS float*)(L + 2 * 73728))[i] = bflo(b1[576 * j + i]);
#pragma unroll
  for (int i = 0; i < WIN; ++i) a[i] = ap[i * 64];
  SBAR();
  ln_frags(bf[0]);
#pragma unroll
  for (int s = 0; s < KS; ++s) *(LAS v4u*)(L + (w * KS + s) * 1024 + lane * 16) = bf[0][s];
  __syncthreads();
#pragma unroll
  for (int s = 0; s < KS; ++s)
#pragma unroll
    for (int tt = 0; tt < 4; ++tt) bf[tt][s] = *(const LAS v4u*)(L + (tt * KS + s) * 1024 + lane * 16);
  f32x4 acc[2][4];
  LAS unsigned char* h1 = L + 73728 + lane * 16;
#pragma unroll
  for (int t = 0; t < 10; ++t) {
    if (t < 9) {
      const int T = 9 * w + t;
      const f32x4 bias = *(const LAS f32x4*)(L + 2 * 73728 + (32 * (T >> 1) + 8 * g + 4 * (T & 1)) * 4);
#pragma unroll
      for (int tt = 0; tt < 4; ++tt) acc[t & 1][tt] = bias;
    }
#pragma unroll
    for (int s = 0; s < KS; ++s) {
      if (t < 9) {
        const int i = t * KS + s;
        const v4u cur = a[i % WIN];
        a[i % WIN] = ap[(i + WIN) * 64];
#pragma unroll
        for (int tt = 0; tt < 4; ++tt) acc[t & 1][tt] = mfma16(cur, bf[tt][s], acc[t & 1][tt]);
      }
      if (t > 0 && s >= 1 && s <= 4) {
        const int T = 9 * w + t - 1, tt = s - 1;
        f32x4 v = acc[(t - 1) & 1][tt];
#pragma unroll
        for (int e = 0; e < 4; ++e) { const float r = __builtin_fmaxf(v[e], 0.f); v[e] = r * r; }
        *(LAS v2u*)(h1 + (tt * KS + (T >> 1)) * 1024 + (T & 1) * 8) = v2u{pk2(v[0], v[1]), pk2(v[2], v[3])};
      }
      SBAR();
    }
  }
  __syncthreads();
#pragma unroll
  for (int s = 0; s < KS; ++s)
#pragma unroll
    for (int tt = 0; tt < 4; ++tt) bf[tt][s] = *(const LAS v4u*)(h1 + (tt * KS + s) * 1024);
  v4u* pp = (v4u*)(ws + WS_P + ((size_t)(j * 32 + G) * 36 + 9 * w) * 2048) + lane;
#pragma unroll
  for (int t = 0; t < 10; ++t) {
#pragma unroll
    for (int s = 0; s < KS; ++s) {
      if (t < 9) {
        const int i = (9 + t) * KS + s;
        const v4u cur = a[i % WIN];
        if (i + WIN < 18 * KS) a[i % WIN] = ap[(i + WIN) * 64];
#pragma unroll
        for (int tt = 0; tt < 4; ++tt) acc[t & 1][tt] = mfma16(cur, bf[tt][s], s == 0 ? f32x4{0.f, 0.f, 0.f, 0.f} : acc[t & 1][tt]);
      }
      if (t > 0 && (s == 1 || s == 2)) {
        const int pr = s - 1;
        const f32x4 u = acc[(t - 1) & 1][2 * pr], v = acc[(t - 1) & 1][2 * pr + 1];
        pp[((t - 1) * 2 + pr) * 64] = v4u{pk2(u[0], u[1]), pk2(u[2], u[3]), pk2(v[0], v[1]), pk2(v[2], v[3])};
      }
      SBAR();
    }
  }
}

__global__ void __launch_bounds__(256) k_red(const u16* b2, const unsigned char* ws, u16* out) {
  const int gid = blockIdx.x * 256 + threadIdx.x, lane = gid & 63, wv = gid >> 6;
  const int pr = wv & 1, gs = wv >> 1, G = gs / KS, s = gs - KS * G;
  const int tok = lane & 15, g = lane >> 4;
  v4u p[8][2];
#pragma unroll
  for (int j = 0; j < 8; ++j)
#pragma unroll
    for (int e = 0; e < 2; ++e)
      p[j][e] = *(const v4u*)(ws + WS_P + (((size_t)(j * 32 + G) * 36 + 2 * s + e) * 2 + pr) * 1024 + lane * 16);
  v4u hv[2];
#pragma unroll
  for (int e = 0; e < 2; ++e) hv[e] = *(const v4u*)(ws + WS_H + ((size_t)(4 * G + 2 * pr + e) * KS + s) * 1024 + lane * 16);
  const v4u bv = *(const v4u*)(b2 + 32 * s + 8 * g);
#pragma unroll
  for (int e = 0; e < 2; ++e) {
    float r[8];
#pragma unroll
    for (int m = 0; m < 4; ++m) { r[2 * m] = bflo(bv[m]); r[2 * m + 1] = bfhi(bv[m]); }
#pragma unroll
    for (int j = 0; j < 8; ++j)
#pragma unroll
      for (int u = 0; u < 2; ++u)
#pragma unroll
        for (int m = 0; m < 2; ++m) { r[4 * u + 2 * m] += bflo(p[j][u][2 * e + m]); r[4 * u + 2 * m + 1] += bfhi(p[j][u][2 * e + m]); }
    v4u o;
#pragma unroll
    for (int m = 0; m < 4; ++m) o[m] = pk2(r[2 * m] + bflo(hv[e][m]), r[2 * m + 1] + bfhi(hv[e][m]));
    *(v4u*)(out + (size_t)(64 * G + 16 * (2 * pr + e) + tok) * D + 32 * s + 8 * g) = o;
  }
}

extern "C" void kernel_launch(void* const* d_in, const int* in_sizes, int n_in, void* d_out, int out_size, void* d_ws,
                              size_t ws_size, hipStream_t stream) {
  const u16* x = (const u16*)d_in[0];
  const u16* wq = (const u16*)d_in[1];
  const u16* wk = (const u16*)d_in[2];
  const u16* wv = (const u16*)d_in[3];
  const u16* wo = (const u16*)d_in[4];
  const u16* w1 = (const u16*)d_in[6];
  const u16* b1 = (const u16*)d_in[7];
  const u16* w2 = (const u16*)d_in[8];
  const u16* b2 = (const u16*)d_in[9];
  unsigned char* ws = (unsigned char*)d_ws;
  (void)hipFuncSetAttribute((const void*)k_qkv, hipFuncAttributeMaxDynamicSharedMemorySize, K1_LDS);
  (void)hipFuncSetAttribute((const void*)k_attn, hipFuncAttributeMaxDynamicSharedMemorySize, 163840);
  (void)hipFuncSetAttribute((const void*)k_ffn, hipFuncAttributeMaxDynamicSharedMemorySize, FFN_LDS);
  k_pretile<<<(NJOB + 3) / 4, 256, 0, stream>>>(w1, w2, wo, ws);
  k_qkv<<<256, 256, K1_LDS, stream>>>(x, wq, wk, wv, ws);
  k_attn<<<256, 512, 163840, stream>>>(ws);
  k_wo<<<256, 256, 36864, stream>>>(x, ws);
  k_ffn<<<256, 256, FFN_LDS, stream>>>(b1, ws);
  k_red<<<288, 256, 0, stream>>>(b2, ws, (u16*)d_out);
}
```

```cpp
#include <hip/hip_runtime.h>
#include <hip/hip_bf16.h>
#include <stdint.h>

#define CODE_BYTES 38016
#define LAS __attribute__((address_space(3)))
typedef unsigned short u16;
typedef unsigned v32u __attribute__((ext_vector_type(32)));
typedef unsigned v16u __attribute__((ext_vector_type(16)));
typedef unsigned v8u __attribute__((ext_vector_type(8)));
typedef unsigned v4u __attribute__((ext_vector_type(4)));
typedef unsigned v2u __attribute__((ext_vector_type(2)));
typedef float f32x4 __attribute__((ext_vector_type(4)));
typedef float f32x8 __attribute__((ext_vector_type(8)));
typedef float f32x16 __attribute__((ext_vector_type(16)));
typedef __bf16 bf16x8 __attribute__((ext_vector_type(8)));
typedef __bf16 bf16x2 __attribute__((ext_vector_type(2)));
typedef short s16x4 __attribute__((ext_vector_type(4)));

constexpr int S = 2048, D = 576, DH = 18, F = 4608;
constexpr int KS = 18;
constexpr int KVT = 2560;
constexpr int KVH = 81920;
constexpr float QSCALE = 0.23570226039551584f * 1.4426950408889634f;
constexpr float MSAFE = 48.f;

constexpr int SHS = 11;
constexpr size_t WS_Q = 0;
constexpr size_t WS_KV = WS_Q + (size_t)S * D * 2;
constexpr size_t WS_KN = WS_KV + 2 * KVH;
constexpr size_t WS_CNT = WS_KN + 2048;
constexpr size_t WS_HN = WS_CNT + 2048;
constexpr size_t WS_WO = WS_HN + (size_t)S * D * 2;
constexpr size_t WS_W12 = WS_WO + (size_t)D * D * 2;
constexpr size_t WS_P = WS_W12 + (size_t)2 * D * F * 2;
constexpr size_t WS_P8 = WS_P + (size_t)32 * 8 * 4 * 8 * 2048;

__device__ __forceinline__ void ffn_map(int b, int& G, int& j) {
  G = (b & 3) * 8 + (b >> 5); j = (b & 4) + ((b >> 3) & 3);
}
__device__ __forceinline__ float bflo(unsigned u) { return __builtin_bit_cast(float, u << 16); }
__device__ __forceinline__ unsigned pk2(float lo, float hi) {
  bf16x2 r; r[0] = (__bf16)lo; r[1] = (__bf16)hi; return __builtin_bit_cast(unsigned, r);
}
__device__ __forceinline__ void glds16(const void* g, unsigned lds_base) {
  unsigned sv;
  asm volatile("s_mov_b32 %0, m0\n\ts_mov_b32 m0, %2\n\ts_nop 0\n\tglobal_load_lds_dwordx4 %1, off\n\ts_mov_b32 m0, %0"
               : "=&s"(sv) : "v"(g), "s"(lds_base) : "memory");
}
__device__ __forceinline__ void glds16x4(const void* sbase, unsigned voff, unsigned lds_base) {
  unsigned sv;
  asm volatile("s_mov_b32 %0, m0\n\ts_mov_b32 m0, %3\n\ts_nop 0\n\tglobal_load_lds_dwordx4 %1, %2\n\tglobal_load_lds_dwordx4 %1, %2 offset:1024\n\t"
               "global_load_lds_dwordx4 %1, %2 offset:2048\n\tglobal_load_lds_dwordx4 %1, %2 offset:3072\n\ts_mov_b32 m0, %0"
               : "=&s"(sv) : "v"(voff), "s"(sbase), "s"(lds_base) : "memory");
}
#define SBAR() __builtin_amdgcn_sched_barrier(0)
__device__ __forceinline__ unsigned fb(float v) { return __builtin_bit_cast(unsigned, v); }
__device__ __forceinline__ float bf(unsigned v) { return __builtin_bit_cast(float, v); }
__device__ __forceinline__ float max16(float v) { auto r = __builtin_amdgcn_permlane16_swap(fb(v), fb(v), false, false); return __builtin_fmaxf(bf(r[0]), bf(r[1])); }
__device__ __forceinline__ float max32(float v) { auto r = __builtin_amdgcn_permlane32_swap(fb(v), fb(v), false, false); return __builtin_fmaxf(bf(r[0]), bf(r[1])); }
template <int N> __device__ __forceinline__ float ror(float v) {
  return __builtin_bit_cast(float, __builtin_amdgcn_mov_dpp(__builtin_bit_cast(int, v), 0x120 + N, 0xf, 0xf, true));
}
__device__ __forceinline__ float rowmax16(float v) {
  v = __builtin_fmaxf(v, ror<8>(v)); v = __builtin_fmaxf(v, ror<4>(v)); v = __builtin_fmaxf(v, ror<2>(v)); return __builtin_fmaxf(v, ror<1>(v));
}

__device__ __forceinline__ float dot2(unsigned a, float c) {
  return __builtin_amdgcn_fdot2_f32_bf16(__builtin_bit_cast(bf16x2, a), __builtin_bit_cast(bf16x2, a), c, false);
}
__device__ __forceinline__ unsigned rfl(unsigned v) { return __builtin_amdgcn_readfirstlane(v); }
__device__ __forceinline__ unsigned plo(const void* p) { return (unsigned)(uintptr_t)p; }
__device__ __forceinline__ unsigned phi(const void* p) { return (unsigned)((uintptr_t)p >> 32); }
__device__ __forceinline__ void fill_hn(unsigned char* ws, int b, unsigned lane) {
  const __amdgpu_buffer_rsrc_t rs = __builtin_amdgcn_make_buffer_rsrc((void*)(ws + WS_HN + (size_t)b * 9216), 0, 9216, 0x00020000);
#pragma unroll
  for (int i = 0; i < 9; ++i) __builtin_amdgcn_raw_buffer_store_b128(v4u{~0u, ~0u, ~0u, ~0u}, rs, lane * 16, i * 1024, 16);
}
__device__ __forceinline__ void pretile_wo_load(int q, int lane, const u16* wo, v2u (&r)[8]) {
  const int cs = lane & 15, g = lane >> 4;
  const int s = q / 9, blk = q - 9 * s, chunk = 4 * s + g;
  const bool lo = chunk < 64;
  const unsigned row0 = lo ? (chunk >> 1) * DH + 4 * (chunk & 1) : 4 * (chunk - 64) * DH + 16, s1 = lo ? 2 : DH, s2 = lo ? 8 : 2 * DH;
  const unsigned char* src = (const unsigned char*)(wo + blk * 64);
#pragma unroll
  for (int jj = 0; jj < 8; ++jj) r[jj] = *(const v2u*)(src + ((row0 + (jj & 1) + ((jj >> 1) & 1) * s1 + (jj >> 2) * s2) * (D * 2) + 8 * cs));
}
__device__ __forceinline__ void pretile_wo_store(int q, int lane, const v2u (&r)[8], unsigned char* ws) {
  const int cs = lane & 15, g = lane >> 4;
  const int p = cs >> 2, f = q;
  const int frag = p * 162 + f + (f < 40 ? 0 : f < 55 ? 47 : f < 102 ? -15 : f < 127 ? 35 : -25);
  unsigned char* dst = ws + WS_WO + (size_t)frag * 1024 + (g * 16 + 4 * (cs & 3)) * 16;
#pragma unroll
  for (int i = 0; i < 4; ++i) {
    v4u o;
#pragma unroll
    for (int m = 0; m < 4; ++m)
      o[m] = __builtin_amdgcn_perm(r[2 * m + 1][i >> 1], r[2 * m][i >> 1], (i & 1) ? 0x07060302u : 0x05040100u);
    *(v4u*)(dst + i * 16) = o;
  }
}

constexpr int K1_LDS = 8 * 20480, K1_EX = 2 * 20480;
__global__ void __launch_bounds__(512, 2) k_qkv(const u16* x, const u16* wq, const u16* wk, const u16* wv, const u16* wo, unsigned char* ws,
                                                   const u16* w1, const u16* w2, const u16* b1, const u16* b2, const u16* out) {
  extern __shared__ __attribute__((aligned(16))) unsigned char smem[];
  asm volatile(
    "s_load_dwordx8 s[44:51], s[40:41], 0x0\n"
    "s_load_dwordx2 s[64:65], s[40:41], 0x28\n"
    "v_readfirstlane_b32 s52, v0\n"
    "s_and_b32 s53, s42, 7\n"
    "s_mul_i32 s53, s53, 28\n"
    "s_lshr_b32 s54, s42, 3\n"
    "s_add_u32 s53, s53, s54\n"
    "s_cmp_lt_u32 s52, 0x100\n"
    "s_cbranch_scc1 9f\n"
    "s_cmp_ge_u32 s54, 28\n"
    "s_cbranch_scc1 9f\n"
    "s_cmp_ge_u32 s53, 220\n"
    "s_cbranch_scc1 9f\n"
    "s_bfe_u32 s52, s52, 0x20006\n"
    "s_mul_i32 s54, s53, 205\n"
    "s_lshr_b32 s54, s54, 11\n"
    "s_mul_i32 s55, s54, 10\n"
    "s_sub_u32 s55, s53, s55\n"
    "s_mul_i32 s56, s54, 3\n"
    "s_add_u32 s56, s56, s55\n"
    "s_mul_i32 s57, s56, 57\n"
    "s_lshr_b32 s57, s57, 9\n"
    "s_mul_i32 s57, s57, 9\n"
    "s_sub_u32 s56, s56, s57\n"
    "s_and_b32 s57, s52, 1\n"
    "s_lshl_b32 s57, s57, 2\n"
    "v_bfe_u32 v1, v0, 3, 3\n"
    "v_and_b32 v2, 7, v0\n"
    "v_xor_b32 v2, s57, v2\n"
    "v_bfe_u32 v3, v0, 4, 2\n"
    "v_xor_b32 v3, v2, v3\n"
    "v_lshlrev_b32 v3, 4, v3\n"
    "v_and_b32 v4, 2, v1\n"
    "v_xor_b32 v4, v2, v4\n"
    "s_lshl_b32 s57, s52, 3\n"
    "v_add_u32 v1, s57, v1\n"
    "s_movk_i32 s58, 0x480\n"
    "s_mul_i32 s57, s54, 96\n"
    "v_add_u32 v5, s57, v1\n"
    "v_min_u32 v6, 0x7ff, v5\n"
    "v_mad_u32_u24 v6, v6, s58, v3\n"
    "v_add_u32 v7, 32, v5\n"
    "v_min_u32 v7, 0x7ff, v7\n"
    "v_mad_u32_u24 v7, v7, s58, v3\n"
    "v_add_u32 v8, 64, v5\n"
    "v_min_u32 v8, 0x7ff, v8\n"
    "v_mad_u32_u24 v8, v8, s58, v3\n"
    "v_and_b32 v2, 1, v4\n"
    "v_lshlrev_b32 v3, 4, v2\n"
    "v_cmp_le_u32 vcc, 4, v4\n"
    "v_bfe_u32 v5, v4, 1, 1\n"
    "s_lshl_b32 s57, s55, 7\n"
    "v_cndmask_b32 v3, v3, 20, vcc\n"
    "v_cndmask_b32 v5, v5, v2, vcc\n"
    "v_lshl_add_u32 v4, v4, 4, s57\n"
    "v_cmp_eq_u32 s[60:61], 1, v5\n"
    "s_cmp_eq_u32 s55, 9\n"
    "s_cselect_b64 vcc, -1, 0\n"
    "s_cselect_b32 s58, 36, s58\n"
    "v_cndmask_b32 v4, v4, v3, vcc\n"
    "v_mad_u32_u24 v4, v1, s58, v4\n"
    "s_waitcnt lgkmcnt(0)\n"
    "s_cselect_b32 s48, s48, s46\n"
    "s_cselect_b32 s49, s49, s47\n"
    "s_cselect_b32 s50, s50, s46\n"
    "s_cselect_b32 s51, s51, s47\n"
    "v_mov_b32 v10, s48\n"
    "v_mov_b32 v11, s49\n"
    "v_mov_b32 v12, s50\n"
    "v_mov_b32 v13, s51\n"
    "v_cndmask_b32 v10, v10, v12, s[60:61]\n"
    "v_cndmask_b32 v11, v11, v13, s[60:61]\n"
    "v_add_co_u32 v10, vcc, v10, v4\n"
    "s_lshl_b32 s57, s52, 10\n"
    "s_add_u32 s63, s43, s57\n"
    "v_addc_co_u32 v11, vcc, 0, v11, vcc\n"
    "s_lshl_b32 s58, s58, 5\n"
    "s_mov_b32 s43, s63\n"
    "s_mov_b32 s53, 0\n"
    "v_mov_b32 v9, s58\n"
    "s_lshl_b32 s56, s56, 1\n"
    "s_bitcmp1_b32 s42, 3\n"
    "s_cbranch_scc0 6f\n"
    "global_load_dword v14, v[10:11], off\n"
    "6:\n"
    "1:\n"
    "s_lshl_b32 s57, s56, 6\n"
    "s_add_u32 s48, s44, s57\n"
    "s_addc_u32 s49, s45, 0\n"
    "s_mov_b32 m0, s43\n"
    "v_mad_u64_u32 v[12:13], vcc, s56, v9, v[10:11]\n"
    "global_load_lds_dwordx4 v6, s[48:49]\n"
    "s_add_u32 m0, s43, 12288\n"
    "s_add_u32 s56, s56, 1\n"
    "global_load_lds_dwordx4 v[12:13], off\n"
    "s_add_u32 m0, s43, 4096\n"
    "v_mad_u64_u32 v[12:13], vcc, s56, v9, v[10:11]\n"
    "global_load_lds_dwordx4 v7, s[48:49]\n"
    "s_add_u32 m0, s43, 16384\n"
    "s_add_u32 s56, s56, 1\n"
    "global_load_lds_dwordx4 v[12:13], off\n"
    "s_add_u32 m0, s43, 8192\n"
    "s_cmp_eq_u32 s56, 18\n"
    "s_cselect_b32 s56, 0, s56\n"
    "global_load_lds_dwordx4 v8, s[48:49]\n"
    "s_add_u32 s43, s43, 20480\n"
    "s_add_u32 s53, s53, 1\n"
    "s_cmp_eq_u32 s53, 8\n"
    "s_cselect_b32 s43, s63, s43\n"
    "s_cmp_le_u32 s53, 2\n"
    "s_cbranch_scc1 1b\n"
    "s_waitcnt vmcnt(10)\n"
    "s_barrier\n"
    "s_cmp_lt_u32 s53, 9\n"
    "s_cbranch_scc1 1b\n"
    "s_mul_i32 s57, s42, 9216\n"
    "s_lshl_b32 s58, s52, 11\n"
    "s_add_u32 s57, s57, s58\n"
    "s_add_u32 s57, s57, 2527232\n"
    "v_and_b32 v1, 63, v0\n"
    "v_lshl_add_u32 v1, v1, 4, s57\n"
    "v_mov_b32 v16, -1\n"
    "v_mov_b32 v17, -1\n"
    "v_mov_b32 v18, -1\n"
    "v_mov_b32 v19, -1\n"
    "global_store_dwordx4 v1, v[16:19], s[64:65] offset:0 sc1\n"
    "global_store_dwordx4 v1, v[16:19], s[64:65] offset:1024 sc1\n"
    "global_store_dwordx4 v1, v[16:19], s[64:65] offset:2048 sc1\n"
    "s_waitcnt vmcnt(8)\n"
    "s_barrier\n"
    "s_waitcnt vmcnt(3)\n"
    "s_barrier\n"
    "s_barrier\n"
    "s_barrier\n"
    "s_barrier\n"
    "s_endpgm\n"
    "9:\n"
    "s_waitcnt lgkmcnt(0)\n"
    :: "{s[40:41]}"(__builtin_amdgcn_kernarg_segment_ptr()), "{s42}"(blockIdx.x), "{s43}"((unsigned)(uintptr_t)(LAS unsigned char*)smem), "{v0}"(threadIdx.x)
    : "s44", "s45", "s46", "s47", "s48", "s49", "s50", "s51", "s52", "s53", "s54", "s55", "s56", "s57", "s58", "s59", "s60", "s61", "s62", "s63", "s64", "s65", "s74", "s75", "s76", "s77", "s79",
      "v1", "v2", "v3", "v4", "v5", "v6", "v7", "v8", "v9", "v10", "v11", "v12", "v13", "v14", "v15", "v16", "v17", "v18", "v19", "v20", "v21", "v22", "v23", "v59", "vcc", "scc", "memory");
  const unsigned lane = threadIdx.x & 63;
  const int w = __builtin_amdgcn_readfirstlane(threadIdx.x >> 6);
  const int b = blockIdx.x, item = (b & 7) * 28 + (b >> 3);
  const int grp = item / 10, blk = item - 10 * grp;
  const unsigned lds0 = (unsigned)(uintptr_t)(LAS unsigned char*)smem;
  if (item >= 220 && b < 224) { if (w == 0) fill_hn(ws, b, lane); return; }
  if (b >= 224) {
    const int sb = b - 224, q = sb + 32 * w;
    v2u r[8];
    if (w == 7) {
      unsigned junk;
      asm volatile("s_getpc_b64 s[40:41]\n\ts_add_u32 s40, s40, _Z5k_qkvPKtS0_S0_S0_S0_PhS0_S0_S0_S0_S0_@rel32@lo+4\n\ts_addc_u32 s41, s41, _Z5k_qkvPKtS0_S0_S0_S0_PhS0_S0_S0_S0_S0_@rel32@hi+12\n\t"
                   "global_load_dword %0, %1, s[40:41]" : "={v121}"(junk) : "v"(((sb >> 3) * 14 + min(lane, 13u)) * 128) : "s40", "s41", "scc", "memory");
    }
    if (w == 6) {
      const unsigned l = lane * 4 + (sb >> 3);
      const unsigned char* p = l < 144 ? ws + WS_W12 + (min(l, 112u) << 18) : l < 165 ? (const unsigned char*)w1 + ((l - 144) << 18) : l < 186 ? (const unsigned char*)w2 + ((l - 165) << 18)
                             : l < 195 ? (const unsigned char*)out + ((l - 186) << 18) : (const unsigned char*)((l & 1) ? b1 : b2);
      unsigned junk;
      asm volatile("global_load_dword %0, %1, off" : "={v121}"(junk) : "v"(p) : "memory");
    }
    if (q < 162) pretile_wo_load(q, lane, wo, r);
    if (w >= 6) {
      unsigned junk;
      asm volatile("s_getpc_b64 s[40:41]\n\ts_add_u32 s40, s40, _Z6k_mainPKtS0_S0_S0_S0_PhPt@rel32@lo+4\n\ts_addc_u32 s41, s41, _Z6k_mainPKtS0_S0_S0_S0_PhPt@rel32@hi+12\n\t"
                   "global_load_dword %0, %1, s[40:41]" : "={v120}"(junk) : "v"(min((((sb >> 3) * 2 + (w - 6)) * 40 + min(lane, 39u)) * 128, CODE_BYTES - 128u)) : "s40", "s41", "scc", "memory");
    }
    if (w == 7) fill_hn(ws, b, lane);
    if (b == 255 && w == 4 && lane < 8) *(unsigned*)(ws + WS_CNT + lane * 256) = 0u;
    if (q < 162) pretile_wo_store(q, lane, r, ws);
    return;
  }
  const unsigned tok = lane & 15, g = lane >> 4, q4 = tok >> 2;
  const unsigned xa = lds0 + tok * 128;
  v4u ad = {lds0 + 12288 + (8 * g + q4) * 128 + ((w ^ ((q4 >> 1) | ((g & 1) << 1))) * 32) + (lane & 3) * 8,
            xa + ((g ^ (tok >> 1)) * 16), xa + (((4 + g) ^ (tok >> 1)) * 16), lds0 + K1_EX + tok * 4 + (g < 2 ? w : w < 2 ? 6 : w + 2) * 64};
  const bool kv = blk == 9, vt = kv && w == 1;
  const unsigned stg = lds0 + K1_EX + 2048;
  unsigned ooff = stg + (!kv ? tok * 144 + 32 * w + 8 * g
                             : w == 0 ? ((g >> 1) * 32 + tok) * 16 + (g & 1) * 8 : w == 1 ? 1280 + (g >> 1) * 320 + tok * 16 + (g & 1) * 8
                             : w == 2 ? 1024 + tok * 8 : 1280 + (tok >> 3) * 320 + 256 + (tok & 7) * 2);
  if (kv && w >= 2 && g != 2 * w - 3) ooff += 8192 + g * 8192;
  unsigned ooff1 = ooff + (!kv ? 16 * 144 : w == 0 ? 256 : 640);
  const unsigned os2 = kv ? KVT : 32 * 144;
  unsigned exr = lds0 + K1_EX + (vt ? g * 16 : tok * 4);
  unsigned cpr = stg + (kv ? w * 1024 + lane * 16 : (8 * w + (lane >> 3)) * 144 + (lane & 7) * 16);
  unsigned cpw = kv ? 3 * grp * KVT + w * 1024 + lane * 16 : (96 * grp + 8 * w + (lane >> 3)) * (D * 2) + 128 * blk + (lane & 7) * 16;
  asm volatile("" : "+v"(ooff), "+v"(ooff1), "+v"(exr), "+v"(cpr), "+v"(cpw));
  const __amdgpu_buffer_rsrc_t rq = __builtin_amdgcn_make_buffer_rsrc((void*)(ws + (kv ? WS_KV : WS_Q)), 0, kv ? min(3 * grp + 3, 64) * KVT : S * D * 2, 0x00020000);
  f32x16 acc; f32x8 acc2; f32x4 cs;
  asm volatile(
    "v_mov_b32 v8, 0x3f803f80\n"
    "v_mov_b32 v9, 0x3f803f80\n"
    "v_mov_b32 v10, 0x3f803f80\n"
    "v_mov_b32 v11, 0x3f803f80\n"
    "v_mov_b32 v12, 0\n"
    "v_mov_b32 v13, 0\n"
    "v_mov_b32 v14, 0\n"
    "v_mov_b32 v15, 0\n"
    "v_mov_b32 v16, 0\n"
    "v_mov_b32 v17, 0\n"
    "v_mov_b32 v18, 0\n"
    "v_mov_b32 v19, 0\n"
    "v_mov_b32 v20, 0\n"
    "v_mov_b32 v21, 0\n"
    "v_mov_b32 v22, 0\n"
    "v_mov_b32 v23, 0\n"
    "v_mov_b32 v24, 0\n"
    "v_mov_b32 v25, 0\n"
    "v_mov_b32 v26, 0\n"
    "v_mov_b32 v27, 0\n"
    "v_mov_b32 v28, 0\n"
    "v_mov_b32 v29, 0\n"
    "v_mov_b32 v30, 0\n"
    "v_mov_b32 v31, 0\n"
    "v_mov_b32 v32, 0\n"
    "v_mov_b32 v33, 0\n"
    "v_mov_b32 v34, 0\n"
    "v_mov_b32 v35, 0\n"
    "v_mov_b32 v36, 0\n"
    "v_mov_b32 v37, 0\n"
    "v_mov_b32 v38, 0\n"
    "v_mov_b32 v39, 0\n"
    "v_mov_b32 v40, 0\n"
    "v_mov_b32 v41, 0\n"
    "v_mov_b32 v42, 0\n"
    "v_mov_b32 v43, 0\n"
    "v_mov_b32 v44, 0\n"
    "v_mov_b32 v45, 0\n"
    "v_mov_b32 v46, 0\n"
    "v_mov_b32 v47, 0\n"
    "v_mov_b32 v48, 0\n"
    "v_mov_b32 v49, 0\n"
    "v_mov_b32 v50, 0\n"
    "v_mov_b32 v51, 0\n"
    "v_mov_b32 v52, 0\n"
    "v_mov_b32 v53, 0\n"
    "v_mov_b32 v54, 0\n"
    "v_mov_b32 v55, 0\n"
    "s_mov_b32 s41, 0\n"
    "s_cmp_eq_u32 s40, 1\n"
    "s_cbranch_scc1 31f\n"
    "s_cmp_eq_u32 s40, 2\n"
    "s_cbranch_scc1 32f\n"
    "s_cmp_eq_u32 s40, 3\n"
    "s_cbranch_scc1 33f\n"
    "s_cmp_eq_u32 s40, 4\n"
    "s_cbranch_scc1 34f\n"
    "30:\n"
    "s_barrier\n"
    "ds_read_b64_tr_b16 v[80:81], v2 offset:0\n"
    "ds_read_b64_tr_b16 v[82:83], v2 offset:512\n"
    "ds_read_b128 v[56:59], v3 offset:0\n"
    "ds_read_b128 v[60:63], v3 offset:2048\n"
    "ds_read_b128 v[64:67], v3 offset:4096\n"
    "ds_read_b128 v[68:71], v3 offset:6144\n"
    "ds_read_b128 v[72:75], v3 offset:8192\n"
    "ds_read_b128 v[76:79], v3 offset:10240\n"
    "10:\n"
    "s_waitcnt lgkmcnt(0)\n"
    "v_mfma_f32_16x16x32_bf16 v[36:39], v[80:83], v[8:11], v[36:39]\n"
    "ds_read_b64_tr_b16 v[108:109], v2 offset:4096\n"
    "ds_read_b64_tr_b16 v[110:111], v2 offset:4608\n"
    "v_mfma_f32_16x16x32_bf16 v[12:15], v[80:83], v[56:59], v[12:15]\n"
    "ds_read_b128 v[84:87], v4 offset:0\n"
    "ds_read_b128 v[88:91], v4 offset:2048\n"
    "v_mfma_f32_16x16x32_bf16 v[16:19], v[80:83], v[60:63], v[16:19]\n"
    "ds_read_b128 v[92:95], v4 offset:4096\n"
    "ds_read_b128 v[96:99], v4 offset:6144\n"
    "v_mfma_f32_16x16x32_bf16 v[20:23], v[80:83], v[64:67], v[20:23]\n"
    "ds_read_b128 v[100:103], v4 offset:8192\n"
    "ds_read_b128 v[104:107], v4 offset:10240\n"
    "v_mfma_f32_16x16x32_bf16 v[24:27], v[80:83], v[68:71], v[24:27]\n"
    "s_add_u32 s41, s41, 1\n"
    "s_cmp_eq_u32 s41, 8\n"
    "s_cselect_b32 s42, -7, 1\n"
    "v_mfma_f32_16x16x32_bf16 v[28:31], v[80:83], v[72:75], v[28:31]\n"
    "s_mul_i32 s42, s42, 20480\n"
    "v_add_u32 v3, s42, v3\n"
    "v_mfma_f32_16x16x32_bf16 v[32:35], v[80:83], v[76:79], v[32:35]\n"
    "v_add_u32 v2, s42, v2\n"
    "v_add_u32 v4, s42, v4\n"
    "v_mfma_f32_16x16x32_bf16 v[40:43], v[8:11], v[56:59], v[40:43]\n"
    "v_mfma_f32_16x16x32_bf16 v[44:47], v[56:59], v[56:59], v[44:47]\n"
    "s_waitcnt lgkmcnt(0)\n"
    "s_barrier\n"
    "v_mfma_f32_16x16x32_bf16 v[36:39], v[108:111], v[8:11], v[36:39]\n"
    "ds_read_b64_tr_b16 v[80:81], v2 offset:0\n"
    "ds_read_b64_tr_b16 v[82:83], v2 offset:512\n"
    "v_mfma_f32_16x16x32_bf16 v[12:15], v[108:111], v[84:87], v[12:15]\n"
    "ds_read_b128 v[56:59], v3 offset:0\n"
    "ds_read_b128 v[60:63], v3 offset:2048\n"
    "v_mfma_f32_16x16x32_bf16 v[16:19], v[108:111], v[88:91], v[16:19]\n"
    "ds_read_b128 v[64:67], v3 offset:4096\n"
    "ds_read_b128 v[68:71], v3 offset:6144\n"
    "v_mfma_f32_16x16x32_bf16 v[20:23], v[108:111], v[92:95], v[20:23]\n"
    "ds_read_b128 v[72:75], v3 offset:8192\n"
    "ds_read_b128 v[76:79], v3 offset:10240\n"
    "v_mfma_f32_16x16x32_bf16 v[24:27], v[108:111], v[96:99], v[24:27]\n"
    "v_mfma_f32_16x16x32_bf16 v[28:31], v[108:111], v[100:103], v[28:31]\n"
    "v_mfma_f32_16x16x32_bf16 v[32:35], v[108:111], v[104:107], v[32:35]\n"
    "v_mfma_f32_16x16x32_bf16 v[40:43], v[8:11], v[84:87], v[40:43]\n"
    "v_mfma_f32_16x16x32_bf16 v[44:47], v[84:87], v[84:87], v[44:47]\n"
    "s_cmp_lt_u32 s41, 9\n"
    "s_cbranch_scc1 10b\n"
    "s_branch 20f\n"
    "31:\n"
    "s_barrier\n"
    "ds_read_b64_tr_b16 v[80:81], v2 offset:0\n"
    "ds_read_b64_tr_b16 v[82:83], v2 offset:512\n"
    "ds_read_b128 v[56:59], v3 offset:0\n"
    "ds_read_b128 v[60:63], v3 offset:2048\n"
    "ds_read_b128 v[64:67], v3 offset:4096\n"
    "ds_read_b128 v[68:71], v3 offset:6144\n"
    "ds_read_b128 v[72:75], v3 offset:8192\n"
    "ds_read_b128 v[76:79], v3 offset:10240\n"
    "11:\n"
    "s_waitcnt lgkmcnt(0)\n"
    "v_mfma_f32_16x16x32_bf16 v[36:39], v[80:83], v[8:11], v[36:39]\n"
    "ds_read_b64_tr_b16 v[108:109], v2 offset:4096\n"
    "ds_read_b64_tr_b16 v[110:111], v2 offset:4608\n"
    "v_mfma_f32_16x16x32_bf16 v[12:15], v[80:83], v[56:59], v[12:15]\n"
    "ds_read_b128 v[84:87], v4 offset:0\n"
    "ds_read_b128 v[88:91], v4 offset:2048\n"
    "v_mfma_f32_16x16x32_bf16 v[16:19], v[80:83], v[60:63], v[16:19]\n"
    "ds_read_b128 v[92:95], v4 offset:4096\n"
    "ds_read_b128 v[96:99], v4 offset:6144\n"
    "v_mfma_f32_16x16x32_bf16 v[20:23], v[80:83], v[64:67], v[20:23]\n"
    "ds_read_b128 v[100:103], v4 offset:8192\n"
    "ds_read_b128 v[104:107], v4 offset:10240\n"
    "v_mfma_f32_16x16x32_bf16 v[24:27], v[80:83], v[68:71], v[24:27]\n"
    "s_add_u32 s41, s41, 1\n"
    "s_cmp_eq_u32 s41, 8\n"
    "s_cselect_b32 s42, -7, 1\n"
    "v_mfma_f32_16x16x32_bf16 v[28:31], v[80:83], v[72:75], v[28:31]\n"
    "s_mul_i32 s42, s42, 20480\n"
    "v_add_u32 v3, s42, v3\n"
    "v_mfma_f32_16x16x32_bf16 v[32:35], v[80:83], v[76:79], v[32:35]\n"
    "v_add_u32 v2, s42, v2\n"
    "v_add_u32 v4, s42, v4\n"
    "v_mfma_f32_16x16x32_bf16 v[40:43], v[8:11], v[60:63], v[40:43]\n"
    "v_mfma_f32_16x16x32_bf16 v[44:47], v[60:63], v[60:63], v[44:47]\n"
    "s_waitcnt lgkmcnt(0)\n"
    "s_barrier\n"
    "v_mfma_f32_16x16x32_bf16 v[36:39], v[108:111], v[8:11], v[36:39]\n"
    "ds_read_b64_tr_b16 v[80:81], v2 offset:0\n"
    "ds_read_b64_tr_b16 v[82:83], v2 offset:512\n"
    "v_mfma_f32_16x16x32_bf16 v[12:15], v[108:111], v[84:87], v[12:15]\n"
    "ds_read_b128 v[56:59], v3 offset:0\n"
    "ds_read_b128 v[60:63], v3 offset:2048\n"
    "v_mfma_f32_16x16x32_bf16 v[16:19], v[108:111], v[88:91], v[16:19]\n"
    "ds_read_b128 v[64:67], v3 offset:4096\n"
    "ds_read_b128 v[68:71], v3 offset:6144\n"
    "v_mfma_f32_16x16x32_bf16 v[20:23], v[108:111], v[92:95], v[20:23]\n"
    "ds_read_b128 v[72:75], v3 offset:8192\n"
    "ds_read_b128 v[76:79], v3 offset:10240\n"
    "v_mfma_f32_16x16x32_bf16 v[24:27], v[108:111], v[96:99], v[24:27]\n"
    "v_mfma_f32_16x16x32_bf16 v[28:31], v[108:111], v[100:103], v[28:31]\n"
    "v_mfma_f32_16x16x32_bf16 v[32:35], v[108:111], v[104:107], v[32:35]\n"
    "v_mfma_f32_16x16x32_bf16 v[40:43], v[8:11], v[88:91], v[40:43]\n"
    "v_mfma_f32_16x16x32_bf16 v[44:47], v[88:91], v[88:91], v[44:47]\n"
    "s_cmp_lt_u32 s41, 9\n"
    "s_cbranch_scc1 11b\n"
    "s_branch 20f\n"
    "32:\n"
    "s_barrier\n"
    "ds_read_b64_tr_b16 v[80:81], v2 offset:0\n"
    "ds_read_b64_tr_b16 v[82:83], v2 offset:512\n"
    "ds_read_b128 v[56:59], v3 offset:0\n"
    "ds_read_b128 v[60:63], v3 offset:2048\n"
    "ds_read_b128 v[64:67], v3 offset:4096\n"
    "ds_read_b128 v[68:71], v3 offset:6144\n"
    "ds_read_b128 v[72:75], v3 offset:8192\n"
    "ds_read_b128 v[76:79], v3 offset:10240\n"
    "12:\n"
    "s_waitcnt lgkmcnt(0)\n"
    "v_mfma_f32_16x16x32_bf16 v[36:39], v[80:83], v[8:11], v[36:39]\n"
    "ds_read_b64_tr_b16 v[108:109], v2 offset:4096\n"
    "ds_read_b64_tr_b16 v[110:111], v2 offset:4608\n"
    "v_mfma_f32_16x16x32_bf16 v[12:15], v[80:83], v[56:59], v[12:15]\n"
    "ds_read_b128 v[84:87], v4 offset:0\n"
    "ds_read_b128 v[88:91], v4 offset:2048\n"
    "v_mfma_f32_16x16x32_bf16 v[16:19], v[80:83], v[60:63], v[16:19]\n"
    "ds_read_b128 v[92:95], v4 offset:4096\n"
    "ds_read_b128 v[96:99], v4 offset:6144\n"
    "v_mfma_f32_16x16x32_bf16 v[20:23], v[80:83], v[64:67], v[20:23]\n"
    "ds_read_b128 v[100:103], v4 offset:8192\n"
    "ds_read_b128 v[104:107], v4 offset:10240\n"
    "v_mfma_f32_16x16x32_bf16 v[24:27], v[80:83], v[68:71], v[24:27]\n"
    "s_add_u32 s41, s41, 1\n"
    "s_cmp_eq_u32 s41, 8\n"
    "s_cselect_b32 s42, -7, 1\n"
    "v_mfma_f32_16x16x32_bf16 v[28:31], v[80:83], v[72:75], v[28:31]\n"
    "s_mul_i32 s42, s42, 20480\n"
    "v_add_u32 v3, s42, v3\n"
    "v_mfma_f32_16x16x32_bf16 v[32:35], v[80:83], v[76:79], v[32:35]\n"
    "v_add_u32 v2, s42, v2\n"
    "v_add_u32 v4, s42, v4\n"
    "v_mfma_f32_16x16x32_bf16 v[40:43], v[8:11], v[64:67], v[40:43]\n"
    "v_mfma_f32_16x16x32_bf16 v[44:47], v[64:67], v[64:67], v[44:47]\n"
    "v_mfma_f32_16x16x32_bf16 v[48:51], v[8:11], v[72:75], v[48:51]\n"
    "v_mfma_f32_16x16x32_bf16 v[52:55], v[72:75], v[72:75], v[52:55]\n"
    "s_waitcnt lgkmcnt(0)\n"
    "s_barrier\n"
    "v_mfma_f32_16x16x32_bf16 v[36:39], v[108:111], v[8:11], v[36:39]\n"
    "ds_read_b64_tr_b16 v[80:81], v2 offset:0\n"
    "ds_read_b64_tr_b16 v[82:83], v2 offset:512\n"
    "v_mfma_f32_16x16x32_bf16 v[12:15], v[108:111], v[84:87], v[12:15]\n"
    "ds_read_b128 v[56:59], v3 offset:0\n"
    "ds_read_b128 v[60:63], v3 offset:2048\n"
    "v_mfma_f32_16x16x32_bf16 v[16:19], v[108:111], v[88:91], v[16:19]\n"
    "ds_read_b128 v[64:67], v3 offset:4096\n"
    "ds_read_b128 v[68:71], v3 offset:6144\n"
    "v_mfma_f32_16x16x32_bf16 v[20:23], v[108:111], v[92:95], v[20:23]\n"
    "ds_read_b128 v[72:75], v3 offset:8192\n"
    "ds_read_b128 v[76:79], v3 offset:10240\n"
    "v_mfma_f32_16x16x32_bf16 v[24:27], v[108:111], v[96:99], v[24:27]\n"
    "v_mfma_f32_16x16x32_bf16 v[28:31], v[108:111], v[100:103], v[28:31]\n"
    "v_mfma_f32_16x16x32_bf16 v[32:35], v[108:111], v[104:107], v[32:35]\n"
    "v_mfma_f32_16x16x32_bf16 v[40:43], v[8:11], v[92:95], v[40:43]\n"
    "v_mfma_f32_16x16x32_bf16 v[44:47], v[92:95], v[92:95], v[44:47]\n"
    "v_mfma_f32_16x16x32_bf16 v[48:51], v[8:11], v[100:103], v[48:51]\n"
    "v_mfma_f32_16x16x32_bf16 v[52:55], v[100:103], v[100:103], v[52:55]\n"
    "s_cmp_lt_u32 s41, 9\n"
    "s_cbranch_scc1 12b\n"
    "s_branch 20f\n"
    "33:\n"
    "s_barrier\n"
    "ds_read_b64_tr_b16 v[80:81], v2 offset:0\n"
    "ds_read_b64_tr_b16 v[82:83], v2 offset:512\n"
    "ds_read_b128 v[56:59], v3 offset:0\n"
    "ds_read_b128 v[60:63], v3 offset:2048\n"
    "ds_read_b128 v[64:67], v3 offset:4096\n"
    "ds_read_b128 v[68:71], v3 offset:6144\n"
    "ds_read_b128 v[72:75], v3 offset:8192\n"
    "ds_read_b128 v[76:79], v3 offset:10240\n"
    "13:\n"
    "s_waitcnt lgkmcnt(0)\n"
    "v_mfma_f32_16x16x32_bf16 v[36:39], v[80:83], v[8:11], v[36:39]\n"
    "ds_read_b64_tr_b16 v[108:109], v2 offset:4096\n"
    "ds_read_b64_tr_b16 v[110:111], v2 offset:4608\n"
    "v_mfma_f32_16x16x32_bf16 v[12:15], v[80:83], v[56:59], v[12:15]\n"
    "ds_read_b128 v[84:87], v4 offset:0\n"
    "ds_read_b128 v[88:91], v4 offset:2048\n"
    "v_mfma_f32_16x16x32_bf16 v[16:19], v[80:83], v[60:63], v[16:19]\n"
    "ds_read_b128 v[92:95], v4 offset:4096\n"
    "ds_read_b128 v[96:99], v4 offset:6144\n"
    "v_mfma_f32_16x16x32_bf16 v[20:23], v[80:83], v[64:67], v[20:23]\n"
    "ds_read_b128 v[100:103], v4 offset:8192\n"
    "ds_read_b128 v[104:107], v4 offset:10240\n"
    "v_mfma_f32_16x16x32_bf16 v[24:27], v[80:83], v[68:71], v[24:27]\n"
    "s_add_u32 s41, s41, 1\n"
    "s_cmp_eq_u32 s41, 8\n"
    "s_cselect_b32 s42, -7, 1\n"
    "v_mfma_f32_16x16x32_bf16 v[28:31], v[80:83], v[72:75], v[28:31]\n"
    "s_mul_i32 s42, s42, 20480\n"
    "v_add_u32 v3, s42, v3\n"
    "v_mfma_f32_16x16x32_bf16 v[32:35], v[80:83], v[76:79], v[32:35]\n"
    "v_add_u32 v2, s42, v2\n"
    "v_add_u32 v4, s42, v4\n"
    "v_mfma_f32_16x16x32_bf16 v[40:43], v[8:11], v[68:71], v[40:43]\n"
    "v_mfma_f32_16x16x32_bf16 v[44:47], v[68:71], v[68:71], v[44:47]\n"
    "v_mfma_f32_16x16x32_bf16 v[48:51], v[8:11], v[76:79], v[48:51]\n"
    "v_mfma_f32_16x16x32_bf16 v[52:55], v[76:79], v[76:79], v[52:55]\n"
    "s_waitcnt lgkmcnt(0)\n"
    "s_barrier\n"
    "v_mfma_f32_16x16x32_bf16 v[36:39], v[108:111], v[8:11], v[36:39]\n"
    "ds_read_b64_tr_b16 v[80:81], v2 offset:0\n"
    "ds_read_b64_tr_b16 v[82:83], v2 offset:512\n"
    "v_mfma_f32_16x16x32_bf16 v[12:15], v[108:111], v[84:87], v[12:15]\n"
    "ds_read_b128 v[56:59], v3 offset:0\n"
    "ds_read_b128 v[60:63], v3 offset:2048\n"
    "v_mfma_f32_16x16x32_bf16 v[16:19], v[108:111], v[88:91], v[16:19]\n"
    "ds_read_b128 v[64:67], v3 offset:4096\n"
    "ds_read_b128 v[68:71], v3 offset:6144\n"
    "v_mfma_f32_16x16x32_bf16 v[20:23], v[108:111], v[92:95], v[20:23]\n"
    "ds_read_b128 v[72:75], v3 offset:8192\n"
    "ds_read_b128 v[76:79], v3 offset:10240\n"
    "v_mfma_f32_16x16x32_bf16 v[24:27], v[108:111], v[96:99], v[24:27]\n"
    "v_mfma_f32_16x16x32_bf16 v[28:31], v[108:111], v[100:103], v[28:31]\n"
    "v_mfma_f32_16x16x32_bf16 v[32:35], v[108:111], v[104:107], v[32:35]\n"
    "v_mfma_f32_16x16x32_bf16 v[40:43], v[8:11], v[96:99], v[40:43]\n"
    "v_mfma_f32_16x16x32_bf16 v[44:47], v[96:99], v[96:99], v[44:47]\n"
    "v_mfma_f32_16x16x32_bf16 v[48:51], v[8:11], v[104:107], v[48:51]\n"
    "v_mfma_f32_16x16x32_bf16 v[52:55], v[104:107], v[104:107], v[52:55]\n"
    "s_cmp_lt_u32 s41, 9\n"
    "s_cbranch_scc1 13b\n"
    "s_branch 20f\n"
    "34:\n"
    "s_barrier\n"
    "ds_read_b64_tr_b16 v[80:81], v2 offset:0\n"
    "ds_read_b64_tr_b16 v[82:83], v2 offset:512\n"
    "ds_read_b128 v[56:59], v3 offset:0\n"
    "ds_read_b128 v[60:63], v3 offset:2048\n"
    "ds_read_b128 v[64:67], v3 offset:4096\n"
    "ds_read_b128 v[68:71], v3 offset:6144\n"
    "ds_read_b128 v[72:75], v3 offset:8192\n"
    "ds_read_b128 v[76:79], v3 offset:10240\n"
    "14:\n"
    "s_waitcnt lgkmcnt(0)\n"
    "v_mfma_f32_16x16x32_bf16 v[36:39], v[8:11], v[80:83], v[36:39]\n"
    "ds_read_b64_tr_b16 v[108:109], v2 offset:4096\n"
    "ds_read_b64_tr_b16 v[110:111], v2 offset:4608\n"
    "v_mfma_f32_16x16x32_bf16 v[12:15], v[56:59], v[80:83], v[12:15]\n"
    "ds_read_b128 v[84:87], v4 offset:0\n"
    "ds_read_b128 v[88:91], v4 offset:2048\n"
    "v_mfma_f32_16x16x32_bf16 v[16:19], v[60:63], v[80:83], v[16:19]\n"
    "ds_read_b128 v[92:95], v4 offset:4096\n"
    "ds_read_b128 v[96:99], v4 offset:6144\n"
    "v_mfma_f32_16x16x32_bf16 v[20:23], v[64:67], v[80:83], v[20:23]\n"
    "ds_read_b128 v[100:103], v4 offset:8192\n"
    "ds_read_b128 v[104:107], v4 offset:10240\n"
    "v_mfma_f32_16x16x32_bf16 v[24:27], v[68:71], v[80:83], v[24:27]\n"
    "s_add_u32 s41, s41, 1\n"
    "s_cmp_eq_u32 s41, 8\n"
    "s_cselect_b32 s42, -7, 1\n"
    "v_mfma_f32_16x16x32_bf16 v[28:31], v[72:75], v[80:83], v[28:31]\n"
    "s_mul_i32 s42, s42, 20480\n"
    "v_add_u32 v3, s42, v3\n"
    "v_mfma_f32_16x16x32_bf16 v[32:35], v[76:79], v[80:83], v[32:35]\n"
    "v_add_u32 v2, s42, v2\n"
    "v_add_u32 v4, s42, v4\n"
    "v_mfma_f32_16x16x32_bf16 v[40:43], v[8:11], v[60:63], v[40:43]\n"
    "v_mfma_f32_16x16x32_bf16 v[44:47], v[60:63], v[60:63], v[44:47]\n"
    "s_waitcnt lgkmcnt(0)\n"
    "s_barrier\n"
    "v_mfma_f32_16x16x32_bf16 v[36:39], v[8:11], v[108:111], v[36:39]\n"
    "ds_read_b64_tr_b16 v[80:81], v2 offset:0\n"
    "ds_read_b64_tr_b16 v[82:83], v2 offset:512\n"
    "v_mfma_f32_16x16x32_bf16 v[12:15], v[84:87], v[108:111], v[12:15]\n"
    "ds_read_b128 v[56:59], v3 offset:0\n"
    "ds_read_b128 v[60:63], v3 offset:2048\n"
    "v_mfma_f32_16x16x32_bf16 v[16:19], v[88:91], v[108:111], v[16:19]\n"
    "ds_read_b128 v[64:67], v3 offset:4096\n"
    "ds_read_b128 v[68:71], v3 offset:6144\n"
    "v_mfma_f32_16x16x32_bf16 v[20:23], v[92:95], v[108:111], v[20:23]\n"
    "ds_read_b128 v[72:75], v3 offset:8192\n"
    "ds_read_b128 v[76:79], v3 offset:10240\n"
    "v_mfma_f32_16x16x32_bf16 v[24:27], v[96:99], v[108:111], v[24:27]\n"
    "v_mfma_f32_16x16x32_bf16 v[28:31], v[100:103], v[108:111], v[28:31]\n"
    "v_mfma_f32_16x16x32_bf16 v[32:35], v[104:107], v[108:111], v[32:35]\n"
    "v_mfma_f32_16x16x32_bf16 v[40:43], v[8:11], v[88:91], v[40:43]\n"
    "v_mfma_f32_16x16x32_bf16 v[44:47], v[88:91], v[88:91], v[44:47]\n"
    "s_cmp_lt_u32 s41, 9\n"
    "s_cbranch_scc1 14b\n"
    "20:\n"
    "v_and_b32 v6, 1, v0\n"
    "v_cmp_eq_u32 vcc, 1, v6\n"
    "v_and_b32 v6, 2, v0\n"
    "v_cmp_eq_u32 s[44:45], 2, v6\n"
    "v_bfe_u32 v6, v0, 2, 2\n"
    "v_bfe_u32 v7, v0, 4, 2\n"
    "v_cmp_eq_u32 s[46:47], v6, v7\n"
    "s_nop 1\n"
    "v_cndmask_b32 v44, v44, v45, vcc\n"
    "v_cndmask_b32 v46, v46, v47, vcc\n"
    "v_cndmask_b32 v44, v44, v46, s[44:45]\n"
    "v_cndmask_b32 v44, 0, v44, s[46:47]\n"
    "v_cndmask_b32 v52, v52, v53, vcc\n"
    "v_cndmask_b32 v54, v54, v55, vcc\n"
    "v_cndmask_b32 v52, v52, v54, s[44:45]\n"
    "v_cndmask_b32 v52, 0, v52, s[46:47]\n"
    "s_nop 1\n"
    "v_permlane32_swap_b32 v44, v52\n"
    "s_mov_b32 vcc_lo, 0\n"
    "s_mov_b32 vcc_hi, -1\n"
    "v_add_f32 v44, v44, v52\n"
    "v_cndmask_b32 v6, v40, v48, vcc\n"
    "v_mov_b32 v7, v44\n"
    "s_nop 1\n"
    "v_permlane16_swap_b32 v44, v7\n"
    "s_nop 1\n"
    "v_add_f32 v7, v44, v7\n"
    "v_mul_f32 v6, 0x3ae38e39, v6\n"
    "v_mul_f32 v7, 0x3ae38e39, v7\n"
    "v_fma_f32 v7, -v6, v6, v7\n"
    "v_max_f32 v7, 0, v7\n"
    "v_cvt_pk_bf16_f32 v6, v6, v7\n"
    "v_and_b32 v7, 0xffff0000, v6\n"
    "v_lshlrev_b32 v6, 16, v6\n"
    "v_add_f32 v7, 0x3727c5ac, v7\n"
    "v_cvt_pk_bf16_f32 v7, v7, v7\n"
    "v_lshlrev_b32 v7, 16, v7\n"
    "v_sqrt_f32 v7, v7\n"
    "ds_write_b32 v5, v6\n"
    "v_cvt_pk_bf16_f32 v7, v7, v7\n"
    "v_lshlrev_b32 v7, 16, v7\n"
    "v_rcp_f32 v7, v7\n"
    "s_nop 0\n"
    "ds_write_b32 v5, v7 offset:512\n"
    "s_waitcnt lgkmcnt(0)\n"
    "s_barrier\n"
    : "+{v[2:5]}"(ad), "={v[12:27]}"(acc), "={v[28:35]}"(acc2), "={v[36:39]}"(cs)
    : "{s40}"(vt ? 4 : w), "{v0}"(threadIdx.x)
    : "v6", "v7", "v8", "v9", "v10", "v11", "v40", "v41", "v42", "v43", "v44", "v45", "v46", "v47", "v48", "v49", "v50", "v51", "v52", "v53", "v54", "v55", "v56", "v57", "v58", "v59", "v60", "v61", "v62", "v63", "v64", "v65", "v66", "v67", "v68", "v69", "v70", "v71", "v72", "v73", "v74", "v75", "v76", "v77", "v78", "v79", "v80", "v81", "v82", "v83", "v84", "v85", "v86", "v87", "v88", "v89", "v90", "v91", "v92", "v93", "v94", "v95", "v96", "v97", "v98", "v99", "v100", "v101", "v102", "v103", "v104", "v105", "v106", "v107", "v108", "v109", "v110", "v111", "s41", "s42", "s44", "s45", "s46", "s47", "vcc", "scc", "memory");
  auto val = [&](int t, int e) { return t < 4 ? acc[4 * t + e] : acc2[4 * t - 16 + e]; };
  v2u ov[6];
  if (!vt) {
    float mu[6], rd[6];
#pragma unroll
    for (int t = 0; t < 6; ++t) { const LAS float* sp = (const LAS float*)(uintptr_t)(exr + t * 64); mu[t] = sp[0]; rd[t] = sp[128]; }
    const float sc = kv ? 1.f : QSCALE;
#pragma unroll
    for (int t = 0; t < 6; ++t) {
      float o[4];
#pragma unroll
      for (int e = 0; e < 4; ++e) o[e] = __builtin_fmaf(-mu[t], cs[e], val(t, e)) * (rd[t] * sc);
      ov[t] = v2u{pk2(o[0], o[1]), pk2(o[2], o[3])};
    }
  } else {
    f32x4 mu[6], rd[6];
#pragma unroll
    for (int t = 0; t < 6; ++t) { mu[t] = *(const LAS f32x4*)(uintptr_t)(exr + t * 64); rd[t] = *(const LAS f32x4*)(uintptr_t)(exr + t * 64 + 512); }
#pragma unroll
    for (int t = 0; t < 6; ++t) {
      float o[4];
#pragma unroll
      for (int e = 0; e < 4; ++e) o[e] = __builtin_fmaf(-mu[t][e], cs[0], val(t, e)) * rd[t][e];
      ov[t] = v2u{pk2(o[0], o[1]), pk2(o[2], o[3])};
    }
  }
  if (!kv || w < 2) {
#pragma unroll
    for (int t = 0; t < 6; ++t) *(LAS v2u*)(uintptr_t)(((t & 1) ? ooff1 : ooff) + (t >> 1) * os2) = ov[t];
  } else if (w == 2) {
#pragma unroll
    for (int t = 0; t < 6; ++t) *(LAS v2u*)(uintptr_t)(ooff + (t & 1) * 128 + (t >> 1) * KVT) = v2u{ov[t][1], 0x3f803f80u};
  } else {
#pragma unroll
    for (int t = 0; t < 6; ++t) {
      LAS u16* vp = (LAS u16*)(uintptr_t)(ooff + (t & 1) * 640 + (t >> 1) * KVT);
      vp[0] = (u16)ov[t][1]; vp[8] = (u16)(ov[t][1] >> 16); vp[16] = 0x3f80; vp[24] = 0;
    }
  }
  asm volatile("s_waitcnt lgkmcnt(0)\n\ts_barrier" ::: "memory");
  {
    const unsigned sr = kv ? 4096 : 32 * 144, sw = kv ? 4096 : 32 * D * 2;
    v4u c[3];
#pragma unroll
    for (int i = 0; i < 3; ++i) c[i] = *(const LAS v4u*)(uintptr_t)(cpr + i * sr);
#pragma unroll
    for (int i = 0; i < 3; ++i) __builtin_amdgcn_raw_buffer_store_b128(c[i], rq, cpw, i * sw, 16);
  }
  if (kv && w >= 2) {
    const unsigned key = 48 * (w - 2) + min(lane, 47u), ka = stg + (key >> 5) * KVT + (key & 31) * 16;
    const v4u k0 = *(const LAS v4u*)(uintptr_t)ka, k1 = *(const LAS v4u*)(uintptr_t)(ka + 512);
    const unsigned k2 = *(const LAS unsigned*)(uintptr_t)(ka + 1024 - (key & 31) * 8);
    const float n2 = (dot2(k0[0], dot2(k0[1], dot2(k2, 0.f))) + dot2(k0[2], dot2(k0[3], 0.f))) + (dot2(k1[0], dot2(k1[1], 0.f)) + dot2(k1[2], dot2(k1[3], 0.f)));
    const float nm = max32(max16(rowmax16(n2)));
    if (lane == 0) ((float*)(ws + WS_KN))[(w - 2) * 32 + grp] = nm;
  }
}

__device__ __forceinline__ f32x16 qk(v4u k0, v2u k1, v4u q0, v2u q1) {
  f32x16 z = {};
  f32x16 s = __builtin_amdgcn_mfma_f32_32x32x16_bf16(__builtin_bit_cast(bf16x8, k0), __builtin_bit_cast(bf16x8, q0), z, 0, 0, 0);
  return __builtin_amdgcn_mfma_f32_32x32x8bf16_1k(__builtin_bit_cast(s16x4, k1), __builtin_bit_cast(s16x4, q1), s, 0, 0, 0);
}
__device__ __forceinline__ unsigned bf_trunc(float f) { return __builtin_bit_cast(unsigned, f) >> 16; }
__device__ __forceinline__ int sel(int mask, int a, int b) { return b ^ ((a ^ b) & mask); }
__device__ __forceinline__ void pt_setup(int row, int blk, int j, const u16* w1, const u16* w2, unsigned char* ws, const u16*& src, unsigned char*& dst) {
  const int m2 = (KS - 1 - row) >> 31, s = row - (KS & m2);
  const uintptr_t base = (uintptr_t)w1 ^ (((uintptr_t)w1 ^ (uintptr_t)w2) & (uintptr_t)(intptr_t)m2);
  src = (const u16*)(base + (unsigned)(j * sel(m2, D * D * 2, D * 2) + s * sel(m2, 64 * D, 64 * F) + 128 * blk));
  const int t = sel(m2, 9 + ((((blk - j - 1) & 7) + 1) & ((blk - 8) >> 31)), blk);
  dst = ws + (unsigned)(WS_W12 + ((j * 72 + t) * KS + s) * 1024);
}

__global__ void __launch_bounds__(256, 1) k_main(const u16* x, const u16* w1, const u16* b1, const u16* w2, const u16* b2, unsigned char* ws, u16* out) {
  extern __shared__ __attribute__((aligned(16))) unsigned char smem[];
  const int w = __builtin_amdgcn_readfirstlane(threadIdx.x >> 6);
  const int tq = w & 1, kh = w >> 1;
  const unsigned lds0 = (unsigned)(uintptr_t)(LAS unsigned char*)smem;
  int G, j; ffn_map(blockIdx.x, G, j);
  const int tb = 8 * G + j;
  const unsigned lane = threadIdx.x & 63, r32 = lane & 31, hi = lane >> 5;
  v16u qa; v8u qb; unsigned kn1;
  const unsigned qs = lds0 + kh * KVH + 65536 + tq * 8192;
  {
    const unsigned char* qg = ws + (unsigned)((8 * tb + 4 * tq) * (D * 2));
    glds16x4(qg, lane * 16, qs); glds16(qg + 4096 + lane * 16, qs + 4096);
    asm volatile("global_load_dword v10, %1, %2" : "=&{v10}"(kn1) : "v"(hi * 128 + min(r32, 21u) * 4), "s"(ws + WS_KN) : "memory");
  }
  const unsigned char* kvg = ws + (unsigned)(WS_KV + kh * KVH + tq * 4096);
  const unsigned lb = lds0 + kh * KVH;
  glds16x4(kvg, lane * 16, lb + tq * 4096);
  SBAR();
  const u16* pp[3]; unsigned char* pd[3];
  const int id2 = 2 * G + w - 1, r2 = (id2 * 57) >> 9;
  const int mx2 = ((-w) >> 31) & ((w - 3) >> 31) & ((id2 - 36) >> 31), has2 = mx2 | ((w - 1) >> 31);
  pt_setup(G, 2 * w, j, w1, w2, ws, pp[0], pd[0]); pt_setup(G, 2 * w + 1, j, w1, w2, ws, pp[1], pd[1]);
  pt_setup(sel(mx2, 32 + r2, G), sel(mx2, id2 - 9 * r2, 8), j, w1, w2, ws, pp[2], pd[2]);
  const unsigned pk = (7 & ((KS - 1 - G) >> 31)) | (4 & mx2);
  const unsigned wlm = lb + tq * (25 * 1024), vrow = r32 < 19 ? r32 : 19;
  v8u vin = {lane * 16u, lb + lane * 16, lb + 1024 + r32 * 8, lb + 1280 + hi * 320 + vrow * 16, lb + tq * 12288 + (lane >> 2) * 128 + (lane & 3) * 8,
             (lane >> 3) * (F * 2) + (lane & 7) * 16, (lane >> 3) * (D * 2) + (lane & 7) * 16, wlm + lane * 16};
  const unsigned char* wop = ws + (unsigned)(WS_WO + w * 162 * 1024);
  const unsigned char* wlp = wop + (162 - 25) * 1024;
  const u16* xb = x + (unsigned)(8 * tb * D + 1024 * w);
  const unsigned char* pbase = ws + (unsigned)(WS_P + (G * 32 + w) * 16384);
  const unsigned char* f0 = pbase + (j << SHS);
  const unsigned char* f8 = ws + (unsigned)(WS_P8 + (G * 8 + 2 * w) * 8192 + j * 1024);
  const unsigned char* cnt = ws + (unsigned)(WS_CNT + j * 256);
  const unsigned char* hnw = ws + (unsigned)(WS_HN + tb * 9216);
  const unsigned char* hnr = ws + (unsigned)(WS_HN + (8 * G + 2 * w) * 9216);
  const unsigned char* wp = ws + (unsigned)(WS_W12 + (j * 4 + w) * (2 * 9 * KS * 1024));
  const u16* b1p = b1 + (unsigned)(D * j + 16 * w);
  const u16* b2p = b2 + (unsigned)(16 * w);
  const u16* op = out + (unsigned)(64 * G * D + 64 * j + 16 * w);
  unsigned sc_[48] = {plo(kvg), phi(kvg), plo(wop), phi(wop), plo(wlp), phi(wlp), plo(xb), phi(xb), plo(f0), phi(f0), plo(f8), phi(f8), plo(cnt), phi(cnt),
                      plo(pp[0]), phi(pp[0]), plo(pp[1]), phi(pp[1]), plo(pp[2]), phi(pp[2]), plo(pd[0]), phi(pd[0]), plo(pd[1]), phi(pd[1]), plo(pd[2]), phi(pd[2]),
                      plo(hnw), phi(hnw), plo(hnr), phi(hnr), lds0, (unsigned)w, (unsigned)j, (unsigned)(2 - has2), pk, wlm,
                      plo(wp), phi(wp), 2u * 9 * KS * 1024, 0x00020000u, plo(b1p), phi(b1p), plo(b2p), phi(b2p), plo(op), phi(op), plo(pbase), phi(pbase)};
#pragma unroll
  for (int i = 0; i < 48; ++i) sc_[i] = rfl(sc_[i]);
#pragma unroll
  for (int i = 0; i < 48; i += 8) asm volatile("" : "+s"(sc_[i]), "+s"(sc_[i + 1]), "+s"(sc_[i + 2]), "+s"(sc_[i + 3]), "+s"(sc_[i + 4]), "+s"(sc_[i + 5]), "+s"(sc_[i + 6]), "+s"(sc_[i + 7]));
  v32u oa = {}, ob = {};
  unsigned mneg;
  v2u qad = {qs + r32 * (DH * 2) + hi * 16, qs + r32 * (DH * 2)};
  asm volatile(
    "s_waitcnt vmcnt(4)\n"
    "ds_read2_b32 v[38:39], v20 offset0:0 offset1:1\n"
    "ds_read2_b32 v[40:41], v20 offset0:2 offset1:3\n"
    "ds_read_b32 v42, v21 offset:32\n"
    "v_add_u32 v20, 1152, v20\n"
    "ds_read2_b32 v[44:45], v20 offset0:0 offset1:1\n"
    "ds_read2_b32 v[46:47], v20 offset0:2 offset1:3\n"
    "ds_read_b32 v48, v21 offset:1184\n"
    "v_add_u32 v20, 1152, v20\n"
    "ds_read2_b32 v[50:51], v20 offset0:0 offset1:1\n"
    "ds_read2_b32 v[52:53], v20 offset0:2 offset1:3\n"
    "ds_read_b32 v54, v21 offset:2336\n"
    "v_add_u32 v20, 1152, v20\n"
    "ds_read2_b32 v[56:57], v20 offset0:0 offset1:1\n"
    "ds_read2_b32 v[58:59], v20 offset0:2 offset1:3\n"
    "ds_read_b32 v60, v21 offset:3488\n"
    "v_mov_b32 v11, v10\n"
    "v_mov_b32 v12, 0\n"
    "v_mov_b32 v13, 0\n"
    "v_mov_b32 v14, 0\n"
    "v_mov_b32 v15, 0\n"
    "v_permlane32_swap_b32 v10, v11\n"
    "s_mov_b32 vcc_lo, -1\n"
    "s_mov_b32 vcc_hi, 0\n"
    "s_waitcnt lgkmcnt(0)\n"
    "v_cndmask_b32 v42, 0, v42, vcc\n"
    "v_cndmask_b32 v48, 0, v48, vcc\n"
    "v_cndmask_b32 v54, 0, v54, vcc\n"
    "v_cndmask_b32 v60, 0, v60, vcc\n"
    "v_max_f32 v10, v10, v11\n"
    "v_dot2c_f32_bf16 v12, v38, v38\n"
    "v_dot2c_f32_bf16 v13, v44, v44\n"
    "v_dot2c_f32_bf16 v14, v50, v50\n"
    "v_dot2c_f32_bf16 v15, v56, v56\n"
    "v_max_f32_dpp v10, v10, v10 row_ror:8 row_mask:0xf bank_mask:0xf\n"
    "v_dot2c_f32_bf16 v12, v39, v39\n"
    "v_dot2c_f32_bf16 v13, v45, v45\n"
    "v_dot2c_f32_bf16 v14, v51, v51\n"
    "v_dot2c_f32_bf16 v15, v57, v57\n"
    "v_max_f32_dpp v10, v10, v10 row_ror:4 row_mask:0xf bank_mask:0xf\n"
    "v_dot2c_f32_bf16 v12, v40, v40\n"
    "v_dot2c_f32_bf16 v13, v46, v46\n"
    "v_dot2c_f32_bf16 v14, v52, v52\n"
    "v_dot2c_f32_bf16 v15, v58, v58\n"
    "v_max_f32_dpp v10, v10, v10 row_ror:2 row_mask:0xf bank_mask:0xf\n"
    "v_dot2c_f32_bf16 v12, v41, v41\n"
    "v_dot2c_f32_bf16 v13, v47, v47\n"
    "v_dot2c_f32_bf16 v14, v53, v53\n"
    "v_dot2c_f32_bf16 v15, v59, v59\n"
    "v_max_f32_dpp v10, v10, v10 row_ror:1 row_mask:0xf bank_mask:0xf\n"
    "v_dot2c_f32_bf16 v12, v42, v42\n"
    "v_dot2c_f32_bf16 v13, v48, v48\n"
    "v_dot2c_f32_bf16 v14, v54, v54\n"
    "v_dot2c_f32_bf16 v15, v60, v60\n"
    "v_mov_b32 v11, v10\n"
    "s_nop 0\n"
    "v_mov_b32 v16, v12\n"
    "v_mov_b32 v17, v13\n"
    "v_mov_b32 v18, v14\n"
    "v_mov_b32 v19, v15\n"
    "v_permlane16_swap_b32 v10, v11\n"
    "v_permlane32_swap_b32 v12, v16\n"
    "v_permlane32_swap_b32 v13, v17\n"
    "v_permlane32_swap_b32 v14, v18\n"
    "v_permlane32_swap_b32 v15, v19\n"
    "v_max_f32 v10, v10, v11\n"
    "v_add_f32 v12, v12, v16\n"
    "v_add_f32 v13, v13, v17\n"
    "v_add_f32 v14, v14, v18\n"
    "v_add_f32 v15, v15, v19\n"
    "v_mul_f32 v12, v12, v10\n"
    "v_mul_f32 v13, v13, v10\n"
    "v_mul_f32 v14, v14, v10\n"
    "v_mul_f32 v15, v15, v10\n"
    "v_sqrt_f32 v12, v12\n"
    "v_sqrt_f32 v13, v13\n"
    "v_sqrt_f32 v14, v14\n"
    "v_sqrt_f32 v15, v15\n"
    "v_mul_f32 v12, 0xbf828f5c, v12\n"
    "v_mul_f32 v13, 0xbf828f5c, v13\n"
    "v_mul_f32 v14, 0xbf828f5c, v14\n"
    "v_mul_f32 v15, 0xbf828f5c, v15\n"
    "v_lshrrev_b32 v16, 16, v12\n"
    "v_lshrrev_b32 v17, 16, v13\n"
    "v_lshrrev_b32 v18, 16, v14\n"
    "v_lshrrev_b32 v19, 16, v15\n"
    "v_max3_u32 v11, v12, v13, v14\n"
    "v_max_u32 v11, v11, v15\n"
    "v_cndmask_b32 v43, 0, v16, vcc\n"
    "v_cndmask_b32 v49, 0, v17, vcc\n"
    "v_cndmask_b32 v55, 0, v18, vcc\n"
    "v_cndmask_b32 v61, 0, v19, vcc\n"
    : "=&{v[38:53]}"(qa), "=&{v[54:61]}"(qb), "+{v10}"(kn1), "=&{v11}"(mneg), "+{v[2:9]}"(vin), "+{v[62:93]}"(oa), "+{v[94:125]}"(ob), "+{v[20:21]}"(qad)
    :: "v12", "v13", "v14", "v15", "v16", "v17", "v18", "v19", "vcc", "memory");
  if (__builtin_expect(__any(!(__builtin_bit_cast(float, mneg) >= -MSAFE)), 0)) {
    const v4u q0[4] = {{qa[0], qa[1], qa[2], qa[3]}, {qa[6], qa[7], qa[8], qa[9]}, {qa[12], qa[13], qa[14], qa[15]}, {qb[2], qb[3], qb[4], qb[5]}};
    const unsigned q16[4] = {qa[4], qa[10], qb[0], qb[6]};
    unsigned mq[4];
    float mx[4] = {-INFINITY, -INFINITY, -INFINITY, -INFINITY};
    for (int t = 0; t < 64; ++t) {
      const unsigned char* kt = ws + WS_KV + t * KVT;
      const v4u k0 = *(const v4u*)(kt + lane * 16); const v2u k1 = *(const v2u*)(kt + 1024 + r32 * 8);
#pragma unroll
      for (int r = 0; r < 4; ++r) {
        const f32x16 s = qk(k0, k1, q0[r], v2u{q16[r], 0u});
#pragma unroll
        for (int e = 0; e < 16; ++e) mx[r] = __builtin_fmaxf(mx[r], s[e]);
      }
    }
#pragma unroll
    for (int r = 0; r < 4; ++r) {
      const float nm = -max32(mx[r]);
      const unsigned m1 = bf_trunc(nm); const float r1 = nm - bflo(m1);
      const unsigned m2 = bf_trunc(r1); const float r2 = r1 - bflo(m2);
      mq[r] = hi ? bf_trunc(r2) : (m1 | (m2 << 16));
    }
    qa[5] = mq[0]; qa[11] = mq[1]; qb[1] = mq[2]; qb[7] = mq[3];
  }
  v16u sa, sb, sc;
#pragma unroll
  for (int i = 0; i < 16; ++i) { sa[i] = sc_[i]; sb[i] = sc_[16 + i]; sc[i] = sc_[32 + i]; }
  asm volatile(
    "s_waitcnt vmcnt(0)\n"
    "s_barrier\n"
    "ds_read_b128 v[10:13], v3 offset:0\n"
    "ds_read_b64 v[14:15], v4 offset:0\n"
    "ds_read_b128 v[16:19], v5 offset:0\n"
    "ds_read_b128 v[20:23], v5 offset:640\n"
    "s_mov_b32 s86, m0\n"
    "s_mov_b32 s92, 262144\n"
    "v_readfirstlane_b32 s84, v3\n"
    "s_and_b32 s85, s67, 1\n"
    "s_lshl_b32 s85, s85, 12\n"
    "s_add_u32 m0, s84, s85\n"
    "s_waitcnt lgkmcnt(2)\n"
    "v_mfma_f32_32x32x16_bf16 v[126:141], v[10:13], v[38:41], 0\n"
    "v_mfma_f32_32x32x8_bf16 v[126:141], v[14:15], v[42:43], v[126:141]\n"
    "v_mov_b32 v158, 0xdddd\n"
    "v_mov_b32 v160, -1\n"
    "v_mov_b32 v161, -1\n"
    "v_mov_b32 v162, -1\n"
    "v_mov_b32 v163, -1\n"
    "s_waitcnt lgkmcnt(0)\n"
    "s_nop 5\n"
    "v_exp_f32 v126, v126\n"
    "v_exp_f32 v127, v127\n"
    "v_exp_f32 v128, v128\n"
    "v_exp_f32 v129, v129\n"
    "v_exp_f32 v130, v130\n"
    "v_mfma_f32_32x32x16_bf16 v[142:157], v[10:13], v[44:47], 0\n"
    "ds_read_b128 v[24:27], v3 offset:2560\n"
    "ds_read_b64 v[28:29], v4 offset:2560\n"
    "s_add_u32 m0, m0, 8192\n"
    "v_exp_f32 v131, v131\n"
    "v_exp_f32 v132, v132\n"
    "v_exp_f32 v133, v133\n"
    "v_mfma_f32_32x32x8_bf16 v[142:157], v[14:15], v[48:49], v[142:157]\n"
    "s_add_u32 s36, s36, 8192\n"
    "s_addc_u32 s37, s37, 0\n"
    "global_load_lds_dwordx4 v2, s[36:37] offset:0\n"
    "v_exp_f32 v134, v134\n"
    "v_exp_f32 v135, v135\n"
    "v_exp_f32 v136, v136\n"
    "v_exp_f32 v137, v137\n"
    "v_smfmac_f32_32x32x32_bf16 v[62:77], v[16:19], v[126:133], v158\n"
    "global_load_lds_dwordx4 v2, s[36:37] offset:1024\n"
    "global_load_lds_dwordx4 v2, s[36:37] offset:2048\n"
    "v_exp_f32 v138, v138\n"
    "v_exp_f32 v139, v139\n"
    "v_exp_f32 v140, v140\n"
    "v_exp_f32 v141, v141\n"
    "v_exp_f32 v142, v142\n"
    "v_exp_f32 v143, v143\n"
    "v_smfmac_f32_32x32x32_bf16 v[62:77], v[20:23], v[134:141], v158\n"
    "ds_read_b128 v[30:33], v5 offset:2560\n"
    "global_load_lds_dwordx4 v2, s[36:37] offset:3072\n"
    "v_exp_f32 v144, v144\n"
    "v_exp_f32 v145, v145\n"
    "v_exp_f32 v146, v146\n"
    "v_mfma_f32_32x32x16_bf16 v[126:141], v[10:13], v[50:53], 0\n"
    "s_add_u32 m0, m0, 8192\n"
    "v_exp_f32 v147, v147\n"
    "v_exp_f32 v148, v148\n"
    "v_exp_f32 v149, v149\n"
    "v_mfma_f32_32x32x8_bf16 v[126:141], v[14:15], v[54:55], v[126:141]\n"
    "s_add_u32 s36, s36, 8192\n"
    "s_addc_u32 s37, s37, 0\n"
    "v_exp_f32 v150, v150\n"
    "v_exp_f32 v151, v151\n"
    "v_exp_f32 v152, v152\n"
    "v_exp_f32 v153, v153\n"
    "v_smfmac_f32_32x32x32_bf16 v[78:93], v[16:19], v[142:149], v158\n"
    "global_load_lds_dwordx4 v2, s[36:37] offset:0\n"
    "global_load_lds_dwordx4 v2, s[36:37] offset:1024\n"
    "v_exp_f32 v154, v154\n"
    "v_exp_f32 v155, v155\n"
    "v_exp_f32 v156, v156\n"
    "v_exp_f32 v157, v157\n"
    "v_exp_f32 v126, v126\n"
    "v_exp_f32 v127, v127\n"
    "v_smfmac_f32_32x32x32_bf16 v[78:93], v[20:23], v[150:157], v158\n"
    "ds_read_b128 v[34:37], v5 offset:3200\n"
    "global_load_lds_dwordx4 v2, s[36:37] offset:2048\n"
    "v_exp_f32 v128, v128\n"
    "v_exp_f32 v129, v129\n"
    "v_exp_f32 v130, v130\n"
    "v_mfma_f32_32x32x16_bf16 v[142:157], v[10:13], v[56:59], 0\n"
    "global_load_lds_dwordx4 v2, s[36:37] offset:3072\n"
    "s_add_u32 m0, m0, 8192\n"
    "v_exp_f32 v131, v131\n"
    "v_exp_f32 v132, v132\n"
    "v_exp_f32 v133, v133\n"
    "v_mfma_f32_32x32x8_bf16 v[142:157], v[14:15], v[60:61], v[142:157]\n"
    "s_add_u32 s36, s36, 8192\n"
    "s_addc_u32 s37, s37, 0\n"
    "v_exp_f32 v134, v134\n"
    "v_exp_f32 v135, v135\n"
    "v_exp_f32 v136, v136\n"
    "v_exp_f32 v137, v137\n"
    "v_smfmac_f32_32x32x32_bf16 v[94:109], v[16:19], v[126:133], v158\n"
    "global_load_lds_dwordx4 v2, s[36:37] offset:0\n"
    "v_exp_f32 v138, v138\n"
    "v_exp_f32 v139, v139\n"
    "v_exp_f32 v140, v140\n"
    "v_exp_f32 v141, v141\n"
    "v_exp_f32 v142, v142\n"
    "v_exp_f32 v143, v143\n"
    "v_smfmac_f32_32x32x32_bf16 v[94:109], v[20:23], v[134:141], v158\n"
    "v_exp_f32 v144, v144\n"
    "v_exp_f32 v145, v145\n"
    "v_exp_f32 v146, v146\n"
    "s_waitcnt lgkmcnt(0)\n"
    "v_mfma_f32_32x32x16_bf16 v[126:141], v[24:27], v[38:41], 0\n"
    "v_exp_f32 v147, v147\n"
    "v_exp_f32 v148, v148\n"
    "v_exp_f32 v149, v149\n"
    "v_mfma_f32_32x32x8_bf16 v[126:141], v[28:29], v[42:43], v[126:141]\n"
    "global_load_lds_dwordx4 v2, s[36:37] offset:1024\n"
    "global_load_lds_dwordx4 v2, s[36:37] offset:2048\n"
    "v_exp_f32 v150, v150\n"
    "v_exp_f32 v151, v151\n"
    "v_exp_f32 v152, v152\n"
    "v_exp_f32 v153, v153\n"
    "v_smfmac_f32_32x32x32_bf16 v[110:125], v[16:19], v[142:149], v158\n"
    "global_load_lds_dwordx4 v2, s[36:37] offset:3072\n"
    "v_exp_f32 v154, v154\n"
    "v_exp_f32 v155, v155\n"
    "v_exp_f32 v156, v156\n"
    "v_exp_f32 v157, v157\n"
    "v_exp_f32 v126, v126\n"
    "v_exp_f32 v127, v127\n"
    "v_smfmac_f32_32x32x32_bf16 v[110:125], v[20:23], v[150:157], v158\n"
    "v_exp_f32 v128, v128\n"
    "v_exp_f32 v129, v129\n"
    "v_exp_f32 v130, v130\n"
    "v_mfma_f32_32x32x16_bf16 v[142:157], v[24:27], v[44:47], 0\n"
    "v_exp_f32 v131, v131\n"
    "v_exp_f32 v132, v132\n"
    "v_exp_f32 v133, v133\n"
    "v_mfma_f32_32x32x8_bf16 v[142:157], v[28:29], v[48:49], v[142:157]\n"
    "ds_read_b128 v[10:13], v3 offset:5120\n"
    "ds_read_b64 v[14:15], v4 offset:5120\n"
    "v_exp_f32 v134, v134\n"
    "v_exp_f32 v135, v135\n"
    "v_exp_f32 v136, v136\n"
    "v_exp_f32 v137, v137\n"
    "v_smfmac_f32_32x32x32_bf16 v[62:77], v[30:33], v[126:133], v158\n"
    "global_load_dwordx4 a[0:3], v2, s[38:39] offset:0\n"
    "global_load_dwordx4 a[4:7], v2, s[38:39] offset:1024\n"
    "v_exp_f32 v138, v138\n"
    "v_exp_f32 v139, v139\n"
    "v_exp_f32 v140, v140\n"
    "v_exp_f32 v141, v141\n"
    "v_exp_f32 v142, v142\n"
    "v_exp_f32 v143, v143\n"
    "v_smfmac_f32_32x32x32_bf16 v[62:77], v[34:37], v[134:141], v158\n"
    "v_exp_f32 v144, v144\n"
    "v_exp_f32 v145, v145\n"
    "v_exp_f32 v146, v146\n"
    "v_mfma_f32_32x32x16_bf16 v[126:141], v[24:27], v[50:53], 0\n"
    "v_exp_f32 v147, v147\n"
    "v_exp_f32 v148, v148\n"
    "v_exp_f32 v149, v149\n"
    "v_mfma_f32_32x32x8_bf16 v[126:141], v[28:29], v[54:55], v[126:141]\n"
    "ds_read_b128 v[16:19], v5 offset:5120\n"
    "global_load_dwordx4 a[8:11], v2, s[38:39] offset:2048\n"
    "v_exp_f32 v150, v150\n"
    "v_exp_f32 v151, v151\n"
    "v_exp_f32 v152, v152\n"
    "v_exp_f32 v153, v153\n"
    "v_smfmac_f32_32x32x32_bf16 v[78:93], v[30:33], v[142:149], v158\n"
    "global_load_dwordx4 a[12:15], v2, s[38:39] offset:3072\n"
    "v_exp_f32 v154, v154\n"
    "v_exp_f32 v155, v155\n"
    "v_exp_f32 v156, v156\n"
    "v_exp_f32 v157, v157\n"
    "v_exp_f32 v126, v126\n"
    "v_exp_f32 v127, v127\n"
    "v_smfmac_f32_32x32x32_bf16 v[78:93], v[34:37], v[150:157], v158\n"
    "v_exp_f32 v128, v128\n"
    "v_exp_f32 v129, v129\n"
    "v_exp_f32 v130, v130\n"
    "v_mfma_f32_32x32x16_bf16 v[142:157], v[24:27], v[56:59], 0\n"
    "ds_read_b128 v[20:23], v5 offset:5760\n"
    "v_exp_f32 v131, v131\n"
    "v_exp_f32 v132, v132\n"
    "v_exp_f32 v133, v133\n"
    "v_mfma_f32_32x32x8_bf16 v[142:157], v[28:29], v[60:61], v[142:157]\n"
    "s_add_u32 s38, s38, 8192\n"
    "s_addc_u32 s39, s39, 0\n"
    "v_exp_f32 v134, v134\n"
    "v_exp_f32 v135, v135\n"
    "v_exp_f32 v136, v136\n"
    "v_exp_f32 v137, v137\n"
    "v_smfmac_f32_32x32x32_bf16 v[94:109], v[30:33], v[126:133], v158\n"
    "global_load_dwordx4 a[16:19], v2, s[38:39] offset:-4096\n"
    "global_load_dwordx4 a[20:23], v2, s[38:39] offset:-3072\n"
    "v_exp_f32 v138, v138\n"
    "v_exp_f32 v139, v139\n"
    "v_exp_f32 v140, v140\n"
    "v_exp_f32 v141, v141\n"
    "v_exp_f32 v142, v142\n"
    "v_exp_f32 v143, v143\n"
    "v_smfmac_f32_32x32x32_bf16 v[94:109], v[34:37], v[134:141], v158\n"
    "v_exp_f32 v144, v144\n"
    "v_exp_f32 v145, v145\n"
    "v_exp_f32 v146, v146\n"
    "s_waitcnt lgkmcnt(0)\n"
    "v_mfma_f32_32x32x16_bf16 v[126:141], v[10:13], v[38:41], 0\n"
    "v_exp_f32 v147, v147\n"
    "v_exp_f32 v148, v148\n"
    "v_exp_f32 v149, v149\n"
    "v_mfma_f32_32x32x8_bf16 v[126:141], v[14:15], v[42:43], v[126:141]\n"
    "global_load_dwordx4 a[24:27], v2, s[38:39] offset:-2048\n"
    "global_load_dwordx4 a[28:31], v2, s[38:39] offset:-1024\n"
    "v_exp_f32 v150, v150\n"
    "v_exp_f32 v151, v151\n"
    "v_exp_f32 v152, v152\n"
    "v_exp_f32 v153, v153\n"
    "v_smfmac_f32_32x32x32_bf16 v[110:125], v[30:33], v[142:149], v158\n"
    "s_nop 0\n"
    "v_exp_f32 v154, v154\n"
    "v_exp_f32 v155, v155\n"
    "v_exp_f32 v156, v156\n"
    "v_exp_f32 v157, v157\n"
    "s_waitcnt vmcnt(8)\n"
    "s_barrier\n"
    "v_exp_f32 v126, v126\n"
    "v_exp_f32 v127, v127\n"
    "v_smfmac_f32_32x32x32_bf16 v[110:125], v[34:37], v[150:157], v158\n"
    "ds_read_b128 v[24:27], v3 offset:7680\n"
    "ds_read_b64 v[28:29], v4 offset:7680\n"
    "v_exp_f32 v128, v128\n"
    "v_exp_f32 v129, v129\n"
    "v_exp_f32 v130, v130\n"
    "v_mfma_f32_32x32x16_bf16 v[142:157], v[10:13], v[44:47], 0\n"
    "s_add_u32 m0, m0, 8192\n"
    "v_exp_f32 v131, v131\n"
    "v_exp_f32 v132, v132\n"
    "v_exp_f32 v133, v133\n"
    "v_mfma_f32_32x32x8_bf16 v[142:157], v[14:15], v[48:49], v[142:157]\n"
    "s_add_u32 s36, s36, 8192\n"
    "s_addc_u32 s37, s37, 0\n"
    "v_exp_f32 v134, v134\n"
    "v_exp_f32 v135, v135\n"
    "v_exp_f32 v136, v136\n"
    "v_exp_f32 v137, v137\n"
    "v_smfmac_f32_32x32x32_bf16 v[62:77], v[16:19], v[126:133], v158\n"
    "global_load_lds_dwordx4 v2, s[36:37] offset:0\n"
    "global_load_dwordx4 a[32:35], v2, s[38:39] offset:0\n"
    "v_exp_f32 v138, v138\n"
    "v_exp_f32 v139, v139\n"
    "v_exp_f32 v140, v140\n"
    "v_exp_f32 v141, v141\n"
    "v_exp_f32 v142, v142\n"
    "v_exp_f32 v143, v143\n"
    "v_smfmac_f32_32x32x32_bf16 v[62:77], v[20:23], v[134:141], v158\n"
    "v_exp_f32 v144, v144\n"
    "v_exp_f32 v145, v145\n"
    "v_exp_f32 v146, v146\n"
    "v_mfma_f32_32x32x16_bf16 v[126:141], v[10:13], v[50:53], 0\n"
    "v_exp_f32 v147, v147\n"
    "v_exp_f32 v148, v148\n"
    "v_exp_f32 v149, v149\n"
    "v_mfma_f32_32x32x8_bf16 v[126:141], v[14:15], v[54:55], v[126:141]\n"
    "ds_read_b128 v[30:33], v5 offset:7680\n"
    "global_load_lds_dwordx4 v2, s[36:37] offset:1024\n"
    "v_exp_f32 v150, v150\n"
    "v_exp_f32 v151, v151\n"
    "v_exp_f32 v152, v152\n"
    "v_exp_f32 v153, v153\n"
    "v_smfmac_f32_32x32x32_bf16 v[78:93], v[16:19], v[142:149], v158\n"
    "global_load_dwordx4 a[36:39], v2, s[38:39] offset:1024\n"
    "v_exp_f32 v154, v154\n"
    "v_exp_f32 v155, v155\n"
    "v_exp_f32 v156, v156\n"
    "v_exp_f32 v157, v157\n"
    "v_exp_f32 v126, v126\n"
    "v_exp_f32 v127, v127\n"
    "v_smfmac_f32_32x32x32_bf16 v[78:93], v[20:23], v[150:157], v158\n"
    "v_exp_f32 v128, v128\n"
    "v_exp_f32 v129, v129\n"
    "v_exp_f32 v130, v130\n"
    "v_mfma_f32_32x32x16_bf16 v[142:157], v[10:13], v[56:59], 0\n"
    "v_exp_f32 v131, v131\n"
    "v_exp_f32 v132, v132\n"
    "v_exp_f32 v133, v133\n"
    "v_mfma_f32_32x32x8_bf16 v[142:157], v[14:15], v[60:61], v[142:157]\n"
    "ds_read_b128 v[34:37], v5 offset:8320\n"
    "global_load_lds_dwordx4 v2, s[36:37] offset:2048\n"
    "v_exp_f32 v134, v134\n"
    "v_exp_f32 v135, v135\n"
    "v_exp_f32 v136, v136\n"
    "v_exp_f32 v137, v137\n"
    "v_smfmac_f32_32x32x32_bf16 v[94:109], v[16:19], v[126:133], v158\n"
    "global_load_dwordx4 a[40:43], v2, s[38:39] offset:2048\n"
    "v_exp_f32 v138, v138\n"
    "v_exp_f32 v139, v139\n"
    "v_exp_f32 v140, v140\n"
    "v_exp_f32 v141, v141\n"
    "v_exp_f32 v142, v142\n"
    "v_exp_f32 v143, v143\n"
    "v_smfmac_f32_32x32x32_bf16 v[94:109], v[20:23], v[134:141], v158\n"
    "v_exp_f32 v144, v144\n"
    "v_exp_f32 v145, v145\n"
    "v_exp_f32 v146, v146\n"
    "s_waitcnt lgkmcnt(0)\n"
    "v_mfma_f32_32x32x16_bf16 v[126:141], v[24:27], v[38:41], 0\n"
    "s_add_u32 s90, s68, 1\n"
    "s_and_b32 s90, s90, 7\n"
    "v_exp_f32 v147, v147\n"
    "v_exp_f32 v148, v148\n"
    "v_exp_f32 v149, v149\n"
    "v_mfma_f32_32x32x8_bf16 v[126:141], v[28:29], v[42:43], v[126:141]\n"
    "s_lshl_b32 s90, s90, 16\n"
    "v_exp_f32 v150, v150\n"
    "v_exp_f32 v151, v151\n"
    "v_exp_f32 v152, v152\n"
    "v_exp_f32 v153, v153\n"
    "v_smfmac_f32_32x32x32_bf16 v[110:125], v[16:19], v[142:149], v158\n"
    "s_add_u32 s94, s44, s90\n"
    "s_addc_u32 s95, s45, 0\n"
    "global_store_dwordx4 v2, v[160:163], s[94:95] offset:0 sc1\n"
    "v_exp_f32 v154, v154\n"
    "v_exp_f32 v155, v155\n"
    "v_exp_f32 v156, v156\n"
    "v_exp_f32 v157, v157\n"
    "v_exp_f32 v126, v126\n"
    "v_exp_f32 v127, v127\n"
    "v_smfmac_f32_32x32x32_bf16 v[110:125], v[20:23], v[150:157], v158\n"
    "v_exp_f32 v128, v128\n"
    "v_exp_f32 v129, v129\n"
    "v_exp_f32 v130, v130\n"
    "v_mfma_f32_32x32x16_bf16 v[142:157], v[24:27], v[44:47], 0\n"
    "v_exp_f32 v131, v131\n"
    "v_exp_f32 v132, v132\n"
    "v_exp_f32 v133, v133\n"
    "v_mfma_f32_32x32x8_bf16 v[142:157], v[28:29], v[48:49], v[142:157]\n"
    "ds_read_b128 v[10:13], v3 offset:10240\n"
    "ds_read_b64 v[14:15], v4 offset:10240\n"
    "v_exp_f32 v134, v134\n"
    "v_exp_f32 v135, v135\n"
    "v_exp_f32 v136, v136\n"
    "v_exp_f32 v137, v137\n"
    "v_smfmac_f32_32x32x32_bf16 v[62:77], v[30:33], v[126:133], v158\n"
    "global_load_lds_dwordx4 v2, s[36:37] offset:3072\n"
    "global_load_dwordx4 a[44:47], v2, s[38:39] offset:3072\n"
    "v_exp_f32 v138, v138\n"
    "v_exp_f32 v139, v139\n"
    "v_exp_f32 v140, v140\n"
    "v_exp_f32 v141, v141\n"
    "v_exp_f32 v142, v142\n"
    "v_exp_f32 v143, v143\n"
    "v_smfmac_f32_32x32x32_bf16 v[62:77], v[34:37], v[134:141], v158\n"
    "ds_read_b128 v[16:19], v5 offset:10240\n"
    "s_add_u32 m0, m0, 8192\n"
    "v_exp_f32 v144, v144\n"
    "v_exp_f32 v145, v145\n"
    "v_exp_f32 v146, v146\n"
    "v_mfma_f32_32x32x16_bf16 v[126:141], v[24:27], v[50:53], 0\n"
    "s_add_u32 s36, s36, 8192\n"
    "s_addc_u32 s37, s37, 0\n"
    "v_exp_f32 v147, v147\n"
    "v_exp_f32 v148, v148\n"
    "v_exp_f32 v149, v149\n"
    "v_mfma_f32_32x32x8_bf16 v[126:141], v[28:29], v[54:55], v[126:141]\n"
    "global_load_lds_dwordx4 v2, s[36:37] offset:0\n"
    "v_exp_f32 v150, v150\n"
    "v_exp_f32 v151, v151\n"
    "v_exp_f32 v152, v152\n"
    "v_exp_f32 v153, v153\n"
    "v_smfmac_f32_32x32x32_bf16 v[78:93], v[30:33], v[142:149], v158\n"
    "s_add_u32 s38, s38, 8192\n"
    "s_addc_u32 s39, s39, 0\n"
    "global_load_dwordx4 a[48:51], v2, s[38:39] offset:-4096\n"
    "v_exp_f32 v154, v154\n"
    "v_exp_f32 v155, v155\n"
    "v_exp_f32 v156, v156\n"
    "v_exp_f32 v157, v157\n"
    "v_exp_f32 v126, v126\n"
    "v_exp_f32 v127, v127\n"
    "v_smfmac_f32_32x32x32_bf16 v[78:93], v[34:37], v[150:157], v158\n"
    "v_exp_f32 v128, v128\n"
    "v_exp_f32 v129, v129\n"
    "v_exp_f32 v130, v130\n"
    "v_mfma_f32_32x32x16_bf16 v[142:157], v[24:27], v[56:59], 0\n"
    "v_exp_f32 v131, v131\n"
    "v_exp_f32 v132, v132\n"
    "v_exp_f32 v133, v133\n"
    "v_mfma_f32_32x32x8_bf16 v[142:157], v[28:29], v[60:61], v[142:157]\n"
    "ds_read_b128 v[20:23], v5 offset:10880\n"
    "global_load_lds_dwordx4 v2, s[36:37] offset:1024\n"
    "v_exp_f32 v134, v134\n"
    "v_exp_f32 v135, v135\n"
    "v_exp_f32 v136, v136\n"
    "v_exp_f32 v137, v137\n"
    "v_smfmac_f32_32x32x32_bf16 v[94:109], v[30:33], v[126:133], v158\n"
    "global_load_dwordx4 a[52:55], v2, s[38:39] offset:-3072\n"
    "v_exp_f32 v138, v138\n"
    "v_exp_f32 v139, v139\n"
    "v_exp_f32 v140, v140\n"
    "v_exp_f32 v141, v141\n"
    "v_exp_f32 v142, v142\n"
    "v_exp_f32 v143, v143\n"
    "v_smfmac_f32_32x32x32_bf16 v[94:109], v[34:37], v[134:141], v158\n"
    "v_exp_f32 v144, v144\n"
    "v_exp_f32 v145, v145\n"
    "v_exp_f32 v146, v146\n"
    "s_waitcnt lgkmcnt(0)\n"
    "v_mfma_f32_32x32x16_bf16 v[126:141], v[10:13], v[38:41], 0\n"
    "v_exp_f32 v147, v147\n"
    "v_exp_f32 v148, v148\n"
    "v_exp_f32 v149, v149\n"
    "v_mfma_f32_32x32x8_bf16 v[126:141], v[14:15], v[42:43], v[126:141]\n"
    "global_store_dwordx4 v2, v[160:163], s[94:95] offset:1024 sc1\n"
    "v_exp_f32 v150, v150\n"
    "v_exp_f32 v151, v151\n"
    "v_exp_f32 v152, v152\n"
    "v_exp_f32 v153, v153\n"
    "v_smfmac_f32_32x32x32_bf16 v[110:125], v[30:33], v[142:149], v158\n"
    "s_nop 1\n"
    "v_exp_f32 v154, v154\n"
    "v_exp_f32 v155, v155\n"
    "v_exp_f32 v156, v156\n"
    "v_exp_f32 v157, v157\n"
    "v_exp_f32 v126, v126\n"
    "v_exp_f32 v127, v127\n"
    "v_smfmac_f32_32x32x32_bf16 v[110:125], v[34:37], v[150:157], v158\n"
    "v_exp_f32 v128, v128\n"
    "v_exp_f32 v129, v129\n"
    "v_exp_f32 v130, v130\n"
    "v_mfma_f32_32x32x16_bf16 v[142:157], v[10:13], v[44:47], 0\n"
    "v_exp_f32 v131, v131\n"
    "v_exp_f32 v132, v132\n"
    "v_exp_f32 v133, v133\n"
    "v_mfma_f32_32x32x8_bf16 v[142:157], v[14:15], v[48:49], v[142:157]\n"
    "ds_read_b128 v[24:27], v3 offset:12800\n"
    "ds_read_b64 v[28:29], v4 offset:12800\n"
    "v_exp_f32 v134, v134\n"
    "v_exp_f32 v135, v135\n"
    "v_exp_f32 v136, v136\n"
    "v_exp_f32 v137, v137\n"
    "v_smfmac_f32_32x32x32_bf16 v[62:77], v[16:19], v[126:133], v158\n"
    "global_load_lds_dwordx4 v2, s[36:37] offset:2048\n"
    "global_load_dwordx4 a[56:59], v2, s[38:39] offset:-2048\n"
    "v_exp_f32 v138, v138\n"
    "v_exp_f32 v139, v139\n"
    "v_exp_f32 v140, v140\n"
    "v_exp_f32 v141, v141\n"
    "v_exp_f32 v142, v142\n"
    "v_exp_f32 v143, v143\n"
    "v_smfmac_f32_32x32x32_bf16 v[62:77], v[20:23], v[134:141], v158\n"
    "v_exp_f32 v144, v144\n"
    "v_exp_f32 v145, v145\n"
    "v_exp_f32 v146, v146\n"
    "v_mfma_f32_32x32x16_bf16 v[126:141], v[10:13], v[50:53], 0\n"
    "v_exp_f32 v147, v147\n"
    "v_exp_f32 v148, v148\n"
    "v_exp_f32 v149, v149\n"
    "v_mfma_f32_32x32x8_bf16 v[126:141], v[14:15], v[54:55], v[126:141]\n"
    "ds_read_b128 v[30:33], v5 offset:12800\n"
    "global_load_lds_dwordx4 v2, s[36:37] offset:3072\n"
    "v_exp_f32 v150, v150\n"
    "v_exp_f32 v151, v151\n"
    "v_exp_f32 v152, v152\n"
    "v_exp_f32 v153, v153\n"
    "v_smfmac_f32_32x32x32_bf16 v[78:93], v[16:19], v[142:149], v158\n"
    "global_load_dwordx4 a[60:63], v2, s[38:39] offset:-1024\n"
    "v_exp_f32 v154, v154\n"
    "v_exp_f32 v155, v155\n"
    "v_exp_f32 v156, v156\n"
    "v_exp_f32 v157, v157\n"
    "v_exp_f32 v126, v126\n"
    "v_exp_f32 v127, v127\n"
    "v_smfmac_f32_32x32x32_bf16 v[78:93], v[20:23], v[150:157], v158\n"
    "v_exp_f32 v128, v128\n"
    "v_exp_f32 v129, v129\n"
    "v_exp_f32 v130, v130\n"
    "v_mfma_f32_32x32x16_bf16 v[142:157], v[10:13], v[56:59], 0\n"
    "ds_read_b128 v[34:37], v5 offset:13440\n"
    "s_add_u32 m0, m0, 8192\n"
    "v_exp_f32 v131, v131\n"
    "v_exp_f32 v132, v132\n"
    "v_exp_f32 v133, v133\n"
    "v_mfma_f32_32x32x8_bf16 v[142:157], v[14:15], v[60:61], v[142:157]\n"
    "s_add_u32 s36, s36, 8192\n"
    "s_addc_u32 s37, s37, 0\n"
    "v_exp_f32 v134, v134\n"
    "v_exp_f32 v135, v135\n"
    "v_exp_f32 v136, v136\n"
    "v_exp_f32 v137, v137\n"
    "v_smfmac_f32_32x32x32_bf16 v[94:109], v[16:19], v[126:133], v158\n"
    "global_load_lds_dwordx4 v2, s[36:37] offset:0\n"
    "global_load_dwordx4 a[64:67], v2, s[38:39] offset:0\n"
    "v_exp_f32 v138, v138\n"
    "v_exp_f32 v139, v139\n"
    "v_exp_f32 v140, v140\n"
    "v_exp_f32 v141, v141\n"
    "v_exp_f32 v142, v142\n"
    "v_exp_f32 v143, v143\n"
    "v_smfmac_f32_32x32x32_bf16 v[94:109], v[20:23], v[134:141], v158\n"
    "v_exp_f32 v144, v144\n"
    "v_exp_f32 v145, v145\n"
    "v_exp_f32 v146, v146\n"
    "s_waitcnt lgkmcnt(0)\n"
    "v_mfma_f32_32x32x16_bf16 v[126:141], v[24:27], v[38:41], 0\n"
    "s_add_u32 s90, s68, 2\n"
    "s_and_b32 s90, s90, 7\n"
    "v_exp_f32 v147, v147\n"
    "v_exp_f32 v148, v148\n"
    "v_exp_f32 v149, v149\n"
    "v_mfma_f32_32x32x8_bf16 v[126:141], v[28:29], v[42:43], v[126:141]\n"
    "s_lshl_b32 s90, s90, 16\n"
    "v_exp_f32 v150, v150\n"
    "v_exp_f32 v151, v151\n"
    "v_exp_f32 v152, v152\n"
    "v_exp_f32 v153, v153\n"
    "v_smfmac_f32_32x32x32_bf16 v[110:125], v[16:19], v[142:149], v158\n"
    "s_add_u32 s94, s44, s90\n"
    "s_addc_u32 s95, s45, 0\n"
    "global_store_dwordx4 v2, v[160:163], s[94:95] offset:0 sc1\n"
    "v_exp_f32 v154, v154\n"
    "v_exp_f32 v155, v155\n"
    "v_exp_f32 v156, v156\n"
    "v_exp_f32 v157, v157\n"
    "v_exp_f32 v126, v126\n"
    "v_exp_f32 v127, v127\n"
    "v_smfmac_f32_32x32x32_bf16 v[110:125], v[20:23], v[150:157], v158\n"
    "v_exp_f32 v128, v128\n"
    "v_exp_f32 v129, v129\n"
    "v_exp_f32 v130, v130\n"
    "v_mfma_f32_32x32x16_bf16 v[142:157], v[24:27], v[44:47], 0\n"
    "v_exp_f32 v131, v131\n"
    "v_exp_f32 v132, v132\n"
    "v_exp_f32 v133, v133\n"
    "v_mfma_f32_32x32x8_bf16 v[142:157], v[28:29], v[48:49], v[142:157]\n"
    "ds_read_b128 v[10:13], v3 offset:15360\n"
    "ds_read_b64 v[14:15], v4 offset:15360\n"
    "v_exp_f32 v134, v134\n"
    "v_exp_f32 v135, v135\n"
    "v_exp_f32 v136, v136\n"
    "v_exp_f32 v137, v137\n"
    "v_smfmac_f32_32x32x32_bf16 v[62:77], v[30:33], v[126:133], v158\n"
    "global_load_lds_dwordx4 v2, s[36:37] offset:1024\n"
    "global_load_dwordx4 a[68:71], v2, s[38:39] offset:1024\n"
    "v_exp_f32 v138, v138\n"
    "v_exp_f32 v139, v139\n"
    "v_exp_f32 v140, v140\n"
    "v_exp_f32 v141, v141\n"
    "v_exp_f32 v142, v142\n"
    "v_exp_f32 v143, v143\n"
    "v_smfmac_f32_32x32x32_bf16 v[62:77], v[34:37], v[134:141], v158\n"
    "v_exp_f32 v144, v144\n"
    "v_exp_f32 v145, v145\n"
    "v_exp_f32 v146, v146\n"
    "v_mfma_f32_32x32x16_bf16 v[126:141], v[24:27], v[50:53], 0\n"
    "v_exp_f32 v147, v147\n"
    "v_exp_f32 v148, v148\n"
    "v_exp_f32 v149, v149\n"
    "v_mfma_f32_32x32x8_bf16 v[126:141], v[28:29], v[54:55], v[126:141]\n"
    "ds_read_b128 v[16:19], v5 offset:15360\n"
    "global_load_lds_dwordx4 v2, s[36:37] offset:2048\n"
    "v_exp_f32 v150, v150\n"
    "v_exp_f32 v151, v151\n"
    "v_exp_f32 v152, v152\n"
    "v_exp_f32 v153, v153\n"
    "v_smfmac_f32_32x32x32_bf16 v[78:93], v[30:33], v[142:149], v158\n"
    "global_load_dwordx4 a[72:75], v2, s[38:39] offset:2048\n"
    "v_exp_f32 v154, v154\n"
    "v_exp_f32 v155, v155\n"
    "v_exp_f32 v156, v156\n"
    "v_exp_f32 v157, v157\n"
    "v_exp_f32 v126, v126\n"
    "v_exp_f32 v127, v127\n"
    "v_smfmac_f32_32x32x32_bf16 v[78:93], v[34:37], v[150:157], v158\n"
    "v_exp_f32 v128, v128\n"
    "v_exp_f32 v129, v129\n"
    "v_exp_f32 v130, v130\n"
    "v_mfma_f32_32x32x16_bf16 v[142:157], v[24:27], v[56:59], 0\n"
    "v_exp_f32 v131, v131\n"
    "v_exp_f32 v132, v132\n"
    "v_exp_f32 v133, v133\n"
    "v_mfma_f32_32x32x8_bf16 v[142:157], v[28:29], v[60:61], v[142:157]\n"
    "ds_read_b128 v[20:23], v5 offset:16000\n"
    "global_load_lds_dwordx4 v2, s[36:37] offset:3072\n"
    "v_exp_f32 v134, v134\n"
    "v_exp_f32 v135, v135\n"
    "v_exp_f32 v136, v136\n"
    "v_exp_f32 v137, v137\n"
    "v_smfmac_f32_32x32x32_bf16 v[94:109], v[30:33], v[126:133], v158\n"
    "global_load_dwordx4 a[76:79], v2, s[38:39] offset:3072\n"
    "v_exp_f32 v138, v138\n"
    "v_exp_f32 v139, v139\n"
    "v_exp_f32 v140, v140\n"
    "v_exp_f32 v141, v141\n"
    "v_exp_f32 v142, v142\n"
    "v_exp_f32 v143, v143\n"
    "v_smfmac_f32_32x32x32_bf16 v[94:109], v[34:37], v[134:141], v158\n"
    "v_exp_f32 v144, v144\n"
    "v_exp_f32 v145, v145\n"
    "v_exp_f32 v146, v146\n"
    "s_waitcnt lgkmcnt(0)\n"
    "v_mfma_f32_32x32x16_bf16 v[126:141], v[10:13], v[38:41], 0\n"
    "v_exp_f32 v147, v147\n"
    "v_exp_f32 v148, v148\n"
    "v_exp_f32 v149, v149\n"
    "v_mfma_f32_32x32x8_bf16 v[126:141], v[14:15], v[42:43], v[126:141]\n"
    "global_store_dwordx4 v2, v[160:163], s[94:95] offset:1024 sc1\n"
    "v_exp_f32 v150, v150\n"
    "v_exp_f32 v151, v151\n"
    "v_exp_f32 v152, v152\n"
    "v_exp_f32 v153, v153\n"
    "v_smfmac_f32_32x32x32_bf16 v[110:125], v[30:33], v[142:149], v158\n"
    "s_nop 1\n"
    "v_exp_f32 v154, v154\n"
    "v_exp_f32 v155, v155\n"
    "v_exp_f32 v156, v156\n"
    "v_exp_f32 v157, v157\n"
    "v_exp_f32 v126, v126\n"
    "v_exp_f32 v127, v127\n"
    "v_smfmac_f32_32x32x32_bf16 v[110:125], v[34:37], v[150:157], v158\n"
    "v_exp_f32 v128, v128\n"
    "v_exp_f32 v129, v129\n"
    "v_exp_f32 v130, v130\n"
    "v_mfma_f32_32x32x16_bf16 v[142:157], v[10:13], v[44:47], 0\n"
    "ds_read_b128 v[24:27], v3 offset:17920\n"
    "ds_read_b64 v[28:29], v4 offset:17920\n"
    "s_add_u32 m0, m0, 8192\n"
    "v_exp_f32 v131, v131\n"
    "v_exp_f32 v132, v132\n"
    "v_exp_f32 v133, v133\n"
    "v_mfma_f32_32x32x8_bf16 v[142:157], v[14:15], v[48:49], v[142:157]\n"
    "s_add_u32 s36, s36, 8192\n"
    "s_addc_u32 s37, s37, 0\n"
    "global_load_lds_dwordx4 v2, s[36:37] offset:0\n"
    "v_exp_f32 v134, v134\n"
    "v_exp_f32 v135, v135\n"
    "v_exp_f32 v136, v136\n"
    "v_exp_f32 v137, v137\n"
    "v_smfmac_f32_32x32x32_bf16 v[62:77], v[16:19], v[126:133], v158\n"
    "s_add_u32 s38, s38, 8192\n"
    "s_addc_u32 s39, s39, 0\n"
    "global_load_dwordx4 a[80:83], v2, s[38:39] offset:-4096\n"
    "v_exp_f32 v138, v138\n"
    "v_exp_f32 v139, v139\n"
    "v_exp_f32 v140, v140\n"
    "v_exp_f32 v141, v141\n"
    "v_exp_f32 v142, v142\n"
    "v_exp_f32 v143, v143\n"
    "v_smfmac_f32_32x32x32_bf16 v[62:77], v[20:23], v[134:141], v158\n"
    "v_exp_f32 v144, v144\n"
    "v_exp_f32 v145, v145\n"
    "v_exp_f32 v146, v146\n"
    "v_mfma_f32_32x32x16_bf16 v[126:141], v[10:13], v[50:53], 0\n"
    "v_exp_f32 v147, v147\n"
    "v_exp_f32 v148, v148\n"
    "v_exp_f32 v149, v149\n"
    "v_mfma_f32_32x32x8_bf16 v[126:141], v[14:15], v[54:55], v[126:141]\n"
    "ds_read_b128 v[30:33], v5 offset:17920\n"
    "global_load_lds_dwordx4 v2, s[36:37] offset:1024\n"
    "v_exp_f32 v150, v150\n"
    "v_exp_f32 v151, v151\n"
    "v_exp_f32 v152, v152\n"
    "v_exp_f32 v153, v153\n"
    "v_smfmac_f32_32x32x32_bf16 v[78:93], v[16:19], v[142:149], v158\n"
    "global_load_dwordx4 a[84:87], v2, s[38:39] offset:-3072\n"
    "v_exp_f32 v154, v154\n"
    "v_exp_f32 v155, v155\n"
    "v_exp_f32 v156, v156\n"
    "v_exp_f32 v157, v157\n"
    "v_exp_f32 v126, v126\n"
    "v_exp_f32 v127, v127\n"
    "v_smfmac_f32_32x32x32_bf16 v[78:93], v[20:23], v[150:157], v158\n"
    "v_exp_f32 v128, v128\n"
    "v_exp_f32 v129, v129\n"
    "v_exp_f32 v130, v130\n"
    "v_mfma_f32_32x32x16_bf16 v[142:157], v[10:13], v[56:59], 0\n"
    "v_exp_f32 v131, v131\n"
    "v_exp_f32 v132, v132\n"
    "v_exp_f32 v133, v133\n"
    "v_mfma_f32_32x32x8_bf16 v[142:157], v[14:15], v[60:61], v[142:157]\n"
    "ds_read_b128 v[34:37], v5 offset:18560\n"
    "global_load_lds_dwordx4 v2, s[36:37] offset:2048\n"
    "v_exp_f32 v134, v134\n"
    "v_exp_f32 v135, v135\n"
    "v_exp_f32 v136, v136\n"
    "v_exp_f32 v137, v137\n"
    "v_smfmac_f32_32x32x32_bf16 v[94:109], v[16:19], v[126:133], v158\n"
    "global_load_dwordx4 a[88:91], v2, s[38:39] offset:-2048\n"
    "v_exp_f32 v138, v138\n"
    "v_exp_f32 v139, v139\n"
    "v_exp_f32 v140, v140\n"
    "v_exp_f32 v141, v141\n"
    "v_exp_f32 v142, v142\n"
    "v_exp_f32 v143, v143\n"
    "v_smfmac_f32_32x32x32_bf16 v[94:109], v[20:23], v[134:141], v158\n"
    "v_exp_f32 v144, v144\n"
    "v_exp_f32 v145, v145\n"
    "v_exp_f32 v146, v146\n"
    "s_waitcnt lgkmcnt(0)\n"
    "v_mfma_f32_32x32x16_bf16 v[126:141], v[24:27], v[38:41], 0\n"
    "s_add_u32 s90, s68, 3\n"
    "s_and_b32 s90, s90, 7\n"
    "v_exp_f32 v147, v147\n"
    "v_exp_f32 v148, v148\n"
    "v_exp_f32 v149, v149\n"
    "v_mfma_f32_32x32x8_bf16 v[126:141], v[28:29], v[42:43], v[126:141]\n"
    "s_lshl_b32 s90, s90, 16\n"
    "v_exp_f32 v150, v150\n"
    "v_exp_f32 v151, v151\n"
    "v_exp_f32 v152, v152\n"
    "v_exp_f32 v153, v153\n"
    "v_smfmac_f32_32x32x32_bf16 v[110:125], v[16:19], v[142:149], v158\n"
    "s_add_u32 s94, s44, s90\n"
    "s_addc_u32 s95, s45, 0\n"
    "global_store_dwordx4 v2, v[160:163], s[94:95] offset:0 sc1\n"
    "v_exp_f32 v154, v154\n"
    "v_exp_f32 v155, v155\n"
    "v_exp_f32 v156, v156\n"
    "v_exp_f32 v157, v157\n"
    "v_exp_f32 v126, v126\n"
    "v_exp_f32 v127, v127\n"
    "v_smfmac_f32_32x32x32_bf16 v[110:125], v[20:23], v[150:157], v158\n"
    "v_exp_f32 v128, v128\n"
    "v_exp_f32 v129, v129\n"
    "v_exp_f32 v130, v130\n"
    "v_mfma_f32_32x32x16_bf16 v[142:157], v[24:27], v[44:47], 0\n"
    "v_exp_f32 v131, v131\n"
    "v_exp_f32 v132, v132\n"
    "v_exp_f32 v133, v133\n"
    "v_mfma_f32_32x32x8_bf16 v[142:157], v[28:29], v[48:49], v[142:157]\n"
    "ds_read_b128 v[10:13], v3 offset:20480\n"
    "ds_read_b64 v[14:15], v4 offset:20480\n"
    "v_exp_f32 v134, v134\n"
    "v_exp_f32 v135, v135\n"
    "v_exp_f32 v136, v136\n"
    "v_exp_f32 v137, v137\n"
    "v_smfmac_f32_32x32x32_bf16 v[62:77], v[30:33], v[126:133], v158\n"
    "global_load_lds_dwordx4 v2, s[36:37] offset:3072\n"
    "global_load_dwordx4 a[92:95], v2, s[38:39] offset:-1024\n"
    "v_exp_f32 v138, v138\n"
    "v_exp_f32 v139, v139\n"
    "v_exp_f32 v140, v140\n"
    "v_exp_f32 v141, v141\n"
    "v_exp_f32 v142, v142\n"
    "v_exp_f32 v143, v143\n"
    "v_smfmac_f32_32x32x32_bf16 v[62:77], v[34:37], v[134:141], v158\n"
    "v_exp_f32 v144, v144\n"
    "v_exp_f32 v145, v145\n"
    "v_exp_f32 v146, v146\n"
    "v_mfma_f32_32x32x16_bf16 v[126:141], v[24:27], v[50:53], 0\n"
    "ds_read_b128 v[16:19], v5 offset:20480\n"
    "s_add_u32 m0, m0, 8192\n"
    "v_exp_f32 v147, v147\n"
    "v_exp_f32 v148, v148\n"
    "v_exp_f32 v149, v149\n"
    "v_mfma_f32_32x32x8_bf16 v[126:141], v[28:29], v[54:55], v[126:141]\n"
    "s_add_u32 s36, s36, 8192\n"
    "s_addc_u32 s37, s37, 0\n"
    "v_exp_f32 v150, v150\n"
    "v_exp_f32 v151, v151\n"
    "v_exp_f32 v152, v152\n"
    "v_exp_f32 v153, v153\n"
    "v_smfmac_f32_32x32x32_bf16 v[78:93], v[30:33], v[142:149], v158\n"
    "global_load_lds_dwordx4 v2, s[36:37] offset:0\n"
    "global_load_dwordx4 a[96:99], v2, s[38:39] offset:0\n"
    "v_exp_f32 v154, v154\n"
    "v_exp_f32 v155, v155\n"
    "v_exp_f32 v156, v156\n"
    "v_exp_f32 v157, v157\n"
    "v_exp_f32 v126, v126\n"
    "v_exp_f32 v127, v127\n"
    "v_smfmac_f32_32x32x32_bf16 v[78:93], v[34:37], v[150:157], v158\n"
    "v_exp_f32 v128, v128\n"
    "v_exp_f32 v129, v129\n"
    "v_exp_f32 v130, v130\n"
    "v_mfma_f32_32x32x16_bf16 v[142:157], v[24:27], v[56:59], 0\n"
    "v_exp_f32 v131, v131\n"
    "v_exp_f32 v132, v132\n"
    "v_exp_f32 v133, v133\n"
    "v_mfma_f32_32x32x8_bf16 v[142:157], v[28:29], v[60:61], v[142:157]\n"
    "ds_read_b128 v[20:23], v5 offset:21120\n"
    "global_load_lds_dwordx4 v2, s[36:37] offset:1024\n"
    "v_exp_f32 v134, v134\n"
    "v_exp_f32 v135, v135\n"
    "v_exp_f32 v136, v136\n"
    "v_exp_f32 v137, v137\n"
    "v_smfmac_f32_32x32x32_bf16 v[94:109], v[30:33], v[126:133], v158\n"
    "global_load_dwordx4 a[100:103], v2, s[38:39] offset:1024\n"
    "v_exp_f32 v138, v138\n"
    "v_exp_f32 v139, v139\n"
    "v_exp_f32 v140, v140\n"
    "v_exp_f32 v141, v141\n"
    "v_exp_f32 v142, v142\n"
    "v_exp_f32 v143, v143\n"
    "v_smfmac_f32_32x32x32_bf16 v[94:109], v[34:37], v[134:141], v158\n"
    "v_exp_f32 v144, v144\n"
    "v_exp_f32 v145, v145\n"
    "v_exp_f32 v146, v146\n"
    "s_waitcnt lgkmcnt(0)\n"
    "v_mfma_f32_32x32x16_bf16 v[126:141], v[10:13], v[38:41], 0\n"
    "v_exp_f32 v147, v147\n"
    "v_exp_f32 v148, v148\n"
    "v_exp_f32 v149, v149\n"
    "v_mfma_f32_32x32x8_bf16 v[126:141], v[14:15], v[42:43], v[126:141]\n"
    "global_store_dwordx4 v2, v[160:163], s[94:95] offset:1024 sc1\n"
    "v_exp_f32 v150, v150\n"
    "v_exp_f32 v151, v151\n"
    "v_exp_f32 v152, v152\n"
    "v_exp_f32 v153, v153\n"
    "v_smfmac_f32_32x32x32_bf16 v[110:125], v[30:33], v[142:149], v158\n"
    "s_nop 1\n"
    "v_exp_f32 v154, v154\n"
    "v_exp_f32 v155, v155\n"
    "v_exp_f32 v156, v156\n"
    "v_exp_f32 v157, v157\n"
    "v_exp_f32 v126, v126\n"
    "v_exp_f32 v127, v127\n"
    "v_smfmac_f32_32x32x32_bf16 v[110:125], v[34:37], v[150:157], v158\n"
    "v_exp_f32 v128, v128\n"
    "v_exp_f32 v129, v129\n"
    "v_exp_f32 v130, v130\n"
    "v_mfma_f32_32x32x16_bf16 v[142:157], v[10:13], v[44:47], 0\n"
    "v_exp_f32 v131, v131\n"
    "v_exp_f32 v132, v132\n"
    "v_exp_f32 v133, v133\n"
    "v_mfma_f32_32x32x8_bf16 v[142:157], v[14:15], v[48:49], v[142:157]\n"
    "ds_read_b128 v[24:27], v3 offset:23040\n"
    "ds_read_b64 v[28:29], v4 offset:23040\n"
    "v_exp_f32 v134, v134\n"
    "v_exp_f32 v135, v135\n"
    "v_exp_f32 v136, v136\n"
    "v_exp_f32 v137, v137\n"
    "v_smfmac_f32_32x32x32_bf16 v[62:77], v[16:19], v[126:133], v158\n"
    "global_load_lds_dwordx4 v2, s[36:37] offset:2048\n"
    "global_load_dwordx4 a[104:107], v2, s[38:39] offset:2048\n"
    "v_exp_f32 v138, v138\n"
    "v_exp_f32 v139, v139\n"
    "v_exp_f32 v140, v140\n"
    "v_exp_f32 v141, v141\n"
    "v_exp_f32 v142, v142\n"
    "v_exp_f32 v143, v143\n"
    "v_smfmac_f32_32x32x32_bf16 v[62:77], v[20:23], v[134:141], v158\n"
    "v_exp_f32 v144, v144\n"
    "v_exp_f32 v145, v145\n"
    "v_exp_f32 v146, v146\n"
    "v_mfma_f32_32x32x16_bf16 v[126:141], v[10:13], v[50:53], 0\n"
    "v_exp_f32 v147, v147\n"
    "v_exp_f32 v148, v148\n"
    "v_exp_f32 v149, v149\n"
    "v_mfma_f32_32x32x8_bf16 v[126:141], v[14:15], v[54:55], v[126:141]\n"
    "ds_read_b128 v[30:33], v5 offset:23040\n"
    "global_load_lds_dwordx4 v2, s[36:37] offset:3072\n"
    "v_exp_f32 v150, v150\n"
    "v_exp_f32 v151, v151\n"
    "v_exp_f32 v152, v152\n"
    "v_exp_f32 v153, v153\n"
    "v_smfmac_f32_32x32x32_bf16 v[78:93], v[16:19], v[142:149], v158\n"
    "global_load_dwordx4 a[108:111], v2, s[38:39] offset:3072\n"
    "v_exp_f32 v154, v154\n"
    "v_exp_f32 v155, v155\n"
    "v_exp_f32 v156, v156\n"
    "v_exp_f32 v157, v157\n"
    "v_exp_f32 v126, v126\n"
    "v_exp_f32 v127, v127\n"
    "v_smfmac_f32_32x32x32_bf16 v[78:93], v[20:23], v[150:157], v158\n"
    "ds_read_b128 v[34:37], v5 offset:23680\n"
    "s_add_u32 m0, m0, 8192\n"
    "v_exp_f32 v128, v128\n"
    "v_exp_f32 v129, v129\n"
    "v_exp_f32 v130, v130\n"
    "v_mfma_f32_32x32x16_bf16 v[142:157], v[10:13], v[56:59], 0\n"
    "s_add_u32 s36, s36, 8192\n"
    "s_addc_u32 s37, s37, 0\n"
    "v_exp_f32 v131, v131\n"
    "v_exp_f32 v132, v132\n"
    "v_exp_f32 v133, v133\n"
    "v_mfma_f32_32x32x8_bf16 v[142:157], v[14:15], v[60:61], v[142:157]\n"
    "global_load_lds_dwordx4 v2, s[36:37] offset:0\n"
    "v_exp_f32 v134, v134\n"
    "v_exp_f32 v135, v135\n"
    "v_exp_f32 v136, v136\n"
    "v_exp_f32 v137, v137\n"
    "v_smfmac_f32_32x32x32_bf16 v[94:109], v[16:19], v[126:133], v158\n"
    "s_add_u32 s38, s38, 8192\n"
    "s_addc_u32 s39, s39, 0\n"
    "global_load_dwordx4 a[112:115], v2, s[38:39] offset:-4096\n"
    "v_exp_f32 v138, v138\n"
    "v_exp_f32 v139, v139\n"
    "v_exp_f32 v140, v140\n"
    "v_exp_f32 v141, v141\n"
    "v_exp_f32 v142, v142\n"
    "v_exp_f32 v143, v143\n"
    "v_smfmac_f32_32x32x32_bf16 v[94:109], v[20:23], v[134:141], v158\n"
    "v_exp_f32 v144, v144\n"
    "v_exp_f32 v145, v145\n"
    "v_exp_f32 v146, v146\n"
    "s_waitcnt lgkmcnt(0)\n"
    "v_mfma_f32_32x32x16_bf16 v[126:141], v[24:27], v[38:41], 0\n"
    "s_add_u32 s90, s68, 4\n"
    "s_and_b32 s90, s90, 7\n"
    "v_exp_f32 v147, v147\n"
    "v_exp_f32 v148, v148\n"
    "v_exp_f32 v149, v149\n"
    "v_mfma_f32_32x32x8_bf16 v[126:141], v[28:29], v[42:43], v[126:141]\n"
    "s_lshl_b32 s90, s90, 16\n"
    "v_exp_f32 v150, v150\n"
    "v_exp_f32 v151, v151\n"
    "v_exp_f32 v152, v152\n"
    "v_exp_f32 v153, v153\n"
    "v_smfmac_f32_32x32x32_bf16 v[110:125], v[16:19], v[142:149], v158\n"
    "s_add_u32 s94, s44, s90\n"
    "s_addc_u32 s95, s45, 0\n"
    "global_store_dwordx4 v2, v[160:163], s[94:95] offset:0 sc1\n"
    "v_exp_f32 v154, v154\n"
    "v_exp_f32 v155, v155\n"
    "v_exp_f32 v156, v156\n"
    "v_exp_f32 v157, v157\n"
    "v_exp_f32 v126, v126\n"
    "v_exp_f32 v127, v127\n"
    "v_smfmac_f32_32x32x32_bf16 v[110:125], v[20:23], v[150:157], v158\n"
    "v_exp_f32 v128, v128\n"
    "v_exp_f32 v129, v129\n"
    "v_exp_f32 v130, v130\n"
    "v_mfma_f32_32x32x16_bf16 v[142:157], v[24:27], v[44:47], 0\n"
    "v_exp_f32 v131, v131\n"
    "v_exp_f32 v132, v132\n"
    "v_exp_f32 v133, v133\n"
    "v_mfma_f32_32x32x8_bf16 v[142:157], v[28:29], v[48:49], v[142:157]\n"
    "ds_read_b128 v[10:13], v3 offset:25600\n"
    "ds_read_b64 v[14:15], v4 offset:25600\n"
    "v_exp_f32 v134, v134\n"
    "v_exp_f32 v135, v135\n"
    "v_exp_f32 v136, v136\n"
    "v_exp_f32 v137, v137\n"
    "v_smfmac_f32_32x32x32_bf16 v[62:77], v[30:33], v[126:133], v158\n"
    "global_load_lds_dwordx4 v2, s[36:37] offset:1024\n"
    "global_load_dwordx4 a[116:119], v2, s[38:39] offset:-3072\n"
    "v_exp_f32 v138, v138\n"
    "v_exp_f32 v139, v139\n"
    "v_exp_f32 v140, v140\n"
    "v_exp_f32 v141, v141\n"
    "v_exp_f32 v142, v142\n"
    "v_exp_f32 v143, v143\n"
    "v_smfmac_f32_32x32x32_bf16 v[62:77], v[34:37], v[134:141], v158\n"
    "v_exp_f32 v144, v144\n"
    "v_exp_f32 v145, v145\n"
    "v_exp_f32 v146, v146\n"
    "v_mfma_f32_32x32x16_bf16 v[126:141], v[24:27], v[50:53], 0\n"
    "v_exp_f32 v147, v147\n"
    "v_exp_f32 v148, v148\n"
    "v_exp_f32 v149, v149\n"
    "v_mfma_f32_32x32x8_bf16 v[126:141], v[28:29], v[54:55], v[126:141]\n"
    "ds_read_b128 v[16:19], v5 offset:25600\n"
    "global_load_lds_dwordx4 v2, s[36:37] offset:2048\n"
    "v_exp_f32 v150, v150\n"
    "v_exp_f32 v151, v151\n"
    "v_exp_f32 v152, v152\n"
    "v_exp_f32 v153, v153\n"
    "v_smfmac_f32_32x32x32_bf16 v[78:93], v[30:33], v[142:149], v158\n"
    "global_load_dwordx4 a[120:123], v2, s[38:39] offset:-2048\n"
    "v_exp_f32 v154, v154\n"
    "v_exp_f32 v155, v155\n"
    "v_exp_f32 v156, v156\n"
    "v_exp_f32 v157, v157\n"
    "v_exp_f32 v126, v126\n"
    "v_exp_f32 v127, v127\n"
    "v_smfmac_f32_32x32x32_bf16 v[78:93], v[34:37], v[150:157], v158\n"
    "v_exp_f32 v128, v128\n"
    "v_exp_f32 v129, v129\n"
    "v_exp_f32 v130, v130\n"
    "v_mfma_f32_32x32x16_bf16 v[142:157], v[24:27], v[56:59], 0\n"
    "v_exp_f32 v131, v131\n"
    "v_exp_f32 v132, v132\n"
    "v_exp_f32 v133, v133\n"
    "v_mfma_f32_32x32x8_bf16 v[142:157], v[28:29], v[60:61], v[142:157]\n"
    "ds_read_b128 v[20:23], v5 offset:26240\n"
    "global_load_lds_dwordx4 v2, s[36:37] offset:3072\n"
    "v_exp_f32 v134, v134\n"
    "v_exp_f32 v135, v135\n"
    "v_exp_f32 v136, v136\n"
    "v_exp_f32 v137, v137\n"
    "v_smfmac_f32_32x32x32_bf16 v[94:109], v[30:33], v[126:133], v158\n"
    "global_load_dwordx4 a[124:127], v2, s[38:39] offset:-1024\n"
    "v_exp_f32 v138, v138\n"
    "v_exp_f32 v139, v139\n"
    "v_exp_f32 v140, v140\n"
    "v_exp_f32 v141, v141\n"
    "v_exp_f32 v142, v142\n"
    "v_exp_f32 v143, v143\n"
    "v_smfmac_f32_32x32x32_bf16 v[94:109], v[34:37], v[134:141], v158\n"
    "v_exp_f32 v144, v144\n"
    "v_exp_f32 v145, v145\n"
    "v_exp_f32 v146, v146\n"
    "s_waitcnt lgkmcnt(0)\n"
    "v_mfma_f32_32x32x16_bf16 v[126:141], v[10:13], v[38:41], 0\n"
    "v_exp_f32 v147, v147\n"
    "v_exp_f32 v148, v148\n"
    "v_exp_f32 v149, v149\n"
    "v_mfma_f32_32x32x8_bf16 v[126:141], v[14:15], v[42:43], v[126:141]\n"
    "global_store_dwordx4 v2, v[160:163], s[94:95] offset:1024 sc1\n"
    "v_exp_f32 v150, v150\n"
    "v_exp_f32 v151, v151\n"
    "v_exp_f32 v152, v152\n"
    "v_exp_f32 v153, v153\n"
    "v_smfmac_f32_32x32x32_bf16 v[110:125], v[30:33], v[142:149], v158\n"
    "s_nop 1\n"
    "v_exp_f32 v154, v154\n"
    "v_exp_f32 v155, v155\n"
    "v_exp_f32 v156, v156\n"
    "v_exp_f32 v157, v157\n"
    "s_waitcnt vmcnt(20)\n"
    "s_barrier\n"
    "v_exp_f32 v126, v126\n"
    "v_exp_f32 v127, v127\n"
    "v_smfmac_f32_32x32x32_bf16 v[110:125], v[34:37], v[150:157], v158\n"
    "ds_read_b128 v[24:27], v3 offset:28160\n"
    "ds_read_b64 v[28:29], v4 offset:28160\n"
    "v_exp_f32 v128, v128\n"
    "v_exp_f32 v129, v129\n"
    "v_exp_f32 v130, v130\n"
    "v_mfma_f32_32x32x16_bf16 v[142:157], v[10:13], v[44:47], 0\n"
    "s_cmp_lt_u32 s69, 3\n"
    "s_cbranch_scc1 102f\n"
    "s_bitcmp1_b32 s70, 2\n"
    "s_cselect_b32 s85, 1, 8\n"
    "s_cselect_b64 vcc, -1, 0\n"
    "s_mul_i32 s85, s85, 9216\n"
    "v_readfirstlane_b32 s84, v6\n"
    "v_cndmask_b32 v159, v7, v8, vcc\n"
    "s_add_u32 m0, s84, 8192\n"
    "s_nop 0\n"
    "global_load_lds_dwordx4 v159, s[54:55] nt\n"
    "102:\n"
    "v_exp_f32 v131, v131\n"
    "v_exp_f32 v132, v132\n"
    "v_exp_f32 v133, v133\n"
    "v_mfma_f32_32x32x8_bf16 v[142:157], v[14:15], v[48:49], v[142:157]\n"
    "v_exp_f32 v134, v134\n"
    "v_exp_f32 v135, v135\n"
    "v_exp_f32 v136, v136\n"
    "v_exp_f32 v137, v137\n"
    "v_smfmac_f32_32x32x32_bf16 v[62:77], v[16:19], v[126:133], v158\n"
    "s_nop 2\n"
    "v_exp_f32 v138, v138\n"
    "v_exp_f32 v139, v139\n"
    "v_exp_f32 v140, v140\n"
    "v_exp_f32 v141, v141\n"
    "v_exp_f32 v142, v142\n"
    "v_exp_f32 v143, v143\n"
    "v_smfmac_f32_32x32x32_bf16 v[62:77], v[20:23], v[134:141], v158\n"
    "ds_read_b128 v[30:33], v5 offset:28160\n"
    "v_exp_f32 v144, v144\n"
    "v_exp_f32 v145, v145\n"
    "v_exp_f32 v146, v146\n"
    "v_mfma_f32_32x32x16_bf16 v[126:141], v[10:13], v[50:53], 0\n"
    "s_cmp_lt_u32 s69, 3\n"
    "s_cbranch_scc1 104f\n"
    "s_add_u32 m0, m0, 1024\n"
    "s_add_u32 s54, s54, s85\n"
    "s_addc_u32 s55, s55, 0\n"
    "global_load_lds_dwordx4 v159, s[54:55] nt\n"
    "104:\n"
    "v_exp_f32 v147, v147\n"
    "v_exp_f32 v148, v148\n"
    "v_exp_f32 v149, v149\n"
    "v_mfma_f32_32x32x8_bf16 v[126:141], v[14:15], v[54:55], v[126:141]\n"
    "v_exp_f32 v150, v150\n"
    "v_exp_f32 v151, v151\n"
    "v_exp_f32 v152, v152\n"
    "v_exp_f32 v153, v153\n"
    "v_smfmac_f32_32x32x32_bf16 v[78:93], v[16:19], v[142:149], v158\n"
    "s_nop 2\n"
    "v_exp_f32 v154, v154\n"
    "v_exp_f32 v155, v155\n"
    "v_exp_f32 v156, v156\n"
    "v_exp_f32 v157, v157\n"
    "v_exp_f32 v126, v126\n"
    "v_exp_f32 v127, v127\n"
    "v_smfmac_f32_32x32x32_bf16 v[78:93], v[20:23], v[150:157], v158\n"
    "ds_read_b128 v[34:37], v5 offset:28800\n"
    "v_exp_f32 v128, v128\n"
    "v_exp_f32 v129, v129\n"
    "v_exp_f32 v130, v130\n"
    "v_mfma_f32_32x32x16_bf16 v[142:157], v[10:13], v[56:59], 0\n"
    "s_cmp_lt_u32 s69, 3\n"
    "s_cbranch_scc1 106f\n"
    "s_add_u32 m0, m0, 1024\n"
    "s_add_u32 s54, s54, s85\n"
    "s_addc_u32 s55, s55, 0\n"
    "global_load_lds_dwordx4 v159, s[54:55] nt\n"
    "106:\n"
    "v_exp_f32 v131, v131\n"
    "v_exp_f32 v132, v132\n"
    "v_exp_f32 v133, v133\n"
    "v_mfma_f32_32x32x8_bf16 v[142:157], v[14:15], v[60:61], v[142:157]\n"
    "v_exp_f32 v134, v134\n"
    "v_exp_f32 v135, v135\n"
    "v_exp_f32 v136, v136\n"
    "v_exp_f32 v137, v137\n"
    "v_smfmac_f32_32x32x32_bf16 v[94:109], v[16:19], v[126:133], v158\n"
    "s_nop 2\n"
    "v_exp_f32 v138, v138\n"
    "v_exp_f32 v139, v139\n"
    "v_exp_f32 v140, v140\n"
    "v_exp_f32 v141, v141\n"
    "v_exp_f32 v142, v142\n"
    "v_exp_f32 v143, v143\n"
    "v_smfmac_f32_32x32x32_bf16 v[94:109], v[20:23], v[134:141], v158\n"
    "v_exp_f32 v144, v144\n"
    "v_exp_f32 v145, v145\n"
    "v_exp_f32 v146, v146\n"
    "s_waitcnt lgkmcnt(0)\n"
    "v_mfma_f32_32x32x16_bf16 v[126:141], v[24:27], v[38:41], 0\n"
    "s_cmp_lt_u32 s69, 3\n"
    "s_cbranch_scc1 108f\n"
    "s_add_u32 m0, m0, 1024\n"
    "s_add_u32 s54, s54, s85\n"
    "s_addc_u32 s55, s55, 0\n"
    "global_load_lds_dwordx4 v159, s[54:55] nt\n"
    "108:\n"
    "v_exp_f32 v147, v147\n"
    "v_exp_f32 v148, v148\n"
    "v_exp_f32 v149, v149\n"
    "v_mfma_f32_32x32x8_bf16 v[126:141], v[28:29], v[42:43], v[126:141]\n"
    "v_exp_f32 v150, v150\n"
    "v_exp_f32 v151, v151\n"
    "v_exp_f32 v152, v152\n"
    "v_exp_f32 v153, v153\n"
    "v_smfmac_f32_32x32x32_bf16 v[110:125], v[16:19], v[142:149], v158\n"
    "s_nop 2\n"
    "v_exp_f32 v154, v154\n"
    "v_exp_f32 v155, v155\n"
    "v_exp_f32 v156, v156\n"
    "v_exp_f32 v157, v157\n"
    "v_exp_f32 v126, v126\n"
    "v_exp_f32 v127, v127\n"
    "v_smfmac_f32_32x32x32_bf16 v[110:125], v[20:23], v[150:157], v158\n"
    "ds_read_b128 v[10:13], v3 offset:30720\n"
    "ds_read_b64 v[14:15], v4 offset:30720\n"
    "v_exp_f32 v128, v128\n"
    "v_exp_f32 v129, v129\n"
    "v_exp_f32 v130, v130\n"
    "v_mfma_f32_32x32x16_bf16 v[142:157], v[24:27], v[44:47], 0\n"
    "s_bitcmp1_b32 s70, 0\n"
    "s_cselect_b32 s85, 1, 8\n"
    "s_cselect_b64 vcc, -1, 0\n"
    "s_mul_i32 s85, s85, 9216\n"
    "v_readfirstlane_b32 s84, v6\n"
    "v_cndmask_b32 v159, v7, v8, vcc\n"
    "s_add_u32 m0, s84, 0\n"
    "s_nop 0\n"
    "global_load_lds_dwordx4 v159, s[50:51] nt\n"
    "v_exp_f32 v131, v131\n"
    "v_exp_f32 v132, v132\n"
    "v_exp_f32 v133, v133\n"
    "v_mfma_f32_32x32x8_bf16 v[142:157], v[28:29], v[48:49], v[142:157]\n"
    "v_exp_f32 v134, v134\n"
    "v_exp_f32 v135, v135\n"
    "v_exp_f32 v136, v136\n"
    "v_exp_f32 v137, v137\n"
    "v_smfmac_f32_32x32x32_bf16 v[62:77], v[30:33], v[126:133], v158\n"
    "s_nop 2\n"
    "v_exp_f32 v138, v138\n"
    "v_exp_f32 v139, v139\n"
    "v_exp_f32 v140, v140\n"
    "v_exp_f32 v141, v141\n"
    "v_exp_f32 v142, v142\n"
    "v_exp_f32 v143, v143\n"
    "v_smfmac_f32_32x32x32_bf16 v[62:77], v[34:37], v[134:141], v158\n"
    "v_exp_f32 v144, v144\n"
    "v_exp_f32 v145, v145\n"
    "v_exp_f32 v146, v146\n"
    "v_mfma_f32_32x32x16_bf16 v[126:141], v[24:27], v[50:53], 0\n"
    "v_exp_f32 v147, v147\n"
    "v_exp_f32 v148, v148\n"
    "v_exp_f32 v149, v149\n"
    "v_mfma_f32_32x32x8_bf16 v[126:141], v[28:29], v[54:55], v[126:141]\n"
    "ds_read_b128 v[16:19], v5 offset:30720\n"
    "v_exp_f32 v150, v150\n"
    "v_exp_f32 v151, v151\n"
    "v_exp_f32 v152, v152\n"
    "v_exp_f32 v153, v153\n"
    "v_smfmac_f32_32x32x32_bf16 v[78:93], v[30:33], v[142:149], v158\n"
    "s_nop 1\n"
    "v_exp_f32 v154, v154\n"
    "v_exp_f32 v155, v155\n"
    "v_exp_f32 v156, v156\n"
    "v_exp_f32 v157, v157\n"
    "v_exp_f32 v126, v126\n"
    "v_exp_f32 v127, v127\n"
    "v_smfmac_f32_32x32x32_bf16 v[78:93], v[34:37], v[150:157], v158\n"
    "v_exp_f32 v128, v128\n"
    "v_exp_f32 v129, v129\n"
    "v_exp_f32 v130, v130\n"
    "v_mfma_f32_32x32x16_bf16 v[142:157], v[24:27], v[56:59], 0\n"
    "ds_read_b128 v[20:23], v5 offset:31360\n"
    "s_add_u32 m0, m0, 1024\n"
    "v_exp_f32 v131, v131\n"
    "v_exp_f32 v132, v132\n"
    "v_exp_f32 v133, v133\n"
    "v_mfma_f32_32x32x8_bf16 v[142:157], v[28:29], v[60:61], v[142:157]\n"
    "s_add_u32 s50, s50, s85\n"
    "s_addc_u32 s51, s51, 0\n"
    "v_exp_f32 v134, v134\n"
    "v_exp_f32 v135, v135\n"
    "v_exp_f32 v136, v136\n"
    "v_exp_f32 v137, v137\n"
    "v_smfmac_f32_32x32x32_bf16 v[94:109], v[30:33], v[126:133], v158\n"
    "global_load_lds_dwordx4 v159, s[50:51] nt\n"
    "v_exp_f32 v138, v138\n"
    "v_exp_f32 v139, v139\n"
    "v_exp_f32 v140, v140\n"
    "v_exp_f32 v141, v141\n"
    "v_exp_f32 v142, v142\n"
    "v_exp_f32 v143, v143\n"
    "v_smfmac_f32_32x32x32_bf16 v[94:109], v[34:37], v[134:141], v158\n"
    "v_exp_f32 v144, v144\n"
    "v_exp_f32 v145, v145\n"
    "v_exp_f32 v146, v146\n"
    "s_waitcnt lgkmcnt(0)\n"
    "v_mfma_f32_32x32x16_bf16 v[126:141], v[10:13], v[38:41], 0\n"
    "s_add_u32 s90, s68, 5\n"
    "s_and_b32 s90, s90, 7\n"
    "v_exp_f32 v147, v147\n"
    "v_exp_f32 v148, v148\n"
    "v_exp_f32 v149, v149\n"
    "v_mfma_f32_32x32x8_bf16 v[126:141], v[14:15], v[42:43], v[126:141]\n"
    "s_lshl_b32 s90, s90, 16\n"
    "v_exp_f32 v150, v150\n"
    "v_exp_f32 v151, v151\n"
    "v_exp_f32 v152, v152\n"
    "v_exp_f32 v153, v153\n"
    "v_smfmac_f32_32x32x32_bf16 v[110:125], v[30:33], v[142:149], v158\n"
    "s_add_u32 s94, s44, s90\n"
    "s_addc_u32 s95, s45, 0\n"
    "global_store_dwordx4 v2, v[160:163], s[94:95] offset:0 sc1\n"
    "v_exp_f32 v154, v154\n"
    "v_exp_f32 v155, v155\n"
    "v_exp_f32 v156, v156\n"
    "v_exp_f32 v157, v157\n"
    "v_exp_f32 v126, v126\n"
    "v_exp_f32 v127, v127\n"
    "v_smfmac_f32_32x32x32_bf16 v[110:125], v[34:37], v[150:157], v158\n"
    "v_exp_f32 v128, v128\n"
    "v_exp_f32 v129, v129\n"
    "v_exp_f32 v130, v130\n"
    "v_mfma_f32_32x32x16_bf16 v[142:157], v[10:13], v[44:47], 0\n"
    "ds_read_b128 v[24:27], v3 offset:33280\n"
    "ds_read_b64 v[28:29], v4 offset:33280\n"
    "v_exp_f32 v131, v131\n"
    "v_exp_f32 v132, v132\n"
    "v_exp_f32 v133, v133\n"
    "v_mfma_f32_32x32x8_bf16 v[142:157], v[14:15], v[48:49], v[142:157]\n"
    "s_add_u32 m0, m0, 1024\n"
    "v_exp_f32 v134, v134\n"
    "v_exp_f32 v135, v135\n"
    "v_exp_f32 v136, v136\n"
    "v_exp_f32 v137, v137\n"
    "v_smfmac_f32_32x32x32_bf16 v[62:77], v[16:19], v[126:133], v158\n"
    "s_add_u32 s50, s50, s85\n"
    "s_addc_u32 s51, s51, 0\n"
    "global_load_lds_dwordx4 v159, s[50:51] nt\n"
    "v_exp_f32 v138, v138\n"
    "v_exp_f32 v139, v139\n"
    "v_exp_f32 v140, v140\n"
    "v_exp_f32 v141, v141\n"
    "v_exp_f32 v142, v142\n"
    "v_exp_f32 v143, v143\n"
    "v_smfmac_f32_32x32x32_bf16 v[62:77], v[20:23], v[134:141], v158\n"
    "v_exp_f32 v144, v144\n"
    "v_exp_f32 v145, v145\n"
    "v_exp_f32 v146, v146\n"
    "v_mfma_f32_32x32x16_bf16 v[126:141], v[10:13], v[50:53], 0\n"
    "v_exp_f32 v147, v147\n"
    "v_exp_f32 v148, v148\n"
    "v_exp_f32 v149, v149\n"
    "v_mfma_f32_32x32x8_bf16 v[126:141], v[14:15], v[54:55], v[126:141]\n"
    "ds_read_b128 v[30:33], v5 offset:33280\n"
    "v_exp_f32 v150, v150\n"
    "v_exp_f32 v151, v151\n"
    "v_exp_f32 v152, v152\n"
    "v_exp_f32 v153, v153\n"
    "v_smfmac_f32_32x32x32_bf16 v[78:93], v[16:19], v[142:149], v158\n"
    "s_nop 1\n"
    "v_exp_f32 v154, v154\n"
    "v_exp_f32 v155, v155\n"
    "v_exp_f32 v156, v156\n"
    "v_exp_f32 v157, v157\n"
    "v_exp_f32 v126, v126\n"
    "v_exp_f32 v127, v127\n"
    "v_smfmac_f32_32x32x32_bf16 v[78:93], v[20:23], v[150:157], v158\n"
    "v_exp_f32 v128, v128\n"
    "v_exp_f32 v129, v129\n"
    "v_exp_f32 v130, v130\n"
    "v_mfma_f32_32x32x16_bf16 v[142:157], v[10:13], v[56:59], 0\n"
    "ds_read_b128 v[34:37], v5 offset:33920\n"
    "s_add_u32 m0, m0, 1024\n"
    "v_exp_f32 v131, v131\n"
    "v_exp_f32 v132, v132\n"
    "v_exp_f32 v133, v133\n"
    "v_mfma_f32_32x32x8_bf16 v[142:157], v[14:15], v[60:61], v[142:157]\n"
    "s_add_u32 s50, s50, s85\n"
    "s_addc_u32 s51, s51, 0\n"
    "v_exp_f32 v134, v134\n"
    "v_exp_f32 v135, v135\n"
    "v_exp_f32 v136, v136\n"
    "v_exp_f32 v137, v137\n"
    "v_smfmac_f32_32x32x32_bf16 v[94:109], v[16:19], v[126:133], v158\n"
    "global_load_lds_dwordx4 v159, s[50:51] nt\n"
    "v_exp_f32 v138, v138\n"
    "v_exp_f32 v139, v139\n"
    "v_exp_f32 v140, v140\n"
    "v_exp_f32 v141, v141\n"
    "v_exp_f32 v142, v142\n"
    "v_exp_f32 v143, v143\n"
    "v_smfmac_f32_32x32x32_bf16 v[94:109], v[20:23], v[134:141], v158\n"
    "v_exp_f32 v144, v144\n"
    "v_exp_f32 v145, v145\n"
    "v_exp_f32 v146, v146\n"
    "s_waitcnt lgkmcnt(0)\n"
    "v_mfma_f32_32x32x16_bf16 v[126:141], v[24:27], v[38:41], 0\n"
    "v_exp_f32 v147, v147\n"
    "v_exp_f32 v148, v148\n"
    "v_exp_f32 v149, v149\n"
    "v_mfma_f32_32x32x8_bf16 v[126:141], v[28:29], v[42:43], v[126:141]\n"
    "v_exp_f32 v150, v150\n"
    "v_exp_f32 v151, v151\n"
    "v_exp_f32 v152, v152\n"
    "v_exp_f32 v153, v153\n"
    "v_smfmac_f32_32x32x32_bf16 v[110:125], v[16:19], v[142:149], v158\n"
    "s_nop 2\n"
    "v_exp_f32 v154, v154\n"
    "v_exp_f32 v155, v155\n"
    "v_exp_f32 v156, v156\n"
    "v_exp_f32 v157, v157\n"
    "v_exp_f32 v126, v126\n"
    "v_exp_f32 v127, v127\n"
    "v_smfmac_f32_32x32x32_bf16 v[110:125], v[20:23], v[150:157], v158\n"
    "ds_read_b128 v[10:13], v3 offset:35840\n"
    "ds_read_b64 v[14:15], v4 offset:35840\n"
    "v_exp_f32 v128, v128\n"
    "v_exp_f32 v129, v129\n"
    "v_exp_f32 v130, v130\n"
    "v_mfma_f32_32x32x16_bf16 v[142:157], v[24:27], v[44:47], 0\n"
    "s_bitcmp1_b32 s70, 1\n"
    "s_cselect_b32 s85, 1, 8\n"
    "s_cselect_b64 vcc, -1, 0\n"
    "s_mul_i32 s85, s85, 9216\n"
    "v_readfirstlane_b32 s84, v6\n"
    "v_cndmask_b32 v159, v7, v8, vcc\n"
    "s_add_u32 m0, s84, 4096\n"
    "s_nop 0\n"
    "global_load_lds_dwordx4 v159, s[52:53] nt\n"
    "v_exp_f32 v131, v131\n"
    "v_exp_f32 v132, v132\n"
    "v_exp_f32 v133, v133\n"
    "v_mfma_f32_32x32x8_bf16 v[142:157], v[28:29], v[48:49], v[142:157]\n"
    "v_exp_f32 v134, v134\n"
    "v_exp_f32 v135, v135\n"
    "v_exp_f32 v136, v136\n"
    "v_exp_f32 v137, v137\n"
    "v_smfmac_f32_32x32x32_bf16 v[62:77], v[30:33], v[126:133], v158\n"
    "s_nop 2\n"
    "v_exp_f32 v138, v138\n"
    "v_exp_f32 v139, v139\n"
    "v_exp_f32 v140, v140\n"
    "v_exp_f32 v141, v141\n"
    "v_exp_f32 v142, v142\n"
    "v_exp_f32 v143, v143\n"
    "v_smfmac_f32_32x32x32_bf16 v[62:77], v[34:37], v[134:141], v158\n"
    "v_exp_f32 v144, v144\n"
    "v_exp_f32 v145, v145\n"
    "v_exp_f32 v146, v146\n"
    "v_mfma_f32_32x32x16_bf16 v[126:141], v[24:27], v[50:53], 0\n"
    "v_exp_f32 v147, v147\n"
    "v_exp_f32 v148, v148\n"
    "v_exp_f32 v149, v149\n"
    "v_mfma_f32_32x32x8_bf16 v[126:141], v[28:29], v[54:55], v[126:141]\n"
    "ds_read_b128 v[16:19], v5 offset:35840\n"
    "global_store_dwordx4 v2, v[160:163], s[94:95] offset:1024 sc1\n"
    "v_exp_f32 v150, v150\n"
    "v_exp_f32 v151, v151\n"
    "v_exp_f32 v152, v152\n"
    "v_exp_f32 v153, v153\n"
    "v_smfmac_f32_32x32x32_bf16 v[78:93], v[30:33], v[142:149], v158\n"
    "s_nop 0\n"
    "v_exp_f32 v154, v154\n"
    "v_exp_f32 v155, v155\n"
    "v_exp_f32 v156, v156\n"
    "v_exp_f32 v157, v157\n"
    "v_exp_f32 v126, v126\n"
    "v_exp_f32 v127, v127\n"
    "v_smfmac_f32_32x32x32_bf16 v[78:93], v[34:37], v[150:157], v158\n"
    "v_exp_f32 v128, v128\n"
    "v_exp_f32 v129, v129\n"
    "v_exp_f32 v130, v130\n"
    "v_mfma_f32_32x32x16_bf16 v[142:157], v[24:27], v[56:59], 0\n"
    "ds_read_b128 v[20:23], v5 offset:36480\n"
    "s_add_u32 m0, m0, 1024\n"
    "v_exp_f32 v131, v131\n"
    "v_exp_f32 v132, v132\n"
    "v_exp_f32 v133, v133\n"
    "v_mfma_f32_32x32x8_bf16 v[142:157], v[28:29], v[60:61], v[142:157]\n"
    "s_add_u32 s52, s52, s85\n"
    "s_addc_u32 s53, s53, 0\n"
    "v_exp_f32 v134, v134\n"
    "v_exp_f32 v135, v135\n"
    "v_exp_f32 v136, v136\n"
    "v_exp_f32 v137, v137\n"
    "v_smfmac_f32_32x32x32_bf16 v[94:109], v[30:33], v[126:133], v158\n"
    "global_load_lds_dwordx4 v159, s[52:53] nt\n"
    "v_exp_f32 v138, v138\n"
    "v_exp_f32 v139, v139\n"
    "v_exp_f32 v140, v140\n"
    "v_exp_f32 v141, v141\n"
    "v_exp_f32 v142, v142\n"
    "v_exp_f32 v143, v143\n"
    "v_smfmac_f32_32x32x32_bf16 v[94:109], v[34:37], v[134:141], v158\n"
    "v_exp_f32 v144, v144\n"
    "v_exp_f32 v145, v145\n"
    "v_exp_f32 v146, v146\n"
    "s_waitcnt lgkmcnt(0)\n"
    "v_mfma_f32_32x32x16_bf16 v[126:141], v[10:13], v[38:41], 0\n"
    "v_exp_f32 v147, v147\n"
    "v_exp_f32 v148, v148\n"
    "v_exp_f32 v149, v149\n"
    "v_mfma_f32_32x32x8_bf16 v[126:141], v[14:15], v[42:43], v[126:141]\n"
    "v_exp_f32 v150, v150\n"
    "v_exp_f32 v151, v151\n"
    "v_exp_f32 v152, v152\n"
    "v_exp_f32 v153, v153\n"
    "v_smfmac_f32_32x32x32_bf16 v[110:125], v[30:33], v[142:149], v158\n"
    "s_nop 2\n"
    "v_exp_f32 v154, v154\n"
    "v_exp_f32 v155, v155\n"
    "v_exp_f32 v156, v156\n"
    "v_exp_f32 v157, v157\n"
    "v_exp_f32 v126, v126\n"
    "v_exp_f32 v127, v127\n"
    "v_smfmac_f32_32x32x32_bf16 v[110:125], v[34:37], v[150:157], v158\n"
    "v_exp_f32 v128, v128\n"
    "v_exp_f32 v129, v129\n"
    "v_exp_f32 v130, v130\n"
    "v_mfma_f32_32x32x16_bf16 v[142:157], v[10:13], v[44:47], 0\n"
    "ds_read_b128 v[24:27], v3 offset:38400\n"
    "ds_read_b64 v[28:29], v4 offset:38400\n"
    "v_exp_f32 v131, v131\n"
    "v_exp_f32 v132, v132\n"
    "v_exp_f32 v133, v133\n"
    "v_mfma_f32_32x32x8_bf16 v[142:157], v[14:15], v[48:49], v[142:157]\n"
    "s_add_u32 m0, m0, 1024\n"
    "v_exp_f32 v134, v134\n"
    "v_exp_f32 v135, v135\n"
    "v_exp_f32 v136, v136\n"
    "v_exp_f32 v137, v137\n"
    "v_smfmac_f32_32x32x32_bf16 v[62:77], v[16:19], v[126:133], v158\n"
    "s_add_u32 s52, s52, s85\n"
    "s_addc_u32 s53, s53, 0\n"
    "global_load_lds_dwordx4 v159, s[52:53] nt\n"
    "v_exp_f32 v138, v138\n"
    "v_exp_f32 v139, v139\n"
    "v_exp_f32 v140, v140\n"
    "v_exp_f32 v141, v141\n"
    "v_exp_f32 v142, v142\n"
    "v_exp_f32 v143, v143\n"
    "v_smfmac_f32_32x32x32_bf16 v[62:77], v[20:23], v[134:141], v158\n"
    "v_exp_f32 v144, v144\n"
    "v_exp_f32 v145, v145\n"
    "v_exp_f32 v146, v146\n"
    "v_mfma_f32_32x32x16_bf16 v[126:141], v[10:13], v[50:53], 0\n"
    "v_exp_f32 v147, v147\n"
    "v_exp_f32 v148, v148\n"
    "v_exp_f32 v149, v149\n"
    "v_mfma_f32_32x32x8_bf16 v[126:141], v[14:15], v[54:55], v[126:141]\n"
    "ds_read_b128 v[30:33], v5 offset:38400\n"
    "v_exp_f32 v150, v150\n"
    "v_exp_f32 v151, v151\n"
    "v_exp_f32 v152, v152\n"
    "v_exp_f32 v153, v153\n"
    "v_smfmac_f32_32x32x32_bf16 v[78:93], v[16:19], v[142:149], v158\n"
    "s_nop 1\n"
    "v_exp_f32 v154, v154\n"
    "v_exp_f32 v155, v155\n"
    "v_exp_f32 v156, v156\n"
    "v_exp_f32 v157, v157\n"
    "v_exp_f32 v126, v126\n"
    "v_exp_f32 v127, v127\n"
    "v_smfmac_f32_32x32x32_bf16 v[78:93], v[20:23], v[150:157], v158\n"
    "v_exp_f32 v128, v128\n"
    "v_exp_f32 v129, v129\n"
    "v_exp_f32 v130, v130\n"
    "v_mfma_f32_32x32x16_bf16 v[142:157], v[10:13], v[56:59], 0\n"
    "ds_read_b128 v[34:37], v5 offset:39040\n"
    "s_add_u32 m0, m0, 1024\n"
    "v_exp_f32 v131, v131\n"
    "v_exp_f32 v132, v132\n"
    "v_exp_f32 v133, v133\n"
    "v_mfma_f32_32x32x8_bf16 v[142:157], v[14:15], v[60:61], v[142:157]\n"
    "s_add_u32 s52, s52, s85\n"
    "s_addc_u32 s53, s53, 0\n"
    "v_exp_f32 v134, v134\n"
    "v_exp_f32 v135, v135\n"
    "v_exp_f32 v136, v136\n"
    "v_exp_f32 v137, v137\n"
    "v_smfmac_f32_32x32x32_bf16 v[94:109], v[16:19], v[126:133], v158\n"
    "global_load_lds_dwordx4 v159, s[52:53] nt\n"
    "v_exp_f32 v138, v138\n"
    "v_exp_f32 v139, v139\n"
    "v_exp_f32 v140, v140\n"
    "v_exp_f32 v141, v141\n"
    "v_exp_f32 v142, v142\n"
    "v_exp_f32 v143, v143\n"
    "v_smfmac_f32_32x32x32_bf16 v[94:109], v[20:23], v[134:141], v158\n"
    "v_exp_f32 v144, v144\n"
    "v_exp_f32 v145, v145\n"
    "v_exp_f32 v146, v146\n"
    "s_waitcnt lgkmcnt(0)\n"
    "v_mfma_f32_32x32x16_bf16 v[126:141], v[24:27], v[38:41], 0\n"
    "s_add_u32 s90, s68, 6\n"
    "s_and_b32 s90, s90, 7\n"
    "v_exp_f32 v147, v147\n"
    "v_exp_f32 v148, v148\n"
    "v_exp_f32 v149, v149\n"
    "v_mfma_f32_32x32x8_bf16 v[126:141], v[28:29], v[42:43], v[126:141]\n"
    "s_lshl_b32 s90, s90, 16\n"
    "v_exp_f32 v150, v150\n"
    "v_exp_f32 v151, v151\n"
    "v_exp_f32 v152, v152\n"
    "v_exp_f32 v153, v153\n"
    "v_smfmac_f32_32x32x32_bf16 v[110:125], v[16:19], v[142:149], v158\n"
    "s_add_u32 s94, s44, s90\n"
    "s_addc_u32 s95, s45, 0\n"
    "global_store_dwordx4 v2, v[160:163], s[94:95] offset:0 sc1\n"
    "v_exp_f32 v154, v154\n"
    "v_exp_f32 v155, v155\n"
    "v_exp_f32 v156, v156\n"
    "v_exp_f32 v157, v157\n"
    "v_exp_f32 v126, v126\n"
    "v_exp_f32 v127, v127\n"
    "v_smfmac_f32_32x32x32_bf16 v[110:125], v[20:23], v[150:157], v158\n"
    "v_exp_f32 v128, v128\n"
    "v_exp_f32 v129, v129\n"
    "v_exp_f32 v130, v130\n"
    "v_mfma_f32_32x32x16_bf16 v[142:157], v[24:27], v[44:47], 0\n"
    "v_exp_f32 v131, v131\n"
    "v_exp_f32 v132, v132\n"
    "v_exp_f32 v133, v133\n"
    "v_mfma_f32_32x32x8_bf16 v[142:157], v[28:29], v[48:49], v[142:157]\n"
    "ds_read_b128 v[10:13], v3 offset:40960\n"
    "ds_read_b64 v[14:15], v4 offset:40960\n"
    "v_exp_f32 v134, v134\n"
    "v_exp_f32 v135, v135\n"
    "v_exp_f32 v136, v136\n"
    "v_exp_f32 v137, v137\n"
    "v_smfmac_f32_32x32x32_bf16 v[62:77], v[30:33], v[126:133], v158\n"
    "global_load_dwordx4 a[128:131], v2, s[38:39] offset:0\n"
    "global_load_dwordx4 a[132:135], v2, s[38:39] offset:1024\n"
    "v_exp_f32 v138, v138\n"
    "v_exp_f32 v139, v139\n"
    "v_exp_f32 v140, v140\n"
    "v_exp_f32 v141, v141\n"
    "v_exp_f32 v142, v142\n"
    "v_exp_f32 v143, v143\n"
    "v_smfmac_f32_32x32x32_bf16 v[62:77], v[34:37], v[134:141], v158\n"
    "v_exp_f32 v144, v144\n"
    "v_exp_f32 v145, v145\n"
    "v_exp_f32 v146, v146\n"
    "v_mfma_f32_32x32x16_bf16 v[126:141], v[24:27], v[50:53], 0\n"
    "v_exp_f32 v147, v147\n"
    "v_exp_f32 v148, v148\n"
    "v_exp_f32 v149, v149\n"
    "v_mfma_f32_32x32x8_bf16 v[126:141], v[28:29], v[54:55], v[126:141]\n"
    "ds_read_b128 v[16:19], v5 offset:40960\n"
    "global_load_dwordx4 a[136:139], v2, s[38:39] offset:2048\n"
    "v_exp_f32 v150, v150\n"
    "v_exp_f32 v151, v151\n"
    "v_exp_f32 v152, v152\n"
    "v_exp_f32 v153, v153\n"
    "v_smfmac_f32_32x32x32_bf16 v[78:93], v[30:33], v[142:149], v158\n"
    "global_load_dwordx4 a[140:143], v2, s[38:39] offset:3072\n"
    "v_exp_f32 v154, v154\n"
    "v_exp_f32 v155, v155\n"
    "v_exp_f32 v156, v156\n"
    "v_exp_f32 v157, v157\n"
    "v_exp_f32 v126, v126\n"
    "v_exp_f32 v127, v127\n"
    "v_smfmac_f32_32x32x32_bf16 v[78:93], v[34:37], v[150:157], v158\n"
    "v_exp_f32 v128, v128\n"
    "v_exp_f32 v129, v129\n"
    "v_exp_f32 v130, v130\n"
    "v_mfma_f32_32x32x16_bf16 v[142:157], v[24:27], v[56:59], 0\n"
    "ds_read_b128 v[20:23], v5 offset:41600\n"
    "v_exp_f32 v131, v131\n"
    "v_exp_f32 v132, v132\n"
    "v_exp_f32 v133, v133\n"
    "v_mfma_f32_32x32x8_bf16 v[142:157], v[28:29], v[60:61], v[142:157]\n"
    "s_add_u32 s38, s38, 8192\n"
    "s_addc_u32 s39, s39, 0\n"
    "v_exp_f32 v134, v134\n"
    "v_exp_f32 v135, v135\n"
    "v_exp_f32 v136, v136\n"
    "v_exp_f32 v137, v137\n"
    "v_smfmac_f32_32x32x32_bf16 v[94:109], v[30:33], v[126:133], v158\n"
    "global_load_dwordx4 a[144:147], v2, s[38:39] offset:-4096\n"
    "global_load_dwordx4 a[148:151], v2, s[38:39] offset:-3072\n"
    "v_exp_f32 v138, v138\n"
    "v_exp_f32 v139, v139\n"
    "v_exp_f32 v140, v140\n"
    "v_exp_f32 v141, v141\n"
    "v_exp_f32 v142, v142\n"
    "v_exp_f32 v143, v143\n"
    "v_smfmac_f32_32x32x32_bf16 v[94:109], v[34:37], v[134:141], v158\n"
    "v_exp_f32 v144, v144\n"
    "v_exp_f32 v145, v145\n"
    "v_exp_f32 v146, v146\n"
    "s_waitcnt lgkmcnt(0)\n"
    "v_mfma_f32_32x32x16_bf16 v[126:141], v[10:13], v[38:41], 0\n"
    "v_exp_f32 v147, v147\n"
    "v_exp_f32 v148, v148\n"
    "v_exp_f32 v149, v149\n"
    "v_mfma_f32_32x32x8_bf16 v[126:141], v[14:15], v[42:43], v[126:141]\n"
    "global_store_dwordx4 v2, v[160:163], s[94:95] offset:1024 sc1\n"
    "v_exp_f32 v150, v150\n"
    "v_exp_f32 v151, v151\n"
    "v_exp_f32 v152, v152\n"
    "v_exp_f32 v153, v153\n"
    "v_smfmac_f32_32x32x32_bf16 v[110:125], v[30:33], v[142:149], v158\n"
    "s_nop 1\n"
    "v_exp_f32 v154, v154\n"
    "v_exp_f32 v155, v155\n"
    "v_exp_f32 v156, v156\n"
    "v_exp_f32 v157, v157\n"
    "v_exp_f32 v126, v126\n"
    "v_exp_f32 v127, v127\n"
    "v_smfmac_f32_32x32x32_bf16 v[110:125], v[34:37], v[150:157], v158\n"
    "v_exp_f32 v128, v128\n"
    "v_exp_f32 v129, v129\n"
    "v_exp_f32 v130, v130\n"
    "v_mfma_f32_32x32x16_bf16 v[142:157], v[10:13], v[44:47], 0\n"
    "v_exp_f32 v131, v131\n"
    "v_exp_f32 v132, v132\n"
    "v_exp_f32 v133, v133\n"
    "v_mfma_f32_32x32x8_bf16 v[142:157], v[14:15], v[48:49], v[142:157]\n"
    "ds_read_b128 v[24:27], v3 offset:43520\n"
    "ds_read_b64 v[28:29], v4 offset:43520\n"
    "v_exp_f32 v134, v134\n"
    "v_exp_f32 v135, v135\n"
    "v_exp_f32 v136, v136\n"
    "v_exp_f32 v137, v137\n"
    "v_smfmac_f32_32x32x32_bf16 v[62:77], v[16:19], v[126:133], v158\n"
    "global_load_dwordx4 a[152:155], v2, s[38:39] offset:-2048\n"
    "global_load_dwordx4 a[156:159], v2, s[38:39] offset:-1024\n"
    "v_exp_f32 v138, v138\n"
    "v_exp_f32 v139, v139\n"
    "v_exp_f32 v140, v140\n"
    "v_exp_f32 v141, v141\n"
    "v_exp_f32 v142, v142\n"
    "v_exp_f32 v143, v143\n"
    "v_smfmac_f32_32x32x32_bf16 v[62:77], v[20:23], v[134:141], v158\n"
    "v_exp_f32 v144, v144\n"
    "v_exp_f32 v145, v145\n"
    "v_exp_f32 v146, v146\n"
    "v_mfma_f32_32x32x16_bf16 v[126:141], v[10:13], v[50:53], 0\n"
    "v_exp_f32 v147, v147\n"
    "v_exp_f32 v148, v148\n"
    "v_exp_f32 v149, v149\n"
    "v_mfma_f32_32x32x8_bf16 v[126:141], v[14:15], v[54:55], v[126:141]\n"
    "ds_read_b128 v[30:33], v5 offset:43520\n"
    "global_load_dwordx4 a[160:163], v2, s[38:39] offset:0\n"
    "v_exp_f32 v150, v150\n"
    "v_exp_f32 v151, v151\n"
    "v_exp_f32 v152, v152\n"
    "v_exp_f32 v153, v153\n"
    "v_smfmac_f32_32x32x32_bf16 v[78:93], v[16:19], v[142:149], v158\n"
    "global_load_dwordx4 a[164:167], v2, s[38:39] offset:1024\n"
    "v_exp_f32 v154, v154\n"
    "v_exp_f32 v155, v155\n"
    "v_exp_f32 v156, v156\n"
    "v_exp_f32 v157, v157\n"
    "v_exp_f32 v126, v126\n"
    "v_exp_f32 v127, v127\n"
    "v_smfmac_f32_32x32x32_bf16 v[78:93], v[20:23], v[150:157], v158\n"
    "v_exp_f32 v128, v128\n"
    "v_exp_f32 v129, v129\n"
    "v_exp_f32 v130, v130\n"
    "v_mfma_f32_32x32x16_bf16 v[142:157], v[10:13], v[56:59], 0\n"
    "v_exp_f32 v131, v131\n"
    "v_exp_f32 v132, v132\n"
    "v_exp_f32 v133, v133\n"
    "v_mfma_f32_32x32x8_bf16 v[142:157], v[14:15], v[60:61], v[142:157]\n"
    "ds_read_b128 v[34:37], v5 offset:44160\n"
    "global_load_dwordx4 a[168:171], v2, s[38:39] offset:2048\n"
    "v_exp_f32 v134, v134\n"
    "v_exp_f32 v135, v135\n"
    "v_exp_f32 v136, v136\n"
    "v_exp_f32 v137, v137\n"
    "v_smfmac_f32_32x32x32_bf16 v[94:109], v[16:19], v[126:133], v158\n"
    "global_load_dwordx4 a[172:175], v2, s[38:39] offset:3072\n"
    "v_exp_f32 v138, v138\n"
    "v_exp_f32 v139, v139\n"
    "v_exp_f32 v140, v140\n"
    "v_exp_f32 v141, v141\n"
    "v_exp_f32 v142, v142\n"
    "v_exp_f32 v143, v143\n"
    "v_smfmac_f32_32x32x32_bf16 v[94:109], v[20:23], v[134:141], v158\n"
    "v_exp_f32 v144, v144\n"
    "v_exp_f32 v145, v145\n"
    "v_exp_f32 v146, v146\n"
    "s_waitcnt lgkmcnt(0)\n"
    "v_mfma_f32_32x32x16_bf16 v[126:141], v[24:27], v[38:41], 0\n"
    "v_exp_f32 v147, v147\n"
    "v_exp_f32 v148, v148\n"
    "v_exp_f32 v149, v149\n"
    "v_mfma_f32_32x32x8_bf16 v[126:141], v[28:29], v[42:43], v[126:141]\n"
    "s_add_u32 s38, s38, 8192\n"
    "s_addc_u32 s39, s39, 0\n"
    "v_exp_f32 v150, v150\n"
    "v_exp_f32 v151, v151\n"
    "v_exp_f32 v152, v152\n"
    "v_exp_f32 v153, v153\n"
    "v_smfmac_f32_32x32x32_bf16 v[110:125], v[16:19], v[142:149], v158\n"
    "global_load_dwordx4 a[176:179], v2, s[38:39] offset:-4096\n"
    "global_load_dwordx4 a[180:183], v2, s[38:39] offset:-3072\n"
    "v_exp_f32 v154, v154\n"
    "v_exp_f32 v155, v155\n"
    "v_exp_f32 v156, v156\n"
    "v_exp_f32 v157, v157\n"
    "v_exp_f32 v126, v126\n"
    "v_exp_f32 v127, v127\n"
    "v_smfmac_f32_32x32x32_bf16 v[110:125], v[20:23], v[150:157], v158\n"
    "v_exp_f32 v128, v128\n"
    "v_exp_f32 v129, v129\n"
    "v_exp_f32 v130, v130\n"
    "v_mfma_f32_32x32x16_bf16 v[142:157], v[24:27], v[44:47], 0\n"
    "ds_read_b128 v[10:13], v3 offset:46080\n"
    "ds_read_b64 v[14:15], v4 offset:46080\n"
    "v_exp_f32 v131, v131\n"
    "v_exp_f32 v132, v132\n"
    "v_exp_f32 v133, v133\n"
    "v_mfma_f32_32x32x8_bf16 v[142:157], v[28:29], v[48:49], v[142:157]\n"
    "s_waitcnt vmcnt(21)\n"
    "global_load_dwordx4 a[184:187], v2, s[38:39] offset:-2048\n"
    "v_exp_f32 v134, v134\n"
    "v_exp_f32 v135, v135\n"
    "v_exp_f32 v136, v136\n"
    "v_exp_f32 v137, v137\n"
    "v_smfmac_f32_32x32x32_bf16 v[62:77], v[30:33], v[126:133], v158\n"
    "global_load_dwordx4 a[188:191], v2, s[38:39] offset:-1024\n"
    "v_exp_f32 v138, v138\n"
    "v_exp_f32 v139, v139\n"
    "v_exp_f32 v140, v140\n"
    "v_exp_f32 v141, v141\n"
    "v_exp_f32 v142, v142\n"
    "v_exp_f32 v143, v143\n"
    "v_smfmac_f32_32x32x32_bf16 v[62:77], v[34:37], v[134:141], v158\n"
    "v_exp_f32 v144, v144\n"
    "v_exp_f32 v145, v145\n"
    "v_exp_f32 v146, v146\n"
    "v_mfma_f32_32x32x16_bf16 v[126:141], v[24:27], v[50:53], 0\n"
    "ds_read_b128 v[16:19], v5 offset:46080\n"
    "v_exp_f32 v147, v147\n"
    "v_exp_f32 v148, v148\n"
    "v_exp_f32 v149, v149\n"
    "v_mfma_f32_32x32x8_bf16 v[126:141], v[28:29], v[54:55], v[126:141]\n"
    "s_cmp_lt_u32 s69, 3\n"
    "s_cbranch_scc1 110f\n"
    "ds_read_b64_tr_b16 v[240:241], v6 offset:8192\n"
    "ds_read_b64_tr_b16 v[242:243], v6 offset:10240\n"
    "ds_read_b64_tr_b16 v[244:245], v6 offset:8224\n"
    "ds_read_b64_tr_b16 v[246:247], v6 offset:10272\n"
    "110:\n"
    "v_exp_f32 v150, v150\n"
    "v_exp_f32 v151, v151\n"
    "v_exp_f32 v152, v152\n"
    "v_exp_f32 v153, v153\n"
    "v_smfmac_f32_32x32x32_bf16 v[78:93], v[30:33], v[142:149], v158\n"
    "global_load_dwordx4 a[192:195], v2, s[38:39] offset:0\n"
    "v_exp_f32 v154, v154\n"
    "v_exp_f32 v155, v155\n"
    "v_exp_f32 v156, v156\n"
    "v_exp_f32 v157, v157\n"
    "v_exp_f32 v126, v126\n"
    "v_exp_f32 v127, v127\n"
    "v_smfmac_f32_32x32x32_bf16 v[78:93], v[34:37], v[150:157], v158\n"
    "ds_read_b128 v[20:23], v5 offset:46720\n"
    "v_exp_f32 v128, v128\n"
    "v_exp_f32 v129, v129\n"
    "v_exp_f32 v130, v130\n"
    "v_mfma_f32_32x32x16_bf16 v[142:157], v[24:27], v[56:59], 0\n"
    "s_cmp_lt_u32 s69, 3\n"
    "s_cbranch_scc1 112f\n"
    "ds_read_b64_tr_b16 v[248:249], v6 offset:8256\n"
    "ds_read_b64_tr_b16 v[250:251], v6 offset:10304\n"
    "ds_read_b64_tr_b16 v[252:253], v6 offset:8288\n"
    "ds_read_b64_tr_b16 v[254:255], v6 offset:10336\n"
    "112:\n"
    "v_exp_f32 v131, v131\n"
    "v_exp_f32 v132, v132\n"
    "v_exp_f32 v133, v133\n"
    "v_mfma_f32_32x32x8_bf16 v[142:157], v[28:29], v[60:61], v[142:157]\n"
    "global_load_dwordx4 a[196:199], v2, s[38:39] offset:1024\n"
    "global_load_dwordx4 a[200:203], v2, s[38:39] offset:2048\n"
    "v_exp_f32 v134, v134\n"
    "v_exp_f32 v135, v135\n"
    "v_exp_f32 v136, v136\n"
    "v_exp_f32 v137, v137\n"
    "v_smfmac_f32_32x32x32_bf16 v[94:109], v[30:33], v[126:133], v158\n"
    "s_nop 0\n"
    "v_exp_f32 v138, v138\n"
    "v_exp_f32 v139, v139\n"
    "v_exp_f32 v140, v140\n"
    "v_exp_f32 v141, v141\n"
    "v_exp_f32 v142, v142\n"
    "v_exp_f32 v143, v143\n"
    "v_smfmac_f32_32x32x32_bf16 v[94:109], v[34:37], v[134:141], v158\n"
    "v_exp_f32 v144, v144\n"
    "v_exp_f32 v145, v145\n"
    "v_exp_f32 v146, v146\n"
    "s_waitcnt lgkmcnt(0)\n"
    "v_mfma_f32_32x32x16_bf16 v[126:141], v[10:13], v[38:41], 0\n"
    "v_exp_f32 v147, v147\n"
    "v_exp_f32 v148, v148\n"
    "v_exp_f32 v149, v149\n"
    "v_mfma_f32_32x32x8_bf16 v[126:141], v[14:15], v[42:43], v[126:141]\n"
    "s_cmp_lt_u32 s69, 3\n"
    "s_cbranch_scc1 114f\n"
    "global_store_dwordx4 v2, v[240:243], s[60:61] sc1\n"
    "114:\n"
    "v_exp_f32 v150, v150\n"
    "v_exp_f32 v151, v151\n"
    "v_exp_f32 v152, v152\n"
    "v_exp_f32 v153, v153\n"
    "v_smfmac_f32_32x32x32_bf16 v[110:125], v[30:33], v[142:149], v158\n"
    "s_nop 0\n"
    "v_exp_f32 v154, v154\n"
    "v_exp_f32 v155, v155\n"
    "v_exp_f32 v156, v156\n"
    "v_exp_f32 v157, v157\n"
    "v_exp_f32 v126, v126\n"
    "v_exp_f32 v127, v127\n"
    "v_smfmac_f32_32x32x32_bf16 v[110:125], v[34:37], v[150:157], v158\n"
    "v_exp_f32 v128, v128\n"
    "v_exp_f32 v129, v129\n"
    "v_exp_f32 v130, v130\n"
    "v_mfma_f32_32x32x16_bf16 v[142:157], v[10:13], v[44:47], 0\n"
    "ds_read_b128 v[24:27], v3 offset:48640\n"
    "ds_read_b64 v[28:29], v4 offset:48640\n"
    "v_exp_f32 v131, v131\n"
    "v_exp_f32 v132, v132\n"
    "v_exp_f32 v133, v133\n"
    "v_mfma_f32_32x32x8_bf16 v[142:157], v[14:15], v[48:49], v[142:157]\n"
    "global_load_dwordx4 a[204:207], v2, s[38:39] offset:3072\n"
    "v_exp_f32 v134, v134\n"
    "v_exp_f32 v135, v135\n"
    "v_exp_f32 v136, v136\n"
    "v_exp_f32 v137, v137\n"
    "v_smfmac_f32_32x32x32_bf16 v[62:77], v[16:19], v[126:133], v158\n"
    "s_add_u32 s38, s38, 8192\n"
    "s_addc_u32 s39, s39, 0\n"
    "global_load_dwordx4 a[208:211], v2, s[38:39] offset:-4096\n"
    "v_exp_f32 v138, v138\n"
    "v_exp_f32 v139, v139\n"
    "v_exp_f32 v140, v140\n"
    "v_exp_f32 v141, v141\n"
    "v_exp_f32 v142, v142\n"
    "v_exp_f32 v143, v143\n"
    "v_smfmac_f32_32x32x32_bf16 v[62:77], v[20:23], v[134:141], v158\n"
    "ds_read_b128 v[30:33], v5 offset:48640\n"
    "v_exp_f32 v144, v144\n"
    "v_exp_f32 v145, v145\n"
    "v_exp_f32 v146, v146\n"
    "v_mfma_f32_32x32x16_bf16 v[126:141], v[10:13], v[50:53], 0\n"
    "s_cmp_lt_u32 s69, 3\n"
    "s_cbranch_scc1 116f\n"
    "s_add_u32 s60, s60, 331776\n"
    "s_addc_u32 s61, s61, 0\n"
    "global_store_dwordx4 v2, v[244:247], s[60:61] sc1\n"
    "116:\n"
    "v_exp_f32 v147, v147\n"
    "v_exp_f32 v148, v148\n"
    "v_exp_f32 v149, v149\n"
    "v_mfma_f32_32x32x8_bf16 v[126:141], v[14:15], v[54:55], v[126:141]\n"
    "v_exp_f32 v150, v150\n"
    "v_exp_f32 v151, v151\n"
    "v_exp_f32 v152, v152\n"
    "v_exp_f32 v153, v153\n"
    "v_smfmac_f32_32x32x32_bf16 v[78:93], v[16:19], v[142:149], v158\n"
    "s_nop 2\n"
    "v_exp_f32 v154, v154\n"
    "v_exp_f32 v155, v155\n"
    "v_exp_f32 v156, v156\n"
    "v_exp_f32 v157, v157\n"
    "v_exp_f32 v126, v126\n"
    "v_exp_f32 v127, v127\n"
    "v_smfmac_f32_32x32x32_bf16 v[78:93], v[20:23], v[150:157], v158\n"
    "v_exp_f32 v128, v128\n"
    "v_exp_f32 v129, v129\n"
    "v_exp_f32 v130, v130\n"
    "v_mfma_f32_32x32x16_bf16 v[142:157], v[10:13], v[56:59], 0\n"
    "v_exp_f32 v131, v131\n"
    "v_exp_f32 v132, v132\n"
    "v_exp_f32 v133, v133\n"
    "v_mfma_f32_32x32x8_bf16 v[142:157], v[14:15], v[60:61], v[142:157]\n"
    "ds_read_b128 v[34:37], v5 offset:49280\n"
    "global_load_dwordx4 a[212:215], v2, s[38:39] offset:-3072\n"
    "v_exp_f32 v134, v134\n"
    "v_exp_f32 v135, v135\n"
    "v_exp_f32 v136, v136\n"
    "v_exp_f32 v137, v137\n"
    "v_smfmac_f32_32x32x32_bf16 v[94:109], v[16:19], v[126:133], v158\n"
    "global_load_dwordx4 a[216:219], v2, s[38:39] offset:-2048\n"
    "v_exp_f32 v138, v138\n"
    "v_exp_f32 v139, v139\n"
    "v_exp_f32 v140, v140\n"
    "v_exp_f32 v141, v141\n"
    "v_exp_f32 v142, v142\n"
    "v_exp_f32 v143, v143\n"
    "v_smfmac_f32_32x32x32_bf16 v[94:109], v[20:23], v[134:141], v158\n"
    "v_exp_f32 v144, v144\n"
    "v_exp_f32 v145, v145\n"
    "v_exp_f32 v146, v146\n"
    "s_waitcnt lgkmcnt(0)\n"
    "v_mfma_f32_32x32x16_bf16 v[126:141], v[24:27], v[38:41], 0\n"
    "s_cmp_lt_u32 s69, 3\n"
    "s_cbranch_scc1 118f\n"
    "s_add_u32 s60, s60, 331776\n"
    "s_addc_u32 s61, s61, 0\n"
    "global_store_dwordx4 v2, v[248:251], s[60:61] sc1\n"
    "118:\n"
    "v_exp_f32 v147, v147\n"
    "v_exp_f32 v148, v148\n"
    "v_exp_f32 v149, v149\n"
    "v_mfma_f32_32x32x8_bf16 v[126:141], v[28:29], v[42:43], v[126:141]\n"
    "v_exp_f32 v150, v150\n"
    "v_exp_f32 v151, v151\n"
    "v_exp_f32 v152, v152\n"
    "v_exp_f32 v153, v153\n"
    "v_smfmac_f32_32x32x32_bf16 v[110:125], v[16:19], v[142:149], v158\n"
    "s_nop 2\n"
    "v_exp_f32 v154, v154\n"
    "v_exp_f32 v155, v155\n"
    "v_exp_f32 v156, v156\n"
    "v_exp_f32 v157, v157\n"
    "v_exp_f32 v126, v126\n"
    "v_exp_f32 v127, v127\n"
    "v_smfmac_f32_32x32x32_bf16 v[110:125], v[20:23], v[150:157], v158\n"
    "v_exp_f32 v128, v128\n"
    "v_exp_f32 v129, v129\n"
    "v_exp_f32 v130, v130\n"
    "v_mfma_f32_32x32x16_bf16 v[142:157], v[24:27], v[44:47], 0\n"
    "v_exp_f32 v131, v131\n"
    "v_exp_f32 v132, v132\n"
    "v_exp_f32 v133, v133\n"
    "v_mfma_f32_32x32x8_bf16 v[142:157], v[28:29], v[48:49], v[142:157]\n"
    "ds_read_b128 v[10:13], v3 offset:51200\n"
    "ds_read_b64 v[14:15], v4 offset:51200\n"
    "v_exp_f32 v134, v134\n"
    "v_exp_f32 v135, v135\n"
    "v_exp_f32 v136, v136\n"
    "v_exp_f32 v137, v137\n"
    "v_smfmac_f32_32x32x32_bf16 v[62:77], v[30:33], v[126:133], v158\n"
    "global_load_dwordx4 a[220:223], v2, s[38:39] offset:-1024\n"
    "global_load_dwordx4 a[224:227], v2, s[38:39] offset:0\n"
    "v_exp_f32 v138, v138\n"
    "v_exp_f32 v139, v139\n"
    "v_exp_f32 v140, v140\n"
    "v_exp_f32 v141, v141\n"
    "v_exp_f32 v142, v142\n"
    "v_exp_f32 v143, v143\n"
    "v_smfmac_f32_32x32x32_bf16 v[62:77], v[34:37], v[134:141], v158\n"
    "ds_read_b128 v[16:19], v5 offset:51200\n"
    "v_exp_f32 v144, v144\n"
    "v_exp_f32 v145, v145\n"
    "v_exp_f32 v146, v146\n"
    "v_mfma_f32_32x32x16_bf16 v[126:141], v[24:27], v[50:53], 0\n"
    "s_cmp_lt_u32 s69, 3\n"
    "s_cbranch_scc1 120f\n"
    "s_add_u32 s60, s60, 331776\n"
    "s_addc_u32 s61, s61, 0\n"
    "global_store_dwordx4 v2, v[252:255], s[60:61] sc1\n"
    "120:\n"
    "v_exp_f32 v147, v147\n"
    "v_exp_f32 v148, v148\n"
    "v_exp_f32 v149, v149\n"
    "v_mfma_f32_32x32x8_bf16 v[126:141], v[28:29], v[54:55], v[126:141]\n"
    "ds_read_b64_tr_b16 v[240:241], v6 offset:0\n"
    "ds_read_b64_tr_b16 v[242:243], v6 offset:2048\n"
    "ds_read_b64_tr_b16 v[244:245], v6 offset:32\n"
    "v_exp_f32 v150, v150\n"
    "v_exp_f32 v151, v151\n"
    "v_exp_f32 v152, v152\n"
    "v_exp_f32 v153, v153\n"
    "v_smfmac_f32_32x32x32_bf16 v[78:93], v[30:33], v[142:149], v158\n"
    "ds_read_b64_tr_b16 v[246:247], v6 offset:2080\n"
    "v_exp_f32 v154, v154\n"
    "v_exp_f32 v155, v155\n"
    "v_exp_f32 v156, v156\n"
    "v_exp_f32 v157, v157\n"
    "v_exp_f32 v126, v126\n"
    "v_exp_f32 v127, v127\n"
    "v_smfmac_f32_32x32x32_bf16 v[78:93], v[34:37], v[150:157], v158\n"
    "ds_read_b128 v[20:23], v5 offset:51840\n"
    "ds_read_b64_tr_b16 v[248:249], v6 offset:64\n"
    "v_exp_f32 v128, v128\n"
    "v_exp_f32 v129, v129\n"
    "v_exp_f32 v130, v130\n"
    "v_mfma_f32_32x32x16_bf16 v[142:157], v[24:27], v[56:59], 0\n"
    "ds_read_b64_tr_b16 v[250:251], v6 offset:2112\n"
    "ds_read_b64_tr_b16 v[252:253], v6 offset:96\n"
    "v_exp_f32 v131, v131\n"
    "v_exp_f32 v132, v132\n"
    "v_exp_f32 v133, v133\n"
    "v_mfma_f32_32x32x8_bf16 v[142:157], v[28:29], v[60:61], v[142:157]\n"
    "ds_read_b64_tr_b16 v[254:255], v6 offset:2144\n"
    "global_load_dwordx4 a[228:231], v2, s[38:39] offset:1024\n"
    "v_exp_f32 v134, v134\n"
    "v_exp_f32 v135, v135\n"
    "v_exp_f32 v136, v136\n"
    "v_exp_f32 v137, v137\n"
    "v_smfmac_f32_32x32x32_bf16 v[94:109], v[30:33], v[126:133], v158\n"
    "global_load_dwordx4 a[232:235], v2, s[38:39] offset:2048\n"
    "v_exp_f32 v138, v138\n"
    "v_exp_f32 v139, v139\n"
    "v_exp_f32 v140, v140\n"
    "v_exp_f32 v141, v141\n"
    "v_exp_f32 v142, v142\n"
    "v_exp_f32 v143, v143\n"
    "v_smfmac_f32_32x32x32_bf16 v[94:109], v[34:37], v[134:141], v158\n"
    "v_exp_f32 v144, v144\n"
    "v_exp_f32 v145, v145\n"
    "v_exp_f32 v146, v146\n"
    "s_waitcnt lgkmcnt(0)\n"
    "v_mfma_f32_32x32x16_bf16 v[126:141], v[10:13], v[38:41], 0\n"
    "v_exp_f32 v147, v147\n"
    "v_exp_f32 v148, v148\n"
    "v_exp_f32 v149, v149\n"
    "v_mfma_f32_32x32x8_bf16 v[126:141], v[14:15], v[42:43], v[126:141]\n"
    "global_store_dwordx4 v2, v[240:243], s[56:57] sc1\n"
    "v_exp_f32 v150, v150\n"
    "v_exp_f32 v151, v151\n"
    "v_exp_f32 v152, v152\n"
    "v_exp_f32 v153, v153\n"
    "v_smfmac_f32_32x32x32_bf16 v[110:125], v[30:33], v[142:149], v158\n"
    "s_nop 1\n"
    "v_exp_f32 v154, v154\n"
    "v_exp_f32 v155, v155\n"
    "v_exp_f32 v156, v156\n"
    "v_exp_f32 v157, v157\n"
    "v_exp_f32 v126, v126\n"
    "v_exp_f32 v127, v127\n"
    "v_smfmac_f32_32x32x32_bf16 v[110:125], v[34:37], v[150:157], v158\n"
    "v_exp_f32 v128, v128\n"
    "v_exp_f32 v129, v129\n"
    "v_exp_f32 v130, v130\n"
    "v_mfma_f32_32x32x16_bf16 v[142:157], v[10:13], v[44:47], 0\n"
    "v_exp_f32 v131, v131\n"
    "v_exp_f32 v132, v132\n"
    "v_exp_f32 v133, v133\n"
    "v_mfma_f32_32x32x8_bf16 v[142:157], v[14:15], v[48:49], v[142:157]\n"
    "ds_read_b128 v[24:27], v3 offset:53760\n"
    "ds_read_b64 v[28:29], v4 offset:53760\n"
    "v_exp_f32 v134, v134\n"
    "v_exp_f32 v135, v135\n"
    "v_exp_f32 v136, v136\n"
    "v_exp_f32 v137, v137\n"
    "v_smfmac_f32_32x32x32_bf16 v[62:77], v[16:19], v[126:133], v158\n"
    "global_load_dwordx4 a[236:239], v2, s[38:39] offset:3072\n"
    "v_exp_f32 v138, v138\n"
    "v_exp_f32 v139, v139\n"
    "v_exp_f32 v140, v140\n"
    "v_exp_f32 v141, v141\n"
    "v_exp_f32 v142, v142\n"
    "v_exp_f32 v143, v143\n"
    "v_smfmac_f32_32x32x32_bf16 v[62:77], v[20:23], v[134:141], v158\n"
    "v_exp_f32 v144, v144\n"
    "v_exp_f32 v145, v145\n"
    "v_exp_f32 v146, v146\n"
    "v_mfma_f32_32x32x16_bf16 v[126:141], v[10:13], v[50:53], 0\n"
    "v_exp_f32 v147, v147\n"
    "v_exp_f32 v148, v148\n"
    "v_exp_f32 v149, v149\n"
    "v_mfma_f32_32x32x8_bf16 v[126:141], v[14:15], v[54:55], v[126:141]\n"
    "ds_read_b128 v[30:33], v5 offset:53760\n"
    "v_exp_f32 v150, v150\n"
    "v_exp_f32 v151, v151\n"
    "v_exp_f32 v152, v152\n"
    "v_exp_f32 v153, v153\n"
    "v_smfmac_f32_32x32x32_bf16 v[78:93], v[16:19], v[142:149], v158\n"
    "s_add_u32 s56, s56, 331776\n"
    "s_addc_u32 s57, s57, 0\n"
    "global_store_dwordx4 v2, v[244:247], s[56:57] sc1\n"
    "v_exp_f32 v154, v154\n"
    "v_exp_f32 v155, v155\n"
    "v_exp_f32 v156, v156\n"
    "v_exp_f32 v157, v157\n"
    "v_exp_f32 v126, v126\n"
    "v_exp_f32 v127, v127\n"
    "v_smfmac_f32_32x32x32_bf16 v[78:93], v[20:23], v[150:157], v158\n"
    "v_exp_f32 v128, v128\n"
    "v_exp_f32 v129, v129\n"
    "v_exp_f32 v130, v130\n"
    "v_mfma_f32_32x32x16_bf16 v[142:157], v[10:13], v[56:59], 0\n"
    "v_exp_f32 v131, v131\n"
    "v_exp_f32 v132, v132\n"
    "v_exp_f32 v133, v133\n"
    "v_mfma_f32_32x32x8_bf16 v[142:157], v[14:15], v[60:61], v[142:157]\n"
    "ds_read_b128 v[34:37], v5 offset:54400\n"
    "v_exp_f32 v134, v134\n"
    "v_exp_f32 v135, v135\n"
    "v_exp_f32 v136, v136\n"
    "v_exp_f32 v137, v137\n"
    "v_smfmac_f32_32x32x32_bf16 v[94:109], v[16:19], v[126:133], v158\n"
    "s_add_u32 s38, s38, 8192\n"
    "s_addc_u32 s39, s39, 0\n"
    "global_load_dwordx4 a[240:243], v2, s[38:39] offset:-4096\n"
    "v_exp_f32 v138, v138\n"
    "v_exp_f32 v139, v139\n"
    "v_exp_f32 v140, v140\n"
    "v_exp_f32 v141, v141\n"
    "v_exp_f32 v142, v142\n"
    "v_exp_f32 v143, v143\n"
    "v_smfmac_f32_32x32x32_bf16 v[94:109], v[20:23], v[134:141], v158\n"
    "v_exp_f32 v144, v144\n"
    "v_exp_f32 v145, v145\n"
    "v_exp_f32 v146, v146\n"
    "s_waitcnt lgkmcnt(0)\n"
    "v_mfma_f32_32x32x16_bf16 v[126:141], v[24:27], v[38:41], 0\n"
    "v_exp_f32 v147, v147\n"
    "v_exp_f32 v148, v148\n"
    "v_exp_f32 v149, v149\n"
    "v_mfma_f32_32x32x8_bf16 v[126:141], v[28:29], v[42:43], v[126:141]\n"
    "s_add_u32 s56, s56, 331776\n"
    "s_addc_u32 s57, s57, 0\n"
    "v_exp_f32 v150, v150\n"
    "v_exp_f32 v151, v151\n"
    "v_exp_f32 v152, v152\n"
    "v_exp_f32 v153, v153\n"
    "v_smfmac_f32_32x32x32_bf16 v[110:125], v[16:19], v[142:149], v158\n"
    "global_store_dwordx4 v2, v[248:251], s[56:57] sc1\n"
    "v_exp_f32 v154, v154\n"
    "v_exp_f32 v155, v155\n"
    "v_exp_f32 v156, v156\n"
    "v_exp_f32 v157, v157\n"
    "v_exp_f32 v126, v126\n"
    "v_exp_f32 v127, v127\n"
    "v_smfmac_f32_32x32x32_bf16 v[110:125], v[20:23], v[150:157], v158\n"
    "v_exp_f32 v128, v128\n"
    "v_exp_f32 v129, v129\n"
    "v_exp_f32 v130, v130\n"
    "v_mfma_f32_32x32x16_bf16 v[142:157], v[24:27], v[44:47], 0\n"
    "v_exp_f32 v131, v131\n"
    "v_exp_f32 v132, v132\n"
    "v_exp_f32 v133, v133\n"
    "v_mfma_f32_32x32x8_bf16 v[142:157], v[28:29], v[48:49], v[142:157]\n"
    "ds_read_b128 v[10:13], v3 offset:56320\n"
    "ds_read_b64 v[14:15], v4 offset:56320\n"
    "v_exp_f32 v134, v134\n"
    "v_exp_f32 v135, v135\n"
    "v_exp_f32 v136, v136\n"
    "v_exp_f32 v137, v137\n"
    "v_smfmac_f32_32x32x32_bf16 v[62:77], v[30:33], v[126:133], v158\n"
    "s_waitcnt vmcnt(34)\n"
    "global_load_dwordx4 a[244:247], v2, s[38:39] offset:-3072\n"
    "v_exp_f32 v138, v138\n"
    "v_exp_f32 v139, v139\n"
    "v_exp_f32 v140, v140\n"
    "v_exp_f32 v141, v141\n"
    "v_exp_f32 v142, v142\n"
    "v_exp_f32 v143, v143\n"
    "v_smfmac_f32_32x32x32_bf16 v[62:77], v[34:37], v[134:141], v158\n"
    "ds_read_b128 v[16:19], v5 offset:56320\n"
    "v_exp_f32 v144, v144\n"
    "v_exp_f32 v145, v145\n"
    "v_exp_f32 v146, v146\n"
    "v_mfma_f32_32x32x16_bf16 v[126:141], v[24:27], v[50:53], 0\n"
    "s_add_u32 s56, s56, 331776\n"
    "s_addc_u32 s57, s57, 0\n"
    "v_exp_f32 v147, v147\n"
    "v_exp_f32 v148, v148\n"
    "v_exp_f32 v149, v149\n"
    "v_mfma_f32_32x32x8_bf16 v[126:141], v[28:29], v[54:55], v[126:141]\n"
    "global_store_dwordx4 v2, v[252:255], s[56:57] sc1\n"
    "ds_read_b64_tr_b16 v[240:241], v6 offset:4096\n"
    "v_exp_f32 v150, v150\n"
    "v_exp_f32 v151, v151\n"
    "v_exp_f32 v152, v152\n"
    "v_exp_f32 v153, v153\n"
    "v_smfmac_f32_32x32x32_bf16 v[78:93], v[30:33], v[142:149], v158\n"
    "ds_read_b64_tr_b16 v[242:243], v6 offset:6144\n"
    "ds_read_b64_tr_b16 v[244:245], v6 offset:4128\n"
    "ds_read_b64_tr_b16 v[246:247], v6 offset:6176\n"
    "v_exp_f32 v154, v154\n"
    "v_exp_f32 v155, v155\n"
    "v_exp_f32 v156, v156\n"
    "v_exp_f32 v157, v157\n"
    "v_exp_f32 v126, v126\n"
    "v_exp_f32 v127, v127\n"
    "v_smfmac_f32_32x32x32_bf16 v[78:93], v[34:37], v[150:157], v158\n"
    "v_exp_f32 v128, v128\n"
    "v_exp_f32 v129, v129\n"
    "v_exp_f32 v130, v130\n"
    "v_mfma_f32_32x32x16_bf16 v[142:157], v[24:27], v[56:59], 0\n"
    "ds_read_b128 v[20:23], v5 offset:56960\n"
    "ds_read_b64_tr_b16 v[248:249], v6 offset:4160\n"
    "v_exp_f32 v131, v131\n"
    "v_exp_f32 v132, v132\n"
    "v_exp_f32 v133, v133\n"
    "v_mfma_f32_32x32x8_bf16 v[142:157], v[28:29], v[60:61], v[142:157]\n"
    "ds_read_b64_tr_b16 v[250:251], v6 offset:6208\n"
    "ds_read_b64_tr_b16 v[252:253], v6 offset:4192\n"
    "v_exp_f32 v134, v134\n"
    "v_exp_f32 v135, v135\n"
    "v_exp_f32 v136, v136\n"
    "v_exp_f32 v137, v137\n"
    "v_smfmac_f32_32x32x32_bf16 v[94:109], v[30:33], v[126:133], v158\n"
    "ds_read_b64_tr_b16 v[254:255], v6 offset:6240\n"
    "global_load_dwordx4 a[248:251], v2, s[38:39] offset:-2048\n"
    "v_exp_f32 v138, v138\n"
    "v_exp_f32 v139, v139\n"
    "v_exp_f32 v140, v140\n"
    "v_exp_f32 v141, v141\n"
    "v_exp_f32 v142, v142\n"
    "v_exp_f32 v143, v143\n"
    "v_smfmac_f32_32x32x32_bf16 v[94:109], v[34:37], v[134:141], v158\n"
    "v_exp_f32 v144, v144\n"
    "v_exp_f32 v145, v145\n"
    "v_exp_f32 v146, v146\n"
    "s_waitcnt lgkmcnt(0)\n"
    "v_mfma_f32_32x32x16_bf16 v[126:141], v[10:13], v[38:41], 0\n"
    "v_exp_f32 v147, v147\n"
    "v_exp_f32 v148, v148\n"
    "v_exp_f32 v149, v149\n"
    "v_mfma_f32_32x32x8_bf16 v[126:141], v[14:15], v[42:43], v[126:141]\n"
    "global_store_dwordx4 v2, v[240:243], s[58:59] sc1\n"
    "v_exp_f32 v150, v150\n"
    "v_exp_f32 v151, v151\n"
    "v_exp_f32 v152, v152\n"
    "v_exp_f32 v153, v153\n"
    "v_smfmac_f32_32x32x32_bf16 v[110:125], v[30:33], v[142:149], v158\n"
    "s_nop 1\n"
    "v_exp_f32 v154, v154\n"
    "v_exp_f32 v155, v155\n"
    "v_exp_f32 v156, v156\n"
    "v_exp_f32 v157, v157\n"
    "v_exp_f32 v126, v126\n"
    "v_exp_f32 v127, v127\n"
    "v_smfmac_f32_32x32x32_bf16 v[110:125], v[34:37], v[150:157], v158\n"
    "v_exp_f32 v128, v128\n"
    "v_exp_f32 v129, v129\n"
    "v_exp_f32 v130, v130\n"
    "v_mfma_f32_32x32x16_bf16 v[142:157], v[10:13], v[44:47], 0\n"
    "v_exp_f32 v131, v131\n"
    "v_exp_f32 v132, v132\n"
    "v_exp_f32 v133, v133\n"
    "v_mfma_f32_32x32x8_bf16 v[142:157], v[14:15], v[48:49], v[142:157]\n"
    "ds_read_b128 v[24:27], v3 offset:58880\n"
    "ds_read_b64 v[28:29], v4 offset:58880\n"
    "v_exp_f32 v134, v134\n"
    "v_exp_f32 v135, v135\n"
    "v_exp_f32 v136, v136\n"
    "v_exp_f32 v137, v137\n"
    "v_smfmac_f32_32x32x32_bf16 v[62:77], v[16:19], v[126:133], v158\n"
    "global_load_dwordx4 a[252:255], v2, s[38:39] offset:-1024\n"
    "v_exp_f32 v138, v138\n"
    "v_exp_f32 v139, v139\n"
    "v_exp_f32 v140, v140\n"
    "v_exp_f32 v141, v141\n"
    "v_exp_f32 v142, v142\n"
    "v_exp_f32 v143, v143\n"
    "v_smfmac_f32_32x32x32_bf16 v[62:77], v[20:23], v[134:141], v158\n"
    "v_exp_f32 v144, v144\n"
    "v_exp_f32 v145, v145\n"
    "v_exp_f32 v146, v146\n"
    "v_mfma_f32_32x32x16_bf16 v[126:141], v[10:13], v[50:53], 0\n"
    "v_exp_f32 v147, v147\n"
    "v_exp_f32 v148, v148\n"
    "v_exp_f32 v149, v149\n"
    "v_mfma_f32_32x32x8_bf16 v[126:141], v[14:15], v[54:55], v[126:141]\n"
    "ds_read_b128 v[30:33], v5 offset:58880\n"
    "v_exp_f32 v150, v150\n"
    "v_exp_f32 v151, v151\n"
    "v_exp_f32 v152, v152\n"
    "v_exp_f32 v153, v153\n"
    "v_smfmac_f32_32x32x32_bf16 v[78:93], v[16:19], v[142:149], v158\n"
    "s_add_u32 s58, s58, 331776\n"
    "s_addc_u32 s59, s59, 0\n"
    "global_store_dwordx4 v2, v[244:247], s[58:59] sc1\n"
    "v_exp_f32 v154, v154\n"
    "v_exp_f32 v155, v155\n"
    "v_exp_f32 v156, v156\n"
    "v_exp_f32 v157, v157\n"
    "v_exp_f32 v126, v126\n"
    "v_exp_f32 v127, v127\n"
    "v_smfmac_f32_32x32x32_bf16 v[78:93], v[20:23], v[150:157], v158\n"
    "v_exp_f32 v128, v128\n"
    "v_exp_f32 v129, v129\n"
    "v_exp_f32 v130, v130\n"
    "v_mfma_f32_32x32x16_bf16 v[142:157], v[10:13], v[56:59], 0\n"
    "v_exp_f32 v131, v131\n"
    "v_exp_f32 v132, v132\n"
    "v_exp_f32 v133, v133\n"
    "v_mfma_f32_32x32x8_bf16 v[142:157], v[14:15], v[60:61], v[142:157]\n"
    "ds_read_b128 v[34:37], v5 offset:59520\n"
    "global_load_dwordx4 v[164:167], v2, s[38:39] offset:0\n"
    "v_exp_f32 v134, v134\n"
    "v_exp_f32 v135, v135\n"
    "v_exp_f32 v136, v136\n"
    "v_exp_f32 v137, v137\n"
    "v_smfmac_f32_32x32x32_bf16 v[94:109], v[16:19], v[126:133], v158\n"
    "s_nop 0\n"
    "v_exp_f32 v138, v138\n"
    "v_exp_f32 v139, v139\n"
    "v_exp_f32 v140, v140\n"
    "v_exp_f32 v141, v141\n"
    "v_exp_f32 v142, v142\n"
    "v_exp_f32 v143, v143\n"
    "v_smfmac_f32_32x32x32_bf16 v[94:109], v[20:23], v[134:141], v158\n"
    "v_exp_f32 v144, v144\n"
    "v_exp_f32 v145, v145\n"
    "v_exp_f32 v146, v146\n"
    "s_waitcnt lgkmcnt(0)\n"
    "v_mfma_f32_32x32x16_bf16 v[126:141], v[24:27], v[38:41], 0\n"
    "v_exp_f32 v147, v147\n"
    "v_exp_f32 v148, v148\n"
    "v_exp_f32 v149, v149\n"
    "v_mfma_f32_32x32x8_bf16 v[126:141], v[28:29], v[42:43], v[126:141]\n"
    "s_add_u32 s58, s58, 331776\n"
    "s_addc_u32 s59, s59, 0\n"
    "v_exp_f32 v150, v150\n"
    "v_exp_f32 v151, v151\n"
    "v_exp_f32 v152, v152\n"
    "v_exp_f32 v153, v153\n"
    "v_smfmac_f32_32x32x32_bf16 v[110:125], v[16:19], v[142:149], v158\n"
    "global_store_dwordx4 v2, v[248:251], s[58:59] sc1\n"
    "v_exp_f32 v154, v154\n"
    "v_exp_f32 v155, v155\n"
    "v_exp_f32 v156, v156\n"
    "v_exp_f32 v157, v157\n"
    "s_barrier\n"
    "v_exp_f32 v126, v126\n"
    "v_exp_f32 v127, v127\n"
    "v_smfmac_f32_32x32x32_bf16 v[110:125], v[20:23], v[150:157], v158\n"
    "v_add_u32 v3, 61440, v3\n"
    "v_add_u32 v4, 61440, v4\n"
    "v_add_u32 v5, 61440, v5\n"
    "v_exp_f32 v128, v128\n"
    "v_exp_f32 v129, v129\n"
    "v_exp_f32 v130, v130\n"
    "v_mfma_f32_32x32x16_bf16 v[142:157], v[24:27], v[44:47], 0\n"
    "ds_read_b128 v[10:13], v3 offset:0\n"
    "ds_read_b64 v[14:15], v4 offset:0\n"
    "s_lshl_b32 s84, s67, 11\n"
    "v_exp_f32 v131, v131\n"
    "v_exp_f32 v132, v132\n"
    "v_exp_f32 v133, v133\n"
    "v_mfma_f32_32x32x8_bf16 v[142:157], v[28:29], v[48:49], v[142:157]\n"
    "s_add_u32 s84, s84, s66\n"
    "s_add_u32 m0, s84, 133120\n"
    "s_nop 0\n"
    "v_exp_f32 v134, v134\n"
    "v_exp_f32 v135, v135\n"
    "v_exp_f32 v136, v136\n"
    "v_exp_f32 v137, v137\n"
    "v_smfmac_f32_32x32x32_bf16 v[62:77], v[30:33], v[126:133], v158\n"
    "global_load_lds_dwordx4 v2, s[42:43] offset:0\n"
    "v_exp_f32 v138, v138\n"
    "v_exp_f32 v139, v139\n"
    "v_exp_f32 v140, v140\n"
    "v_exp_f32 v141, v141\n"
    "v_exp_f32 v142, v142\n"
    "v_exp_f32 v143, v143\n"
    "v_smfmac_f32_32x32x32_bf16 v[62:77], v[34:37], v[134:141], v158\n"
    "v_exp_f32 v144, v144\n"
    "v_exp_f32 v145, v145\n"
    "v_exp_f32 v146, v146\n"
    "v_mfma_f32_32x32x16_bf16 v[126:141], v[24:27], v[50:53], 0\n"
    "ds_read_b128 v[16:19], v5 offset:0\n"
    "v_exp_f32 v147, v147\n"
    "v_exp_f32 v148, v148\n"
    "v_exp_f32 v149, v149\n"
    "v_mfma_f32_32x32x8_bf16 v[126:141], v[28:29], v[54:55], v[126:141]\n"
    "s_add_u32 s58, s58, 331776\n"
    "s_addc_u32 s59, s59, 0\n"
    "v_exp_f32 v150, v150\n"
    "v_exp_f32 v151, v151\n"
    "v_exp_f32 v152, v152\n"
    "v_exp_f32 v153, v153\n"
    "v_smfmac_f32_32x32x32_bf16 v[78:93], v[30:33], v[142:149], v158\n"
    "global_store_dwordx4 v2, v[252:255], s[58:59] sc1\n"
    "global_load_lds_dwordx4 v2, s[42:43] offset:1024\n"
    "v_exp_f32 v154, v154\n"
    "v_exp_f32 v155, v155\n"
    "v_exp_f32 v156, v156\n"
    "v_exp_f32 v157, v157\n"
    "v_exp_f32 v126, v126\n"
    "v_exp_f32 v127, v127\n"
    "v_smfmac_f32_32x32x32_bf16 v[78:93], v[34:37], v[150:157], v158\n"
    "v_exp_f32 v128, v128\n"
    "v_exp_f32 v129, v129\n"
    "v_exp_f32 v130, v130\n"
    "v_mfma_f32_32x32x16_bf16 v[142:157], v[24:27], v[56:59], 0\n"
    "v_exp_f32 v131, v131\n"
    "v_exp_f32 v132, v132\n"
    "v_exp_f32 v133, v133\n"
    "v_mfma_f32_32x32x8_bf16 v[142:157], v[28:29], v[60:61], v[142:157]\n"
    "ds_read_b128 v[20:23], v5 offset:640\n"
    "global_load_dwordx4 v[168:171], v2, s[38:39] offset:1024\n"
    "v_exp_f32 v134, v134\n"
    "v_exp_f32 v135, v135\n"
    "v_exp_f32 v136, v136\n"
    "v_exp_f32 v137, v137\n"
    "v_smfmac_f32_32x32x32_bf16 v[94:109], v[30:33], v[126:133], v158\n"
    "global_load_lds_dwordx4 v2, s[42:43] offset:2048\n"
    "v_exp_f32 v138, v138\n"
    "v_exp_f32 v139, v139\n"
    "v_exp_f32 v140, v140\n"
    "v_exp_f32 v141, v141\n"
    "v_exp_f32 v142, v142\n"
    "v_exp_f32 v143, v143\n"
    "v_smfmac_f32_32x32x32_bf16 v[94:109], v[34:37], v[134:141], v158\n"
    "v_exp_f32 v144, v144\n"
    "v_exp_f32 v145, v145\n"
    "v_exp_f32 v146, v146\n"
    "s_waitcnt lgkmcnt(0)\n"
    "v_mfma_f32_32x32x16_bf16 v[126:141], v[10:13], v[38:41], 0\n"
    "v_exp_f32 v147, v147\n"
    "v_exp_f32 v148, v148\n"
    "v_exp_f32 v149, v149\n"
    "v_mfma_f32_32x32x8_bf16 v[126:141], v[14:15], v[42:43], v[126:141]\n"
    "global_load_dwordx4 v[172:175], v2, s[38:39] offset:2048\n"
    "v_exp_f32 v150, v150\n"
    "v_exp_f32 v151, v151\n"
    "v_exp_f32 v152, v152\n"
    "v_exp_f32 v153, v153\n"
    "v_smfmac_f32_32x32x32_bf16 v[110:125], v[30:33], v[142:149], v158\n"
    "s_mov_b32 m0, s71\n"
    "s_nop 0\n"
    "global_load_lds_dwordx4 v2, s[40:41] offset:0\n"
    "v_exp_f32 v154, v154\n"
    "v_exp_f32 v155, v155\n"
    "v_exp_f32 v156, v156\n"
    "v_exp_f32 v157, v157\n"
    "v_exp_f32 v126, v126\n"
    "v_exp_f32 v127, v127\n"
    "v_smfmac_f32_32x32x32_bf16 v[110:125], v[34:37], v[150:157], v158\n"
    "v_exp_f32 v128, v128\n"
    "v_exp_f32 v129, v129\n"
    "v_exp_f32 v130, v130\n"
    "v_mfma_f32_32x32x16_bf16 v[142:157], v[10:13], v[44:47], 0\n"
    "v_exp_f32 v131, v131\n"
    "v_exp_f32 v132, v132\n"
    "v_exp_f32 v133, v133\n"
    "v_mfma_f32_32x32x8_bf16 v[142:157], v[14:15], v[48:49], v[142:157]\n"
    "ds_read_b128 v[24:27], v3 offset:2560\n"
    "ds_read_b64 v[28:29], v4 offset:2560\n"
    "v_exp_f32 v134, v134\n"
    "v_exp_f32 v135, v135\n"
    "v_exp_f32 v136, v136\n"
    "v_exp_f32 v137, v137\n"
    "v_smfmac_f32_32x32x32_bf16 v[62:77], v[16:19], v[126:133], v158\n"
    "global_load_dwordx4 v[176:179], v2, s[38:39] offset:3072\n"
    "global_load_lds_dwordx4 v2, s[40:41] offset:1024\n"
    "v_exp_f32 v138, v138\n"
    "v_exp_f32 v139, v139\n"
    "v_exp_f32 v140, v140\n"
    "v_exp_f32 v141, v141\n"
    "v_exp_f32 v142, v142\n"
    "v_exp_f32 v143, v143\n"
    "v_smfmac_f32_32x32x32_bf16 v[62:77], v[20:23], v[134:141], v158\n"
    "ds_read_b128 v[30:33], v5 offset:2560\n"
    "global_load_lds_dwordx4 v2, s[40:41] offset:2048\n"
    "v_exp_f32 v144, v144\n"
    "v_exp_f32 v145, v145\n"
    "v_exp_f32 v146, v146\n"
    "v_mfma_f32_32x32x16_bf16 v[126:141], v[10:13], v[50:53], 0\n"
    "s_add_u32 s90, s68, 7\n"
    "s_and_b32 s90, s90, 7\n"
    "v_exp_f32 v147, v147\n"
    "v_exp_f32 v148, v148\n"
    "v_exp_f32 v149, v149\n"
    "v_mfma_f32_32x32x8_bf16 v[126:141], v[14:15], v[54:55], v[126:141]\n"
    "s_lshl_b32 s90, s90, 16\n"
    "v_exp_f32 v150, v150\n"
    "v_exp_f32 v151, v151\n"
    "v_exp_f32 v152, v152\n"
    "v_exp_f32 v153, v153\n"
    "v_smfmac_f32_32x32x32_bf16 v[78:93], v[16:19], v[142:149], v158\n"
    "s_add_u32 s94, s44, s90\n"
    "s_addc_u32 s95, s45, 0\n"
    "global_store_dwordx4 v2, v[160:163], s[94:95] offset:0 sc1\n"
    "v_exp_f32 v154, v154\n"
    "v_exp_f32 v155, v155\n"
    "v_exp_f32 v156, v156\n"
    "v_exp_f32 v157, v157\n"
    "v_exp_f32 v126, v126\n"
    "v_exp_f32 v127, v127\n"
    "v_smfmac_f32_32x32x32_bf16 v[78:93], v[20:23], v[150:157], v158\n"
    "v_exp_f32 v128, v128\n"
    "v_exp_f32 v129, v129\n"
    "v_exp_f32 v130, v130\n"
    "v_mfma_f32_32x32x16_bf16 v[142:157], v[10:13], v[56:59], 0\n"
    "ds_read_b128 v[34:37], v5 offset:3200\n"
    "v_exp_f32 v131, v131\n"
    "v_exp_f32 v132, v132\n"
    "v_exp_f32 v133, v133\n"
    "v_mfma_f32_32x32x8_bf16 v[142:157], v[14:15], v[60:61], v[142:157]\n"
    "s_add_u32 s38, s38, 8192\n"
    "s_addc_u32 s39, s39, 0\n"
    "v_exp_f32 v134, v134\n"
    "v_exp_f32 v135, v135\n"
    "v_exp_f32 v136, v136\n"
    "v_exp_f32 v137, v137\n"
    "v_smfmac_f32_32x32x32_bf16 v[94:109], v[16:19], v[126:133], v158\n"
    "global_load_dwordx4 v[180:183], v2, s[38:39] offset:-4096\n"
    "global_load_lds_dwordx4 v2, s[40:41] offset:3072\n"
    "v_exp_f32 v138, v138\n"
    "v_exp_f32 v139, v139\n"
    "v_exp_f32 v140, v140\n"
    "v_exp_f32 v141, v141\n"
    "v_exp_f32 v142, v142\n"
    "v_exp_f32 v143, v143\n"
    "v_smfmac_f32_32x32x32_bf16 v[94:109], v[20:23], v[134:141], v158\n"
    "v_exp_f32 v144, v144\n"
    "v_exp_f32 v145, v145\n"
    "v_exp_f32 v146, v146\n"
    "s_waitcnt lgkmcnt(0)\n"
    "v_mfma_f32_32x32x16_bf16 v[126:141], v[24:27], v[38:41], 0\n"
    "global_load_dwordx4 v[184:187], v2, s[38:39] offset:-3072\n"
    "s_add_u32 m0, m0, 4096\n"
    "v_exp_f32 v147, v147\n"
    "v_exp_f32 v148, v148\n"
    "v_exp_f32 v149, v149\n"
    "v_mfma_f32_32x32x8_bf16 v[126:141], v[28:29], v[42:43], v[126:141]\n"
    "s_add_u32 s40, s40, 4096\n"
    "s_addc_u32 s41, s41, 0\n"
    "v_exp_f32 v150, v150\n"
    "v_exp_f32 v151, v151\n"
    "v_exp_f32 v152, v152\n"
    "v_exp_f32 v153, v153\n"
    "v_smfmac_f32_32x32x32_bf16 v[110:125], v[16:19], v[142:149], v158\n"
    "global_load_lds_dwordx4 v2, s[40:41] offset:0\n"
    "v_exp_f32 v154, v154\n"
    "v_exp_f32 v155, v155\n"
    "v_exp_f32 v156, v156\n"
    "v_exp_f32 v157, v157\n"
    "v_exp_f32 v126, v126\n"
    "v_exp_f32 v127, v127\n"
    "v_smfmac_f32_32x32x32_bf16 v[110:125], v[20:23], v[150:157], v158\n"
    "v_exp_f32 v128, v128\n"
    "v_exp_f32 v129, v129\n"
    "v_exp_f32 v130, v130\n"
    "v_mfma_f32_32x32x16_bf16 v[142:157], v[24:27], v[44:47], 0\n"
    "v_exp_f32 v131, v131\n"
    "v_exp_f32 v132, v132\n"
    "v_exp_f32 v133, v133\n"
    "v_mfma_f32_32x32x8_bf16 v[142:157], v[28:29], v[48:49], v[142:157]\n"
    "ds_read_b128 v[10:13], v3 offset:5120\n"
    "ds_read_b64 v[14:15], v4 offset:5120\n"
    "v_exp_f32 v134, v134\n"
    "v_exp_f32 v135, v135\n"
    "v_exp_f32 v136, v136\n"
    "v_exp_f32 v137, v137\n"
    "v_smfmac_f32_32x32x32_bf16 v[62:77], v[30:33], v[126:133], v158\n"
    "global_load_dwordx4 v[188:191], v2, s[38:39] offset:-2048\n"
    "global_load_lds_dwordx4 v2, s[40:41] offset:1024\n"
    "v_exp_f32 v138, v138\n"
    "v_exp_f32 v139, v139\n"
    "v_exp_f32 v140, v140\n"
    "v_exp_f32 v141, v141\n"
    "v_exp_f32 v142, v142\n"
    "v_exp_f32 v143, v143\n"
    "v_smfmac_f32_32x32x32_bf16 v[62:77], v[34:37], v[134:141], v158\n"
    "v_exp_f32 v144, v144\n"
    "v_exp_f32 v145, v145\n"
    "v_exp_f32 v146, v146\n"
    "v_mfma_f32_32x32x16_bf16 v[126:141], v[24:27], v[50:53], 0\n"
    "v_exp_f32 v147, v147\n"
    "v_exp_f32 v148, v148\n"
    "v_exp_f32 v149, v149\n"
    "v_mfma_f32_32x32x8_bf16 v[126:141], v[28:29], v[54:55], v[126:141]\n"
    "ds_read_b128 v[16:19], v5 offset:5120\n"
    "global_load_lds_dwordx4 v2, s[40:41] offset:2048\n"
    "v_exp_f32 v150, v150\n"
    "v_exp_f32 v151, v151\n"
    "v_exp_f32 v152, v152\n"
    "v_exp_f32 v153, v153\n"
    "v_smfmac_f32_32x32x32_bf16 v[78:93], v[30:33], v[142:149], v158\n"
    "global_store_dwordx4 v2, v[160:163], s[94:95] offset:1024 sc1\n"
    "v_exp_f32 v154, v154\n"
    "v_exp_f32 v155, v155\n"
    "v_exp_f32 v156, v156\n"
    "v_exp_f32 v157, v157\n"
    "v_exp_f32 v126, v126\n"
    "v_exp_f32 v127, v127\n"
    "v_smfmac_f32_32x32x32_bf16 v[78:93], v[34:37], v[150:157], v158\n"
    "v_exp_f32 v128, v128\n"
    "v_exp_f32 v129, v129\n"
    "v_exp_f32 v130, v130\n"
    "v_mfma_f32_32x32x16_bf16 v[142:157], v[24:27], v[56:59], 0\n"
    "v_exp_f32 v131, v131\n"
    "v_exp_f32 v132, v132\n"
    "v_exp_f32 v133, v133\n"
    "v_mfma_f32_32x32x8_bf16 v[142:157], v[28:29], v[60:61], v[142:157]\n"
    "ds_read_b128 v[20:23], v5 offset:5760\n"
    "global_load_dwordx4 v[192:195], v2, s[38:39] offset:-1024\n"
    "v_exp_f32 v134, v134\n"
    "v_exp_f32 v135, v135\n"
    "v_exp_f32 v136, v136\n"
    "v_exp_f32 v137, v137\n"
    "v_smfmac_f32_32x32x32_bf16 v[94:109], v[30:33], v[126:133], v158\n"
    "global_load_lds_dwordx4 v2, s[40:41] offset:3072\n"
    "v_exp_f32 v138, v138\n"
    "v_exp_f32 v139, v139\n"
    "v_exp_f32 v140, v140\n"
    "v_exp_f32 v141, v141\n"
    "v_exp_f32 v142, v142\n"
    "v_exp_f32 v143, v143\n"
    "v_smfmac_f32_32x32x32_bf16 v[94:109], v[34:37], v[134:141], v158\n"
    "v_exp_f32 v144, v144\n"
    "v_exp_f32 v145, v145\n"
    "v_exp_f32 v146, v146\n"
    "s_waitcnt lgkmcnt(0)\n"
    "v_mfma_f32_32x32x16_bf16 v[126:141], v[10:13], v[38:41], 0\n"
    "global_load_dwordx4 v[196:199], v2, s[38:39] offset:0\n"
    "s_add_u32 m0, m0, 4096\n"
    "v_exp_f32 v147, v147\n"
    "v_exp_f32 v148, v148\n"
    "v_exp_f32 v149, v149\n"
    "v_mfma_f32_32x32x8_bf16 v[126:141], v[14:15], v[42:43], v[126:141]\n"
    "s_add_u32 s40, s40, 4096\n"
    "s_addc_u32 s41, s41, 0\n"
    "v_exp_f32 v150, v150\n"
    "v_exp_f32 v151, v151\n"
    "v_exp_f32 v152, v152\n"
    "v_exp_f32 v153, v153\n"
    "v_smfmac_f32_32x32x32_bf16 v[110:125], v[30:33], v[142:149], v158\n"
    "global_load_lds_dwordx4 v2, s[40:41] offset:0\n"
    "v_exp_f32 v154, v154\n"
    "v_exp_f32 v155, v155\n"
    "v_exp_f32 v156, v156\n"
    "v_exp_f32 v157, v157\n"
    "v_exp_f32 v126, v126\n"
    "v_exp_f32 v127, v127\n"
    "v_smfmac_f32_32x32x32_bf16 v[110:125], v[34:37], v[150:157], v158\n"
    "v_exp_f32 v128, v128\n"
    "v_exp_f32 v129, v129\n"
    "v_exp_f32 v130, v130\n"
    "v_mfma_f32_32x32x16_bf16 v[142:157], v[10:13], v[44:47], 0\n"
    "v_exp_f32 v131, v131\n"
    "v_exp_f32 v132, v132\n"
    "v_exp_f32 v133, v133\n"
    "v_mfma_f32_32x32x8_bf16 v[142:157], v[14:15], v[48:49], v[142:157]\n"
    "ds_read_b128 v[24:27], v3 offset:7680\n"
    "ds_read_b64 v[28:29], v4 offset:7680\n"
    "v_exp_f32 v134, v134\n"
    "v_exp_f32 v135, v135\n"
    "v_exp_f32 v136, v136\n"
    "v_exp_f32 v137, v137\n"
    "v_smfmac_f32_32x32x32_bf16 v[62:77], v[16:19], v[126:133], v158\n"
    "global_load_dwordx4 v[200:203], v2, s[38:39] offset:1024\n"
    "global_load_lds_dwordx4 v2, s[40:41] offset:1024\n"
    "v_exp_f32 v138, v138\n"
    "v_exp_f32 v139, v139\n"
    "v_exp_f32 v140, v140\n"
    "v_exp_f32 v141, v141\n"
    "v_exp_f32 v142, v142\n"
    "v_exp_f32 v143, v143\n"
    "v_smfmac_f32_32x32x32_bf16 v[62:77], v[20:23], v[134:141], v158\n"
    "v_exp_f32 v144, v144\n"
    "v_exp_f32 v145, v145\n"
    "v_exp_f32 v146, v146\n"
    "v_mfma_f32_32x32x16_bf16 v[126:141], v[10:13], v[50:53], 0\n"
    "v_exp_f32 v147, v147\n"
    "v_exp_f32 v148, v148\n"
    "v_exp_f32 v149, v149\n"
    "v_mfma_f32_32x32x8_bf16 v[126:141], v[14:15], v[54:55], v[126:141]\n"
    "ds_read_b128 v[30:33], v5 offset:7680\n"
    "global_load_lds_dwordx4 v2, s[40:41] offset:2048\n"
    "v_exp_f32 v150, v150\n"
    "v_exp_f32 v151, v151\n"
    "v_exp_f32 v152, v152\n"
    "v_exp_f32 v153, v153\n"
    "v_smfmac_f32_32x32x32_bf16 v[78:93], v[16:19], v[142:149], v158\n"
    "global_store_dwordx4 v2, v[160:163], s[46:47] sc1\n"
    "v_exp_f32 v154, v154\n"
    "v_exp_f32 v155, v155\n"
    "v_exp_f32 v156, v156\n"
    "v_exp_f32 v157, v157\n"
    "v_exp_f32 v126, v126\n"
    "v_exp_f32 v127, v127\n"
    "v_smfmac_f32_32x32x32_bf16 v[78:93], v[20:23], v[150:157], v158\n"
    "v_exp_f32 v128, v128\n"
    "v_exp_f32 v129, v129\n"
    "v_exp_f32 v130, v130\n"
    "v_mfma_f32_32x32x16_bf16 v[142:157], v[10:13], v[56:59], 0\n"
    "v_exp_f32 v131, v131\n"
    "v_exp_f32 v132, v132\n"
    "v_exp_f32 v133, v133\n"
    "v_mfma_f32_32x32x8_bf16 v[142:157], v[14:15], v[60:61], v[142:157]\n"
    "ds_read_b128 v[34:37], v5 offset:8320\n"
    "global_load_dwordx4 v[204:207], v2, s[38:39] offset:2048\n"
    "v_exp_f32 v134, v134\n"
    "v_exp_f32 v135, v135\n"
    "v_exp_f32 v136, v136\n"
    "v_exp_f32 v137, v137\n"
    "v_smfmac_f32_32x32x32_bf16 v[94:109], v[16:19], v[126:133], v158\n"
    "global_load_lds_dwordx4 v2, s[40:41] offset:3072\n"
    "v_exp_f32 v138, v138\n"
    "v_exp_f32 v139, v139\n"
    "v_exp_f32 v140, v140\n"
    "v_exp_f32 v141, v141\n"
    "v_exp_f32 v142, v142\n"
    "v_exp_f32 v143, v143\n"
    "v_smfmac_f32_32x32x32_bf16 v[94:109], v[20:23], v[134:141], v158\n"
    "v_exp_f32 v144, v144\n"
    "v_exp_f32 v145, v145\n"
    "v_exp_f32 v146, v146\n"
    "s_waitcnt lgkmcnt(0)\n"
    "v_mfma_f32_32x32x16_bf16 v[126:141], v[24:27], v[38:41], 0\n"
    "global_load_dwordx4 v[208:211], v2, s[38:39] offset:3072\n"
    "s_add_u32 m0, m0, 4096\n"
    "v_exp_f32 v147, v147\n"
    "v_exp_f32 v148, v148\n"
    "v_exp_f32 v149, v149\n"
    "v_mfma_f32_32x32x8_bf16 v[126:141], v[28:29], v[42:43], v[126:141]\n"
    "s_add_u32 s40, s40, 4096\n"
    "s_addc_u32 s41, s41, 0\n"
    "v_exp_f32 v150, v150\n"
    "v_exp_f32 v151, v151\n"
    "v_exp_f32 v152, v152\n"
    "v_exp_f32 v153, v153\n"
    "v_smfmac_f32_32x32x32_bf16 v[110:125], v[16:19], v[142:149], v158\n"
    "global_load_lds_dwordx4 v2, s[40:41] offset:0\n"
    "v_exp_f32 v154, v154\n"
    "v_exp_f32 v155, v155\n"
    "v_exp_f32 v156, v156\n"
    "v_exp_f32 v157, v157\n"
    "v_exp_f32 v126, v126\n"
    "v_exp_f32 v127, v127\n"
    "v_smfmac_f32_32x32x32_bf16 v[110:125], v[20:23], v[150:157], v158\n"
    "v_exp_f32 v128, v128\n"
    "v_exp_f32 v129, v129\n"
    "v_exp_f32 v130, v130\n"
    "v_mfma_f32_32x32x16_bf16 v[142:157], v[24:27], v[44:47], 0\n"
    "ds_read_b128 v[10:13], v3 offset:10240\n"
    "ds_read_b64 v[14:15], v4 offset:10240\n"
    "v_exp_f32 v131, v131\n"
    "v_exp_f32 v132, v132\n"
    "v_exp_f32 v133, v133\n"
    "v_mfma_f32_32x32x8_bf16 v[142:157], v[28:29], v[48:49], v[142:157]\n"
    "s_add_u32 s38, s38, 8192\n"
    "s_addc_u32 s39, s39, 0\n"
    "v_exp_f32 v134, v134\n"
    "v_exp_f32 v135, v135\n"
    "v_exp_f32 v136, v136\n"
    "v_exp_f32 v137, v137\n"
    "v_smfmac_f32_32x32x32_bf16 v[62:77], v[30:33], v[126:133], v158\n"
    "global_load_dwordx4 v[212:215], v2, s[38:39] offset:-4096\n"
    "global_load_lds_dwordx4 v2, s[40:41] offset:1024\n"
    "v_exp_f32 v138, v138\n"
    "v_exp_f32 v139, v139\n"
    "v_exp_f32 v140, v140\n"
    "v_exp_f32 v141, v141\n"
    "v_exp_f32 v142, v142\n"
    "v_exp_f32 v143, v143\n"
    "v_smfmac_f32_32x32x32_bf16 v[62:77], v[34:37], v[134:141], v158\n"
    "v_exp_f32 v144, v144\n"
    "v_exp_f32 v145, v145\n"
    "v_exp_f32 v146, v146\n"
    "v_mfma_f32_32x32x16_bf16 v[126:141], v[24:27], v[50:53], 0\n"
    "ds_read_b128 v[16:19], v5 offset:10240\n"
    "global_load_lds_dwordx4 v2, s[40:41] offset:2048\n"
    "v_exp_f32 v147, v147\n"
    "v_exp_f32 v148, v148\n"
    "v_exp_f32 v149, v149\n"
    "v_mfma_f32_32x32x8_bf16 v[126:141], v[28:29], v[54:55], v[126:141]\n"
    "s_add_u32 s94, s46, 8192\n"
    "s_addc_u32 s95, s47, 0\n"
    "v_exp_f32 v150, v150\n"
    "v_exp_f32 v151, v151\n"
    "v_exp_f32 v152, v152\n"
    "v_exp_f32 v153, v153\n"
    "v_smfmac_f32_32x32x32_bf16 v[78:93], v[30:33], v[142:149], v158\n"
    "global_store_dwordx4 v2, v[160:163], s[94:95] sc1\n"
    "v_exp_f32 v154, v154\n"
    "v_exp_f32 v155, v155\n"
    "v_exp_f32 v156, v156\n"
    "v_exp_f32 v157, v157\n"
    "v_exp_f32 v126, v126\n"
    "v_exp_f32 v127, v127\n"
    "v_smfmac_f32_32x32x32_bf16 v[78:93], v[34:37], v[150:157], v158\n"
    "v_exp_f32 v128, v128\n"
    "v_exp_f32 v129, v129\n"
    "v_exp_f32 v130, v130\n"
    "v_mfma_f32_32x32x16_bf16 v[142:157], v[24:27], v[56:59], 0\n"
    "v_exp_f32 v131, v131\n"
    "v_exp_f32 v132, v132\n"
    "v_exp_f32 v133, v133\n"
    "v_mfma_f32_32x32x8_bf16 v[142:157], v[28:29], v[60:61], v[142:157]\n"
    "ds_read_b128 v[20:23], v5 offset:10880\n"
    "global_load_dwordx4 v[216:219], v2, s[38:39] offset:-3072\n"
    "v_exp_f32 v134, v134\n"
    "v_exp_f32 v135, v135\n"
    "v_exp_f32 v136, v136\n"
    "v_exp_f32 v137, v137\n"
    "v_smfmac_f32_32x32x32_bf16 v[94:109], v[30:33], v[126:133], v158\n"
    "global_load_lds_dwordx4 v2, s[40:41] offset:3072\n"
    "v_exp_f32 v138, v138\n"
    "v_exp_f32 v139, v139\n"
    "v_exp_f32 v140, v140\n"
    "v_exp_f32 v141, v141\n"
    "v_exp_f32 v142, v142\n"
    "v_exp_f32 v143, v143\n"
    "v_smfmac_f32_32x32x32_bf16 v[94:109], v[34:37], v[134:141], v158\n"
    "v_exp_f32 v144, v144\n"
    "v_exp_f32 v145, v145\n"
    "v_exp_f32 v146, v146\n"
    "s_waitcnt lgkmcnt(0)\n"
    "v_mfma_f32_32x32x16_bf16 v[126:141], v[10:13], v[38:41], 0\n"
    "global_load_dwordx4 v[220:223], v2, s[38:39] offset:-2048\n"
    "s_add_u32 m0, m0, 4096\n"
    "v_exp_f32 v147, v147\n"
    "v_exp_f32 v148, v148\n"
    "v_exp_f32 v149, v149\n"
    "v_mfma_f32_32x32x8_bf16 v[126:141], v[14:15], v[42:43], v[126:141]\n"
    "s_add_u32 s40, s40, 4096\n"
    "s_addc_u32 s41, s41, 0\n"
    "v_exp_f32 v150, v150\n"
    "v_exp_f32 v151, v151\n"
    "v_exp_f32 v152, v152\n"
    "v_exp_f32 v153, v153\n"
    "v_smfmac_f32_32x32x32_bf16 v[110:125], v[30:33], v[142:149], v158\n"
    "global_load_lds_dwordx4 v2, s[40:41] offset:0\n"
    "v_exp_f32 v154, v154\n"
    "v_exp_f32 v155, v155\n"
    "v_exp_f32 v156, v156\n"
    "v_exp_f32 v157, v157\n"
    "v_exp_f32 v126, v126\n"
    "v_exp_f32 v127, v127\n"
    "v_smfmac_f32_32x32x32_bf16 v[110:125], v[34:37], v[150:157], v158\n"
    "v_exp_f32 v128, v128\n"
    "v_exp_f32 v129, v129\n"
    "v_exp_f32 v130, v130\n"
    "v_mfma_f32_32x32x16_bf16 v[142:157], v[10:13], v[44:47], 0\n"
    "v_exp_f32 v131, v131\n"
    "v_exp_f32 v132, v132\n"
    "v_exp_f32 v133, v133\n"
    "v_mfma_f32_32x32x8_bf16 v[142:157], v[14:15], v[48:49], v[142:157]\n"
    "ds_read_b128 v[24:27], v3 offset:12800\n"
    "ds_read_b64 v[28:29], v4 offset:12800\n"
    "v_exp_f32 v134, v134\n"
    "v_exp_f32 v135, v135\n"
    "v_exp_f32 v136, v136\n"
    "v_exp_f32 v137, v137\n"
    "v_smfmac_f32_32x32x32_bf16 v[62:77], v[16:19], v[126:133], v158\n"
    "global_load_dwordx4 v[224:227], v2, s[38:39] offset:-1024\n"
    "global_load_lds_dwordx4 v2, s[40:41] offset:1024\n"
    "v_exp_f32 v138, v138\n"
    "v_exp_f32 v139, v139\n"
    "v_exp_f32 v140, v140\n"
    "v_exp_f32 v141, v141\n"
    "v_exp_f32 v142, v142\n"
    "v_exp_f32 v143, v143\n"
    "v_smfmac_f32_32x32x32_bf16 v[62:77], v[20:23], v[134:141], v158\n"
    "v_exp_f32 v144, v144\n"
    "v_exp_f32 v145, v145\n"
    "v_exp_f32 v146, v146\n"
    "v_mfma_f32_32x32x16_bf16 v[126:141], v[10:13], v[50:53], 0\n"
    "v_exp_f32 v147, v147\n"
    "v_exp_f32 v148, v148\n"
    "v_exp_f32 v149, v149\n"
    "v_mfma_f32_32x32x8_bf16 v[126:141], v[14:15], v[54:55], v[126:141]\n"
    "ds_read_b128 v[30:33], v5 offset:12800\n"
    "v_exp_f32 v150, v150\n"
    "v_exp_f32 v151, v151\n"
    "v_exp_f32 v152, v152\n"
    "v_exp_f32 v153, v153\n"
    "v_smfmac_f32_32x32x32_bf16 v[78:93], v[16:19], v[142:149], v158\n"
    "s_nop 1\n"
    "v_exp_f32 v154, v154\n"
    "v_exp_f32 v155, v155\n"
    "v_exp_f32 v156, v156\n"
    "v_exp_f32 v157, v157\n"
    "v_exp_f32 v126, v126\n"
    "v_exp_f32 v127, v127\n"
    "v_smfmac_f32_32x32x32_bf16 v[78:93], v[20:23], v[150:157], v158\n"
    "v_exp_f32 v128, v128\n"
    "v_exp_f32 v129, v129\n"
    "v_exp_f32 v130, v130\n"
    "v_mfma_f32_32x32x16_bf16 v[142:157], v[10:13], v[56:59], 0\n"
    "v_exp_f32 v131, v131\n"
    "v_exp_f32 v132, v132\n"
    "v_exp_f32 v133, v133\n"
    "v_mfma_f32_32x32x8_bf16 v[142:157], v[14:15], v[60:61], v[142:157]\n"
    "ds_read_b128 v[34:37], v5 offset:13440\n"
    "v_exp_f32 v134, v134\n"
    "v_exp_f32 v135, v135\n"
    "v_exp_f32 v136, v136\n"
    "v_exp_f32 v137, v137\n"
    "v_smfmac_f32_32x32x32_bf16 v[94:109], v[16:19], v[126:133], v158\n"
    "s_nop 1\n"
    "v_exp_f32 v138, v138\n"
    "v_exp_f32 v139, v139\n"
    "v_exp_f32 v140, v140\n"
    "v_exp_f32 v141, v141\n"
    "v_exp_f32 v142, v142\n"
    "v_exp_f32 v143, v143\n"
    "v_smfmac_f32_32x32x32_bf16 v[94:109], v[20:23], v[134:141], v158\n"
    "v_exp_f32 v144, v144\n"
    "v_exp_f32 v145, v145\n"
    "v_exp_f32 v146, v146\n"
    "s_waitcnt lgkmcnt(0)\n"
    "v_mfma_f32_32x32x16_bf16 v[126:141], v[24:27], v[38:41], 0\n"
    "v_exp_f32 v147, v147\n"
    "v_exp_f32 v148, v148\n"
    "v_exp_f32 v149, v149\n"
    "v_mfma_f32_32x32x8_bf16 v[126:141], v[28:29], v[42:43], v[126:141]\n"
    "v_exp_f32 v150, v150\n"
    "v_exp_f32 v151, v151\n"
    "v_exp_f32 v152, v152\n"
    "v_exp_f32 v153, v153\n"
    "v_smfmac_f32_32x32x32_bf16 v[110:125], v[16:19], v[142:149], v158\n"
    "s_nop 2\n"
    "v_exp_f32 v154, v154\n"
    "v_exp_f32 v155, v155\n"
    "v_exp_f32 v156, v156\n"
    "v_exp_f32 v157, v157\n"
    "v_exp_f32 v126, v126\n"
    "v_exp_f32 v127, v127\n"
    "v_smfmac_f32_32x32x32_bf16 v[110:125], v[20:23], v[150:157], v158\n"
    "ds_read_b128 v[10:13], v3 offset:15360\n"
    "ds_read_b64 v[14:15], v4 offset:15360\n"
    "v_exp_f32 v128, v128\n"
    "v_exp_f32 v129, v129\n"
    "v_exp_f32 v130, v130\n"
    "v_mfma_f32_32x32x16_bf16 v[142:157], v[24:27], v[44:47], 0\n"
    "s_waitcnt vmcnt(0)\n"
    "v_mov_b32 v159, 1\n"
    "s_mov_b64 exec, 1\n"
    "global_atomic_add v2, v159, s[48:49]\n"
    "s_mov_b64 exec, -1\n"
    "v_exp_f32 v131, v131\n"
    "v_exp_f32 v132, v132\n"
    "v_exp_f32 v133, v133\n"
    "v_mfma_f32_32x32x8_bf16 v[142:157], v[28:29], v[48:49], v[142:157]\n"
    "v_exp_f32 v134, v134\n"
    "v_exp_f32 v135, v135\n"
    "v_exp_f32 v136, v136\n"
    "v_exp_f32 v137, v137\n"
    "v_smfmac_f32_32x32x32_bf16 v[62:77], v[30:33], v[126:133], v158\n"
    "s_nop 2\n"
    "v_exp_f32 v138, v138\n"
    "v_exp_f32 v139, v139\n"
    "v_exp_f32 v140, v140\n"
    "v_exp_f32 v141, v141\n"
    "v_exp_f32 v142, v142\n"
    "v_exp_f32 v143, v143\n"
    "v_smfmac_f32_32x32x32_bf16 v[62:77], v[34:37], v[134:141], v158\n"
    "v_exp_f32 v144, v144\n"
    "v_exp_f32 v145, v145\n"
    "v_exp_f32 v146, v146\n"
    "v_mfma_f32_32x32x16_bf16 v[126:141], v[24:27], v[50:53], 0\n"
    "v_exp_f32 v147, v147\n"
    "v_exp_f32 v148, v148\n"
    "v_exp_f32 v149, v149\n"
    "v_mfma_f32_32x32x8_bf16 v[126:141], v[28:29], v[54:55], v[126:141]\n"
    "ds_read_b128 v[16:19], v5 offset:15360\n"
    "global_load_dwordx4 v[228:231], v2, s[38:39] offset:0\n"
    "v_exp_f32 v150, v150\n"
    "v_exp_f32 v151, v151\n"
    "v_exp_f32 v152, v152\n"
    "v_exp_f32 v153, v153\n"
    "v_smfmac_f32_32x32x32_bf16 v[78:93], v[30:33], v[142:149], v158\n"
    "global_load_lds_dwordx4 v2, s[40:41] offset:2048\n"
    "v_exp_f32 v154, v154\n"
    "v_exp_f32 v155, v155\n"
    "v_exp_f32 v156, v156\n"
    "v_exp_f32 v157, v157\n"
    "v_exp_f32 v126, v126\n"
    "v_exp_f32 v127, v127\n"
    "v_smfmac_f32_32x32x32_bf16 v[78:93], v[34:37], v[150:157], v158\n"
    "v_exp_f32 v128, v128\n"
    "v_exp_f32 v129, v129\n"
    "v_exp_f32 v130, v130\n"
    "v_mfma_f32_32x32x16_bf16 v[142:157], v[24:27], v[56:59], 0\n"
    "v_exp_f32 v131, v131\n"
    "v_exp_f32 v132, v132\n"
    "v_exp_f32 v133, v133\n"
    "v_mfma_f32_32x32x8_bf16 v[142:157], v[28:29], v[60:61], v[142:157]\n"
    "ds_read_b128 v[20:23], v5 offset:16000\n"
    "global_load_dwordx4 v[232:235], v2, s[38:39] offset:1024\n"
    "v_exp_f32 v134, v134\n"
    "v_exp_f32 v135, v135\n"
    "v_exp_f32 v136, v136\n"
    "v_exp_f32 v137, v137\n"
    "v_smfmac_f32_32x32x32_bf16 v[94:109], v[30:33], v[126:133], v158\n"
    "global_load_lds_dwordx4 v2, s[40:41] offset:3072\n"
    "v_exp_f32 v138, v138\n"
    "v_exp_f32 v139, v139\n"
    "v_exp_f32 v140, v140\n"
    "v_exp_f32 v141, v141\n"
    "v_exp_f32 v142, v142\n"
    "v_exp_f32 v143, v143\n"
    "v_smfmac_f32_32x32x32_bf16 v[94:109], v[34:37], v[134:141], v158\n"
    "v_exp_f32 v144, v144\n"
    "v_exp_f32 v145, v145\n"
    "v_exp_f32 v146, v146\n"
    "s_waitcnt lgkmcnt(0)\n"
    "v_mfma_f32_32x32x16_bf16 v[126:141], v[10:13], v[38:41], 0\n"
    "global_load_dwordx4 v[236:239], v2, s[38:39] offset:2048\n"
    "s_add_u32 m0, m0, 4096\n"
    "v_exp_f32 v147, v147\n"
    "v_exp_f32 v148, v148\n"
    "v_exp_f32 v149, v149\n"
    "v_mfma_f32_32x32x8_bf16 v[126:141], v[14:15], v[42:43], v[126:141]\n"
    "s_add_u32 s40, s40, 4096\n"
    "s_addc_u32 s41, s41, 0\n"
    "v_exp_f32 v150, v150\n"
    "v_exp_f32 v151, v151\n"
    "v_exp_f32 v152, v152\n"
    "v_exp_f32 v153, v153\n"
    "v_smfmac_f32_32x32x32_bf16 v[110:125], v[30:33], v[142:149], v158\n"
    "global_load_lds_dwordx4 v2, s[40:41] offset:0\n"
    "v_exp_f32 v154, v154\n"
    "v_exp_f32 v155, v155\n"
    "v_exp_f32 v156, v156\n"
    "v_exp_f32 v157, v157\n"
    "v_exp_f32 v126, v126\n"
    "v_exp_f32 v127, v127\n"
    "v_smfmac_f32_32x32x32_bf16 v[110:125], v[34:37], v[150:157], v158\n"
    "v_exp_f32 v128, v128\n"
    "v_exp_f32 v129, v129\n"
    "v_exp_f32 v130, v130\n"
    "v_mfma_f32_32x32x16_bf16 v[142:157], v[10:13], v[44:47], 0\n"
    "v_exp_f32 v131, v131\n"
    "v_exp_f32 v132, v132\n"
    "v_exp_f32 v133, v133\n"
    "v_mfma_f32_32x32x8_bf16 v[142:157], v[14:15], v[48:49], v[142:157]\n"
    "ds_read_b128 v[24:27], v3 offset:17920\n"
    "ds_read_b64 v[28:29], v4 offset:17920\n"
    "v_exp_f32 v134, v134\n"
    "v_exp_f32 v135, v135\n"
    "v_exp_f32 v136, v136\n"
    "v_exp_f32 v137, v137\n"
    "v_smfmac_f32_32x32x32_bf16 v[62:77], v[16:19], v[126:133], v158\n"
    "global_load_dwordx4 v[240:243], v2, s[38:39] offset:3072\n"
    "global_load_lds_dwordx4 v2, s[40:41] offset:1024\n"
    "v_exp_f32 v138, v138\n"
    "v_exp_f32 v139, v139\n"
    "v_exp_f32 v140, v140\n"
    "v_exp_f32 v141, v141\n"
    "v_exp_f32 v142, v142\n"
    "v_exp_f32 v143, v143\n"
    "v_smfmac_f32_32x32x32_bf16 v[62:77], v[20:23], v[134:141], v158\n"
    "v_exp_f32 v144, v144\n"
    "v_exp_f32 v145, v145\n"
    "v_exp_f32 v146, v146\n"
    "v_mfma_f32_32x32x16_bf16 v[126:141], v[10:13], v[50:53], 0\n"
    "ds_read_b128 v[30:33], v5 offset:17920\n"
    "v_exp_f32 v147, v147\n"
    "v_exp_f32 v148, v148\n"
    "v_exp_f32 v149, v149\n"
    "v_mfma_f32_32x32x8_bf16 v[126:141], v[14:15], v[54:55], v[126:141]\n"
    "s_add_u32 s38, s38, 8192\n"
    "s_addc_u32 s39, s39, 0\n"
    "v_exp_f32 v150, v150\n"
    "v_exp_f32 v151, v151\n"
    "v_exp_f32 v152, v152\n"
    "v_exp_f32 v153, v153\n"
    "v_smfmac_f32_32x32x32_bf16 v[78:93], v[16:19], v[142:149], v158\n"
    "global_load_dwordx4 v[244:247], v2, s[38:39] offset:-4096\n"
    "global_load_lds_dwordx4 v2, s[40:41] offset:2048\n"
    "v_exp_f32 v154, v154\n"
    "v_exp_f32 v155, v155\n"
    "v_exp_f32 v156, v156\n"
    "v_exp_f32 v157, v157\n"
    "v_exp_f32 v126, v126\n"
    "v_exp_f32 v127, v127\n"
    "v_smfmac_f32_32x32x32_bf16 v[78:93], v[20:23], v[150:157], v158\n"
    "v_exp_f32 v128, v128\n"
    "v_exp_f32 v129, v129\n"
    "v_exp_f32 v130, v130\n"
    "v_mfma_f32_32x32x16_bf16 v[142:157], v[10:13], v[56:59], 0\n"
    "v_exp_f32 v131, v131\n"
    "v_exp_f32 v132, v132\n"
    "v_exp_f32 v133, v133\n"
    "v_mfma_f32_32x32x8_bf16 v[142:157], v[14:15], v[60:61], v[142:157]\n"
    "ds_read_b128 v[34:37], v5 offset:18560\n"
    "global_load_dwordx4 v[248:251], v2, s[38:39] offset:-3072\n"
    "v_exp_f32 v134, v134\n"
    "v_exp_f32 v135, v135\n"
    "v_exp_f32 v136, v136\n"
    "v_exp_f32 v137, v137\n"
    "v_smfmac_f32_32x32x32_bf16 v[94:109], v[16:19], v[126:133], v158\n"
    "global_load_lds_dwordx4 v2, s[40:41] offset:3072\n"
    "v_exp_f32 v138, v138\n"
    "v_exp_f32 v139, v139\n"
    "v_exp_f32 v140, v140\n"
    "v_exp_f32 v141, v141\n"
    "v_exp_f32 v142, v142\n"
    "v_exp_f32 v143, v143\n"
    "v_smfmac_f32_32x32x32_bf16 v[94:109], v[20:23], v[134:141], v158\n"
    "v_exp_f32 v144, v144\n"
    "v_exp_f32 v145, v145\n"
    "v_exp_f32 v146, v146\n"
    "s_waitcnt lgkmcnt(0)\n"
    "v_mfma_f32_32x32x16_bf16 v[126:141], v[24:27], v[38:41], 0\n"
    "global_load_dwordx4 v[252:255], v2, s[38:39] offset:-2048\n"
    "s_add_u32 m0, m0, 4096\n"
    "v_exp_f32 v147, v147\n"
    "v_exp_f32 v148, v148\n"
    "v_exp_f32 v149, v149\n"
    "v_mfma_f32_32x32x8_bf16 v[126:141], v[28:29], v[42:43], v[126:141]\n"
    "s_add_u32 s40, s40, 4096\n"
    "s_addc_u32 s41, s41, 0\n"
    "v_exp_f32 v150, v150\n"
    "v_exp_f32 v151, v151\n"
    "v_exp_f32 v152, v152\n"
    "v_exp_f32 v153, v153\n"
    "v_smfmac_f32_32x32x32_bf16 v[110:125], v[16:19], v[142:149], v158\n"
    "global_load_lds_dwordx4 v2, s[40:41] offset:0\n"
    "v_exp_f32 v154, v154\n"
    "v_exp_f32 v155, v155\n"
    "v_exp_f32 v156, v156\n"
    "v_exp_f32 v157, v157\n"
    "v_exp_f32 v126, v126\n"
    "v_exp_f32 v127, v127\n"
    "v_smfmac_f32_32x32x32_bf16 v[110:125], v[20:23], v[150:157], v158\n"
    "v_exp_f32 v128, v128\n"
    "v_exp_f32 v129, v129\n"
    "v_exp_f32 v130, v130\n"
    "v_mfma_f32_32x32x16_bf16 v[142:157], v[24:27], v[44:47], 0\n"
    "v_exp_f32 v131, v131\n"
    "v_exp_f32 v132, v132\n"
    "v_exp_f32 v133, v133\n"
    "v_mfma_f32_32x32x8_bf16 v[142:157], v[28:29], v[48:49], v[142:157]\n"
    "global_load_dwordx4 v[10:13], v2, s[38:39] offset:-1024\n"
    "global_load_dwordx4 v[14:17], v2, s[38:39] offset:0\n"
    "v_exp_f32 v134, v134\n"
    "v_exp_f32 v135, v135\n"
    "v_exp_f32 v136, v136\n"
    "v_exp_f32 v137, v137\n"
    "v_smfmac_f32_32x32x32_bf16 v[62:77], v[30:33], v[126:133], v158\n"
    "s_nop 0\n"
    "v_exp_f32 v138, v138\n"
    "v_exp_f32 v139, v139\n"
    "v_exp_f32 v140, v140\n"
    "v_exp_f32 v141, v141\n"
    "v_exp_f32 v142, v142\n"
    "v_exp_f32 v143, v143\n"
    "v_smfmac_f32_32x32x32_bf16 v[62:77], v[34:37], v[134:141], v158\n"
    "v_exp_f32 v144, v144\n"
    "v_exp_f32 v145, v145\n"
    "v_exp_f32 v146, v146\n"
    "v_mfma_f32_32x32x16_bf16 v[126:141], v[24:27], v[50:53], 0\n"
    "v_exp_f32 v147, v147\n"
    "v_exp_f32 v148, v148\n"
    "v_exp_f32 v149, v149\n"
    "v_mfma_f32_32x32x8_bf16 v[126:141], v[28:29], v[54:55], v[126:141]\n"
    "global_load_dwordx4 v[46:49], v2, s[38:39] offset:1024\n"
    "global_load_dwordx4 v[18:21], v2, s[38:39] offset:2048\n"
    "v_exp_f32 v150, v150\n"
    "v_exp_f32 v151, v151\n"
    "v_exp_f32 v152, v152\n"
    "v_exp_f32 v153, v153\n"
    "v_smfmac_f32_32x32x32_bf16 v[78:93], v[30:33], v[142:149], v158\n"
    "s_nop 0\n"
    "v_exp_f32 v154, v154\n"
    "v_exp_f32 v155, v155\n"
    "v_exp_f32 v156, v156\n"
    "v_exp_f32 v157, v157\n"
    "v_exp_f32 v126, v126\n"
    "v_exp_f32 v127, v127\n"
    "v_smfmac_f32_32x32x32_bf16 v[78:93], v[34:37], v[150:157], v158\n"
    "v_exp_f32 v128, v128\n"
    "v_exp_f32 v129, v129\n"
    "v_exp_f32 v130, v130\n"
    "v_mfma_f32_32x32x16_bf16 v[142:157], v[24:27], v[56:59], 0\n"
    "v_exp_f32 v131, v131\n"
    "v_exp_f32 v132, v132\n"
    "v_exp_f32 v133, v133\n"
    "v_mfma_f32_32x32x8_bf16 v[142:157], v[28:29], v[60:61], v[142:157]\n"
    "global_load_dwordx4 v[50:53], v2, s[38:39] offset:3072\n"
    "v_exp_f32 v134, v134\n"
    "v_exp_f32 v135, v135\n"
    "v_exp_f32 v136, v136\n"
    "v_exp_f32 v137, v137\n"
    "v_smfmac_f32_32x32x32_bf16 v[94:109], v[30:33], v[126:133], v158\n"
    "s_nop 1\n"
    "v_exp_f32 v138, v138\n"
    "v_exp_f32 v139, v139\n"
    "v_exp_f32 v140, v140\n"
    "v_exp_f32 v141, v141\n"
    "v_exp_f32 v142, v142\n"
    "v_exp_f32 v143, v143\n"
    "v_smfmac_f32_32x32x32_bf16 v[94:109], v[34:37], v[134:141], v158\n"
    "v_exp_f32 v144, v144\n"
    "v_exp_f32 v145, v145\n"
    "v_exp_f32 v146, v146\n"
    "v_exp_f32 v147, v147\n"
    "v_exp_f32 v148, v148\n"
    "v_exp_f32 v149, v149\n"
    "v_exp_f32 v150, v150\n"
    "v_exp_f32 v151, v151\n"
    "v_exp_f32 v152, v152\n"
    "v_exp_f32 v153, v153\n"
    "v_smfmac_f32_32x32x32_bf16 v[110:125], v[30:33], v[142:149], v158\n"
    "s_add_u32 s38, s38, 8192\n"
    "s_addc_u32 s39, s39, 0\n"
    "global_load_dwordx4 v[54:57], v2, s[38:39] offset:-4096\n"
    "global_load_dwordx4 v[58:61], v2, s[38:39] offset:-3072\n"
    "v_exp_f32 v154, v154\n"
    "v_exp_f32 v155, v155\n"
    "v_exp_f32 v156, v156\n"
    "v_exp_f32 v157, v157\n"
    "s_nop 1\n"
    "v_smfmac_f32_32x32x32_bf16 v[110:125], v[34:37], v[150:157], v158\n"
    "v_lshrrev_b32 v159, 4, v2\n"
    "v_and_b32 v24, 31, v159\n"
    "v_lshrrev_b32 v25, 5, v159\n"
    "s_and_b32 s84, s67, 1\n"
    "s_lshr_b32 s85, s67, 1\n"
    "s_lshl_b32 s90, s84, 9\n"
    "s_add_u32 s90, s90, s66\n"
    "s_add_u32 s90, s90, 57344\n"
    "v_lshl_add_u32 v26, v24, 2, s90\n"
    "s_lshl_b32 s90, s85, 10\n"
    "v_add_u32 v27, s90, v26\n"
    "v_mov_b32 v28, 0\n"
    "s_mov_b32 exec_hi, 0\n"
    "ds_write_b32 v27, v72 offset:0\n"
    "ds_write_b32 v27, v88 offset:128\n"
    "ds_write_b32 v27, v104 offset:256\n"
    "ds_write_b32 v27, v120 offset:384\n"
    "s_mov_b32 exec_hi, -1\n"
    "v_lshrrev_b32 v106, 4, v159\n"
    "v_and_b32 v107, 15, v159\n"
    "v_and_b32 v45, 7, v107\n"
    "v_mul_u32_u24 v45, 1152, v45\n"
    "v_lshl_add_u32 v45, v106, 3, v45\n"
    "s_lshl_b32 s90, s67, 5\n"
    "s_add_u32 s90, s90, s66\n"
    "s_add_u32 s90, s90, 133120\n"
    "v_add_u32 v45, s90, v45\n"
    "global_load_dwordx4 v[126:129], v2, s[38:39] offset:-2048\n"
    "global_load_dwordx4 v[130:133], v2, s[38:39] offset:-1024\n"
    "global_load_dwordx4 v[134:137], v2, s[38:39] offset:0\n"
    "global_load_dwordx4 v[138:141], v2, s[38:39] offset:1024\n"
    "s_waitcnt lgkmcnt(0)\n"
    "s_barrier\n"
    "ds_read_b32 v30, v26 offset:0\n"
    "ds_read_b32 v31, v26 offset:1024\n"
    "ds_read_b32 v32, v26 offset:128\n"
    "ds_read_b32 v33, v26 offset:1152\n"
    "ds_read_b32 v34, v26 offset:256\n"
    "ds_read_b32 v35, v26 offset:1280\n"
    "ds_read_b32 v36, v26 offset:384\n"
    "ds_read_b32 v37, v26 offset:1408\n"
    "v_lshl_add_u32 v28, v24, 1, v25\n"
    "v_mul_u32_u24 v28, 272, v28\n"
    "s_lshl_b32 s90, s67, 6\n"
    "s_add_u32 s90, s90, s66\n"
    "s_add_u32 s90, s90, 61440\n"
    "v_add_u32 v28, s90, v28\n"
    "v_lshrrev_b32 v29, 2, v24\n"
    "v_mul_u32_u24 v29, 272, v29\n"
    "v_and_b32 v27, 3, v24\n"
    "v_lshl_add_u32 v29, v27, 2, v29\n"
    "v_add_u32 v29, s90, v29\n"
    "s_waitcnt lgkmcnt(0)\n"
    "v_add_f32 v30, v30, v31\n"
    "v_add_f32 v32, v32, v33\n"
    "v_add_f32 v34, v34, v35\n"
    "v_add_f32 v36, v36, v37\n"
    "v_rcp_f32 v30, v30\n"
    "v_rcp_f32 v32, v32\n"
    "v_rcp_f32 v34, v34\n"
    "v_rcp_f32 v36, v36\n"
    "v_mul_f32 v62, v62, v30\n"
    "v_mul_f32 v63, v63, v30\n"
    "v_mul_f32 v64, v64, v30\n"
    "v_mul_f32 v65, v65, v30\n"
    "v_mul_f32 v66, v66, v30\n"
    "v_mul_f32 v67, v67, v30\n"
    "v_mul_f32 v68, v68, v30\n"
    "v_mul_f32 v69, v69, v30\n"
    "v_mul_f32 v70, v70, v30\n"
    "v_mul_f32 v71, v71, v30\n"
    "v_cvt_pk_bf16_f32 v62, v62, v63\n"
    "v_cvt_pk_bf16_f32 v63, v64, v65\n"
    "v_cvt_pk_bf16_f32 v64, v66, v67\n"
    "v_cvt_pk_bf16_f32 v65, v68, v69\n"
    "v_cvt_pk_bf16_f32 v66, v70, v71\n"
    "ds_write_b128 v28, v[62:65] offset:0\n"
    "global_load_dwordx4 v[142:145], v2, s[38:39] offset:2048\n"
    "v_mul_f32 v78, v78, v32\n"
    "v_mul_f32 v79, v79, v32\n"
    "v_mul_f32 v80, v80, v32\n"
    "v_mul_f32 v81, v81, v32\n"
    "v_mul_f32 v82, v82, v32\n"
    "v_mul_f32 v83, v83, v32\n"
    "v_mul_f32 v84, v84, v32\n"
    "v_mul_f32 v85, v85, v32\n"
    "v_mul_f32 v86, v86, v32\n"
    "v_mul_f32 v87, v87, v32\n"
    "v_cvt_pk_bf16_f32 v78, v78, v79\n"
    "v_cvt_pk_bf16_f32 v79, v80, v81\n"
    "v_cvt_pk_bf16_f32 v80, v82, v83\n"
    "v_cvt_pk_bf16_f32 v81, v84, v85\n"
    "v_cvt_pk_bf16_f32 v82, v86, v87\n"
    "ds_write_b128 v28, v[78:81] offset:16\n"
    "global_load_dwordx4 v[146:149], v2, s[38:39] offset:3072\n"
    "v_mul_f32 v94, v94, v34\n"
    "v_mul_f32 v95, v95, v34\n"
    "v_mul_f32 v96, v96, v34\n"
    "v_mul_f32 v97, v97, v34\n"
    "v_mul_f32 v98, v98, v34\n"
    "v_mul_f32 v99, v99, v34\n"
    "v_mul_f32 v100, v100, v34\n"
    "v_mul_f32 v101, v101, v34\n"
    "v_mul_f32 v102, v102, v34\n"
    "v_mul_f32 v103, v103, v34\n"
    "v_cvt_pk_bf16_f32 v94, v94, v95\n"
    "v_cvt_pk_bf16_f32 v95, v96, v97\n"
    "v_cvt_pk_bf16_f32 v96, v98, v99\n"
    "v_cvt_pk_bf16_f32 v97, v100, v101\n"
    "v_cvt_pk_bf16_f32 v98, v102, v103\n"
    "ds_write_b128 v28, v[94:97] offset:32\n"
    "s_add_u32 s38, s38, 8192\n"
    "s_addc_u32 s39, s39, 0\n"
    "global_load_dwordx4 v[150:153], v2, s[38:39] offset:-4096\n"
    "v_mul_f32 v110, v110, v36\n"
    "v_mul_f32 v111, v111, v36\n"
    "v_mul_f32 v112, v112, v36\n"
    "v_mul_f32 v113, v113, v36\n"
    "v_mul_f32 v114, v114, v36\n"
    "v_mul_f32 v115, v115, v36\n"
    "v_mul_f32 v116, v116, v36\n"
    "v_mul_f32 v117, v117, v36\n"
    "v_mul_f32 v118, v118, v36\n"
    "v_mul_f32 v119, v119, v36\n"
    "v_cvt_pk_bf16_f32 v110, v110, v111\n"
    "v_cvt_pk_bf16_f32 v111, v112, v113\n"
    "v_cvt_pk_bf16_f32 v112, v114, v115\n"
    "v_cvt_pk_bf16_f32 v113, v116, v117\n"
    "v_cvt_pk_bf16_f32 v114, v118, v119\n"
    "ds_write_b128 v28, v[110:113] offset:48\n"
    "global_load_dwordx4 v[154:157], v2, s[38:39] offset:-3072\n"
    "s_mov_b32 exec_hi, 0\n"
    "ds_write_b32 v29, v66 offset:17408\n"
    "ds_write_b32 v29, v82 offset:17424\n"
    "ds_write_b32 v29, v98 offset:17440\n"
    "ds_write_b32 v29, v114 offset:17456\n"
    "s_mov_b32 exec_hi, -1\n"
    "s_add_u32 s90, s66, 61440\n"
    "v_mul_u32_u24 v3, 272, v106\n"
    "v_lshl_add_u32 v3, v107, 4, v3\n"
    "v_add_u32 v3, s90, v3\n"
    "s_waitcnt lgkmcnt(0)\n"
    "s_barrier\n"
    "ds_read_b128 v[98:101], v3\n"
    "ds_read_b64 v[26:27], v45 offset:0\n"
    "ds_read_b64 v[28:29], v45 offset:128\n"
    "ds_read_b64 v[30:31], v45 offset:256\n"
    "ds_read_b64 v[32:33], v45 offset:384\n"
    "ds_read_b64 v[34:35], v45 offset:512\n"
    "ds_read_b64 v[36:37], v45 offset:640\n"
    "ds_read_b64 v[38:39], v45 offset:768\n"
    "ds_read_b64 v[40:41], v45 offset:896\n"
    "ds_read_b64 v[42:43], v45 offset:1024\n"
    "s_mov_b32 s87, 0\n"
    "ds_read_b128 v[102:105], v3 offset:1088\n"
    "s_waitcnt lgkmcnt(10)\n"
    "v_mfma_f32_16x16x32_bf16 v[62:65], a[0:3], v[98:101], 0\n"
    "global_load_dwordx4 a[0:3], v2, s[38:39] offset:-2048\n"
    "v_mfma_f32_16x16x32_bf16 v[66:69], a[4:7], v[98:101], 0\n"
    "v_bfe_u32 v25, v2, 6, 2\n"
    "v_mfma_f32_16x16x32_bf16 v[70:73], a[8:11], v[98:101], 0\n"
    "v_cmp_eq_u32 s[94:95], v106, v25\n"
    "v_mfma_f32_16x16x32_bf16 v[74:77], a[12:15], v[98:101], 0\n"
    "v_and_b32 v25, 1, v107\n"
    "v_mfma_f32_16x16x32_bf16 v[78:81], a[16:19], v[98:101], 0\n"
    "global_load_dwordx4 a[4:7], v2, s[38:39] offset:-1024\n"
    "v_mfma_f32_16x16x32_bf16 v[82:85], a[20:23], v[98:101], 0\n"
    "v_cmp_eq_u32 s[36:37], 1, v25\n"
    "v_mfma_f32_16x16x32_bf16 v[86:89], a[24:27], v[98:101], 0\n"
    "v_and_b32 v25, 2, v107\n"
    "v_mfma_f32_16x16x32_bf16 v[90:93], a[28:31], v[98:101], 0\n"
    "v_cmp_eq_u32 s[40:41], 2, v25\n"
    "v_mfma_f32_16x16x32_bf16 v[94:97], a[32:35], v[98:101], 0\n"
    "global_load_dwordx4 a[8:11], v2, s[38:39] offset:0\n"
    "ds_read_b128 v[98:101], v3 offset:2176\n"
    "s_waitcnt lgkmcnt(1)\n"
    "v_mfma_f32_16x16x32_bf16 v[62:65], a[36:39], v[102:105], v[62:65]\n"
    "v_mov_b32 v108, 0x3f803f80\n"
    "v_mfma_f32_16x16x32_bf16 v[66:69], a[40:43], v[102:105], v[66:69]\n"
    "v_mov_b32 v109, 0x3f803f80\n"
    "v_mfma_f32_16x16x32_bf16 v[70:73], a[44:47], v[102:105], v[70:73]\n"
    "v_mov_b32 v110, 0x3f803f80\n"
    "v_mfma_f32_16x16x32_bf16 v[74:77], a[48:51], v[102:105], v[74:77]\n"
    "global_load_dwordx4 a[12:15], v2, s[38:39] offset:1024\n"
    "v_mfma_f32_16x16x32_bf16 v[78:81], a[52:55], v[102:105], v[78:81]\n"
    "v_mov_b32 v111, 0x3f803f80\n"
    "v_mfma_f32_16x16x32_bf16 v[82:85], a[56:59], v[102:105], v[82:85]\n"
    "v_mov_b32 v44, 0\n"
    "v_mfma_f32_16x16x32_bf16 v[86:89], a[60:63], v[102:105], v[86:89]\n"
    "global_load_dwordx4 a[16:19], v2, s[38:39] offset:2048\n"
    "v_mfma_f32_16x16x32_bf16 v[90:93], a[64:67], v[102:105], v[90:93]\n"
    "v_mov_b32 v45, 0\n"
    "v_mfma_f32_16x16x32_bf16 v[94:97], a[68:71], v[102:105], v[94:97]\n"
    "s_add_u32 s90, s66, 73984\n"
    "ds_read_b128 v[102:105], v3 offset:3264\n"
    "s_waitcnt lgkmcnt(1)\n"
    "v_mfma_f32_16x16x32_bf16 v[62:65], a[72:75], v[98:101], v[62:65]\n"
    "v_add_u32 v4, s90, v2\n"
    "v_mfma_f32_16x16x32_bf16 v[66:69], a[76:79], v[98:101], v[66:69]\n"
    "global_load_dwordx4 a[20:23], v2, s[38:39] offset:3072\n"
    "v_mfma_f32_16x16x32_bf16 v[70:73], a[80:83], v[98:101], v[70:73]\n"
    "s_lshr_b32 s84, s67, 1\n"
    "v_mfma_f32_16x16x32_bf16 v[74:77], a[84:87], v[98:101], v[74:77]\n"
    "s_and_b32 s85, s67, 1\n"
    "v_mfma_f32_16x16x32_bf16 v[78:81], a[88:91], v[98:101], v[78:81]\n"
    "s_lshl_b32 s90, s84, 12\n"
    "v_mfma_f32_16x16x32_bf16 v[82:85], a[92:95], v[98:101], v[82:85]\n"
    "s_add_u32 s38, s38, 8192\n"
    "s_addc_u32 s39, s39, 0\n"
    "global_load_dwordx4 a[24:27], v2, s[38:39] offset:-4096\n"
    "v_mfma_f32_16x16x32_bf16 v[86:89], a[96:99], v[98:101], v[86:89]\n"
    "s_lshl3_add_u32 s90, s85, s90\n"
    "v_mfma_f32_16x16x32_bf16 v[90:93], a[100:103], v[98:101], v[90:93]\n"
    "v_add_u32 v8, s90, v4\n"
    "v_mfma_f32_16x16x32_bf16 v[94:97], a[104:107], v[98:101], v[94:97]\n"
    "global_load_dwordx4 a[28:31], v2, s[38:39] offset:-3072\n"
    "ds_read_b128 v[98:101], v3 offset:4352\n"
    "s_waitcnt lgkmcnt(1)\n"
    "v_mfma_f32_16x16x32_bf16 v[62:65], a[108:111], v[102:105], v[62:65]\n"
    "v_mov_b32 v6, v2\n"
    "v_mfma_f32_16x16x32_bf16 v[66:69], a[112:115], v[102:105], v[66:69]\n"
    "v_lshlrev_b32 v7, 2, v107\n"
    "v_mfma_f32_16x16x32_bf16 v[70:73], a[116:119], v[102:105], v[70:73]\n"
    "v_mul_u32_u24 v7, 1152, v7\n"
    "v_mfma_f32_16x16x32_bf16 v[74:77], a[120:123], v[102:105], v[74:77]\n"
    "global_load_dwordx4 a[32:35], v2, s[38:39] offset:-2048\n"
    "v_mfma_f32_16x16x32_bf16 v[78:81], a[124:127], v[102:105], v[78:81]\n"
    "v_lshl_add_u32 v7, v106, 3, v7\n"
    "v_mfma_f32_16x16x32_bf16 v[82:85], a[128:131], v[102:105], v[82:85]\n"
    "s_lshl_b32 s90, s67, 10\n"
    "v_mfma_f32_16x16x32_bf16 v[86:89], a[132:135], v[102:105], v[86:89]\n"
    "s_add_u32 s90, s90, s66\n"
    "v_mfma_f32_16x16x32_bf16 v[90:93], a[136:139], v[102:105], v[90:93]\n"
    "global_load_dwordx4 a[36:39], v2, s[38:39] offset:-1024\n"
    "v_mfma_f32_16x16x32_bf16 v[94:97], a[140:143], v[102:105], v[94:97]\n"
    "s_add_u32 s88, s90, 147712\n"
    "ds_read_b128 v[102:105], v3 offset:5440\n"
    "s_waitcnt lgkmcnt(1)\n"
    "v_mfma_f32_16x16x32_bf16 v[62:65], a[144:147], v[98:101], v[62:65]\n"
    "v_lshl_add_u32 v5, v106, 4, s88\n"
    "v_mfma_f32_16x16x32_bf16 v[66:69], a[148:151], v[98:101], v[66:69]\n"
    "global_load_dwordx4 a[40:43], v2, s[38:39] offset:0\n"
    "v_mfma_f32_16x16x32_bf16 v[70:73], a[152:155], v[98:101], v[70:73]\n"
    "v_mfma_f32_16x16x32_bf16 v[74:77], a[156:159], v[98:101], v[74:77]\n"
    "s_waitcnt vmcnt(25)\n"
    "v_mfma_f32_16x16x32_bf16 v[78:81], v[10:13], v[98:101], v[78:81]\n"
    "s_waitcnt vmcnt(24)\n"
    "v_mfma_f32_16x16x32_bf16 v[82:85], v[14:17], v[98:101], v[82:85]\n"
    "global_load_dwordx4 a[44:47], v2, s[38:39] offset:1024\n"
    "s_waitcnt vmcnt(24)\n"
    "v_mfma_f32_16x16x32_bf16 v[86:89], v[46:49], v[98:101], v[86:89]\n"
    "s_waitcnt vmcnt(23)\n"
    "v_mfma_f32_16x16x32_bf16 v[90:93], v[18:21], v[98:101], v[90:93]\n"
    "s_waitcnt vmcnt(22)\n"
    "v_mfma_f32_16x16x32_bf16 v[94:97], v[50:53], v[98:101], v[94:97]\n"
    "ds_read_b128 v[98:101], v3 offset:6528\n"
    "s_waitcnt vmcnt(21) lgkmcnt(1)\n"
    "v_mfma_f32_16x16x32_bf16 v[62:65], v[54:57], v[102:105], v[62:65]\n"
    "global_load_dwordx4 a[48:51], v2, s[38:39] offset:2048\n"
    "s_waitcnt vmcnt(21)\n"
    "v_mfma_f32_16x16x32_bf16 v[66:69], v[58:61], v[102:105], v[66:69]\n"
    "v_mov_b32 v158, 0\n"
    "global_load_dword v158, v158, s[48:49] sc1\n"
    "s_waitcnt vmcnt(21)\n"
    "v_mfma_f32_16x16x32_bf16 v[70:73], v[126:129], v[102:105], v[70:73]\n"
    "s_waitcnt vmcnt(20)\n"
    "v_mfma_f32_16x16x32_bf16 v[74:77], v[130:133], v[102:105], v[74:77]\n"
    "v_bfe_u32 v54, v2, 6, 2\n"
    "s_waitcnt vmcnt(19)\n"
    "v_mfma_f32_16x16x32_bf16 v[78:81], v[134:137], v[102:105], v[78:81]\n"
    "global_load_dwordx4 a[52:55], v2, s[38:39] offset:3072\n"
    "s_waitcnt vmcnt(19)\n"
    "v_mfma_f32_16x16x32_bf16 v[82:85], v[138:141], v[102:105], v[82:85]\n"
    "v_cmp_eq_u32 vcc, v106, v54\n"
    "s_waitcnt vmcnt(18)\n"
    "v_mfma_f32_16x16x32_bf16 v[86:89], v[142:145], v[102:105], v[86:89]\n"
    "v_bfe_u32 v54, v2, 4, 1\n"
    "s_waitcnt vmcnt(17)\n"
    "v_mfma_f32_16x16x32_bf16 v[90:93], v[146:149], v[102:105], v[90:93]\n"
    "s_add_u32 s38, s38, 8192\n"
    "s_addc_u32 s39, s39, 0\n"
    "global_load_dwordx4 a[56:59], v2, s[38:39] offset:-4096\n"
    "s_waitcnt vmcnt(17)\n"
    "v_mfma_f32_16x16x32_bf16 v[94:97], v[150:153], v[102:105], v[94:97]\n"
    "v_lshlrev_b32 v54, 4, v54\n"
    "ds_read_b128 v[102:105], v3 offset:7616\n"
    "s_waitcnt vmcnt(16) lgkmcnt(1)\n"
    "v_mfma_f32_16x16x32_bf16 v[62:65], v[154:157], v[98:101], v[62:65]\n"
    "v_mov_b32 v49, 0x3f00\n"
    "v_mfma_f32_16x16x32_bf16 v[66:69], a[160:163], v[98:101], v[66:69]\n"
    "v_lshlrev_b32 v49, v54, v49\n"
    "v_mfma_f32_16x16x32_bf16 v[70:73], a[164:167], v[98:101], v[70:73]\n"
    "global_load_dwordx4 a[60:63], v2, s[38:39] offset:-3072\n"
    "v_mfma_f32_16x16x32_bf16 v[74:77], a[168:171], v[98:101], v[74:77]\n"
    "v_cndmask_b32 v49, 0, v49, vcc\n"
    "v_mfma_f32_16x16x32_bf16 v[78:81], a[172:175], v[98:101], v[78:81]\n"
    "v_bfe_u32 v54, v2, 5, 1\n"
    "v_mfma_f32_16x16x32_bf16 v[82:85], a[176:179], v[98:101], v[82:85]\n"
    "v_cmp_eq_u32 vcc, 0, v54\n"
    "v_mfma_f32_16x16x32_bf16 v[86:89], a[180:183], v[98:101], v[86:89]\n"
    "global_load_dwordx4 a[64:67], v2, s[38:39] offset:-2048\n"
    "v_mfma_f32_16x16x32_bf16 v[90:93], a[184:187], v[98:101], v[90:93]\n"
    "v_mov_b32 v50, 0\n"
    "v_mfma_f32_16x16x32_bf16 v[94:97], a[188:191], v[98:101], v[94:97]\n"
    "v_mov_b32 v51, 0\n"
    "ds_read_b128 v[98:101], v3 offset:8704\n"
    "s_waitcnt lgkmcnt(1)\n"
    "v_mfma_f32_16x16x32_bf16 v[62:65], a[192:195], v[102:105], v[62:65]\n"
    "global_load_dwordx4 a[68:71], v2, s[38:39] offset:-1024\n"
    "v_mfma_f32_16x16x32_bf16 v[66:69], a[196:199], v[102:105], v[66:69]\n"
    "v_cndmask_b32 v46, 0, v49, vcc\n"
    "v_mfma_f32_16x16x32_bf16 v[70:73], a[200:203], v[102:105], v[70:73]\n"
    "v_sub_u32 v47, v49, v46\n"
    "v_mfma_f32_16x16x32_bf16 v[74:77], a[204:207], v[102:105], v[74:77]\n"
    "v_mov_b32 v48, 0\n"
    "v_mfma_f32_16x16x32_bf16 v[78:81], a[208:211], v[102:105], v[78:81]\n"
    "global_load_dwordx4 a[72:75], v2, s[38:39] offset:0\n"
    "v_mfma_f32_16x16x32_bf16 v[82:85], a[212:215], v[102:105], v[82:85]\n"
    "v_mov_b32 v49, 0\n"
    "v_mfma_f32_16x16x32_bf16 v[86:89], a[216:219], v[102:105], v[86:89]\n"
    "v_mov_b32 v52, v46\n"
    "v_mfma_f32_16x16x32_bf16 v[90:93], a[220:223], v[102:105], v[90:93]\n"
    "v_mov_b32 v53, v47\n"
    "v_mfma_f32_16x16x32_bf16 v[94:97], a[224:227], v[102:105], v[94:97]\n"
    "global_load_dwordx4 a[76:79], v2, s[38:39] offset:1024\n"
    "ds_read_b128 v[102:105], v3 offset:9792\n"
    "s_waitcnt lgkmcnt(1)\n"
    "v_mfma_f32_16x16x32_bf16 v[62:65], a[228:231], v[98:101], v[62:65]\n"
    "v_and_b32 v54, 3, v107\n"
    "v_mfma_f32_16x16x32_bf16 v[66:69], a[232:235], v[98:101], v[66:69]\n"
    "v_lshlrev_b32 v54, 5, v54\n"
    "v_mfma_f32_16x16x32_bf16 v[70:73], a[236:239], v[98:101], v[70:73]\n"
    "global_load_dwordx4 a[80:83], v2, s[38:39] offset:2048\n"
    "v_mfma_f32_16x16x32_bf16 v[74:77], a[240:243], v[98:101], v[74:77]\n"
    "v_bfe_u32 v61, v107, 2, 1\n"
    "v_mfma_f32_16x16x32_bf16 v[78:81], a[244:247], v[98:101], v[78:81]\n"
    "v_lshl_add_u32 v54, v61, 4, v54\n"
    "v_mfma_f32_16x16x32_bf16 v[82:85], a[248:251], v[98:101], v[82:85]\n"
    "v_lshl_add_u32 v54, v106, 8, v54\n"
    "v_mfma_f32_16x16x32_bf16 v[86:89], a[252:255], v[98:101], v[86:89]\n"
    "global_load_dwordx4 a[84:87], v2, s[38:39] offset:3072\n"
    "v_mfma_f32_16x16x32_bf16 v[90:93], v[164:167], v[98:101], v[90:93]\n"
    "v_lshrrev_b32 v61, 3, v107\n"
    "v_mfma_f32_16x16x32_bf16 v[94:97], v[168:171], v[98:101], v[94:97]\n"
    "v_lshl_add_u32 v54, v61, 10, v54\n"
    "ds_read_b128 v[98:101], v3 offset:10880\n"
    "s_waitcnt lgkmcnt(1)\n"
    "v_mfma_f32_16x16x32_bf16 v[62:65], v[172:175], v[102:105], v[62:65]\n"
    "s_lshr_b32 s84, s67, 1\n"
    "v_mfma_f32_16x16x32_bf16 v[66:69], v[176:179], v[102:105], v[66:69]\n"
    "s_add_u32 s38, s38, 8192\n"
    "s_addc_u32 s39, s39, 0\n"
    "global_load_dwordx4 a[88:91], v2, s[38:39] offset:-4096\n"
    "v_mfma_f32_16x16x32_bf16 v[70:73], v[180:183], v[102:105], v[70:73]\n"
    "s_and_b32 s85, s67, 1\n"
    "v_mfma_f32_16x16x32_bf16 v[74:77], v[184:187], v[102:105], v[74:77]\n"
    "s_lshl_b32 s90, s84, 7\n"
    "v_mfma_f32_16x16x32_bf16 v[78:81], v[188:191], v[102:105], v[78:81]\n"
    "s_lshl3_add_u32 s90, s85, s90\n"
    "v_mfma_f32_16x16x32_bf16 v[82:85], v[192:195], v[102:105], v[82:85]\n"
    "global_load_dwordx4 a[92:95], v2, s[38:39] offset:-3072\n"
    "v_mfma_f32_16x16x32_bf16 v[86:89], v[196:199], v[102:105], v[86:89]\n"
    "s_add_u32 s91, s66, 151808\n"
    "v_mfma_f32_16x16x32_bf16 v[90:93], v[200:203], v[102:105], v[90:93]\n"
    "s_add_u32 s90, s90, s91\n"
    "v_mfma_f32_16x16x32_bf16 v[94:97], v[204:207], v[102:105], v[94:97]\n"
    "global_load_dwordx4 a[96:99], v2, s[38:39] offset:-2048\n"
    "ds_read_b128 v[102:105], v3 offset:11968\n"
    "s_waitcnt lgkmcnt(1)\n"
    "v_mfma_f32_16x16x32_bf16 v[62:65], v[208:211], v[98:101], v[62:65]\n"
    "v_add_u32 v54, s90, v54\n"
    "v_mfma_f32_16x16x32_bf16 v[66:69], v[212:215], v[98:101], v[66:69]\n"
    "v_and_b32 v55, 7, v107\n"
    "v_mfma_f32_16x16x32_bf16 v[70:73], v[216:219], v[98:101], v[70:73]\n"
    "v_lshlrev_b32 v55, 6, v55\n"
    "v_mfma_f32_16x16x32_bf16 v[74:77], v[220:223], v[98:101], v[74:77]\n"
    "global_load_dwordx4 a[100:103], v2, s[38:39] offset:-1024\n"
    "v_mfma_f32_16x16x32_bf16 v[78:81], v[224:227], v[98:101], v[78:81]\n"
    "v_lshl_add_u32 v55, v61, 3, v55\n"
    "v_mfma_f32_16x16x32_bf16 v[82:85], v[228:231], v[98:101], v[82:85]\n"
    "s_add_u32 s89, s66, 161024\n"
    "v_mfma_f32_16x16x32_bf16 v[86:89], v[232:235], v[98:101], v[86:89]\n"
    "s_lshl4_add_u32 s90, s67, s89\n"
    "v_mfma_f32_16x16x32_bf16 v[90:93], v[236:239], v[98:101], v[90:93]\n"
    "global_load_dwordx4 a[104:107], v2, s[38:39] offset:0\n"
    "v_mfma_f32_16x16x32_bf16 v[94:97], v[240:243], v[98:101], v[94:97]\n"
    "v_add_u32 v55, s90, v55\n"
    "ds_read_b128 v[98:101], v3 offset:13056\n"
    "ds_read_b128 v[10:13], v9 offset:0\n"
    "s_waitcnt lgkmcnt(2)\n"
    "v_mfma_f32_16x16x32_bf16 v[62:65], v[244:247], v[102:105], v[62:65]\n"
    "v_bfe_u32 v56, v2, 5, 2\n"
    "ds_read_b128 v[14:17], v9 offset:1024\n"
    "v_mfma_f32_16x16x32_bf16 v[66:69], v[248:251], v[102:105], v[66:69]\n"
    "global_load_dwordx4 a[108:111], v2, s[38:39] offset:1024\n"
    "ds_read_b128 v[18:21], v9 offset:2048\n"
    "v_mfma_f32_16x16x32_bf16 v[70:73], v[252:255], v[102:105], v[70:73]\n"
    "v_bfe_u32 v61, v2, 4, 1\n"
    "ds_read_b128 v[22:25], v9 offset:3072\n"
    "s_waitcnt lgkmcnt(3)\n"
    "v_mfma_f32_16x16x32_bf16 v[74:77], v[10:13], v[102:105], v[74:77]\n"
    "v_lshl_add_u32 v56, v61, 2, v56\n"
    "ds_read_b128 v[10:13], v9 offset:4096\n"
    "s_waitcnt lgkmcnt(3)\n"
    "v_mfma_f32_16x16x32_bf16 v[78:81], v[14:17], v[102:105], v[78:81]\n"
    "v_lshl_add_u32 v56, v56, 6, s89\n"
    "ds_read_b128 v[14:17], v9 offset:5120\n"
    "s_waitcnt lgkmcnt(3)\n"
    "v_mfma_f32_16x16x32_bf16 v[82:85], v[18:21], v[102:105], v[82:85]\n"
    "global_load_dwordx4 a[112:115], v2, s[38:39] offset:2048\n"
    "ds_read_b128 v[18:21], v9 offset:6144\n"
    "s_waitcnt lgkmcnt(3)\n"
    "v_mfma_f32_16x16x32_bf16 v[86:89], v[22:25], v[102:105], v[86:89]\n"
    "s_lshl_b32 s84, s67, 11\n"
    "ds_read_b128 v[22:25], v9 offset:7168\n"
    "s_waitcnt lgkmcnt(3)\n"
    "v_mfma_f32_16x16x32_bf16 v[90:93], v[10:13], v[102:105], v[90:93]\n"
    "v_add_u32 v60, s84, v2\n"
    "ds_read_b128 v[10:13], v9 offset:8192\n"
    "s_waitcnt lgkmcnt(3)\n"
    "v_mfma_f32_16x16x32_bf16 v[94:97], v[14:17], v[102:105], v[94:97]\n"
    "v_add_u32 v57, s91, v60\n"
    "ds_read_b128 v[102:105], v3 offset:14144\n"
    "ds_read_b128 v[14:17], v9 offset:9216\n"
    "s_waitcnt lgkmcnt(4)\n"
    "v_mfma_f32_16x16x32_bf16 v[62:65], v[18:21], v[98:101], v[62:65]\n"
    "global_load_dwordx4 a[116:119], v2, s[38:39] offset:3072\n"
    "ds_read_b128 v[18:21], v9 offset:10240\n"
    "s_waitcnt lgkmcnt(4)\n"
    "v_mfma_f32_16x16x32_bf16 v[66:69], v[22:25], v[98:101], v[66:69]\n"
    "s_lshl_b32 s85, s67, 8\n"
    "ds_read_b128 v[22:25], v9 offset:11264\n"
    "s_waitcnt lgkmcnt(4)\n"
    "v_mfma_f32_16x16x32_bf16 v[70:73], v[10:13], v[98:101], v[70:73]\n"
    "s_add_u32 s85, s85, 8192\n"
    "ds_read_b128 v[10:13], v9 offset:12288\n"
    "s_waitcnt lgkmcnt(3)\n"
    "v_mfma_f32_16x16x32_bf16 v[74:77], v[14:17], v[98:101], v[74:77]\n"
    "s_add_u32 s38, s38, 8192\n"
    "s_addc_u32 s39, s39, 0\n"
    "global_load_dwordx4 a[120:123], v2, s[38:39] offset:-4096\n"
    "ds_read_b128 v[14:17], v9 offset:13312\n"
    "s_waitcnt lgkmcnt(3)\n"
    "v_mfma_f32_16x16x32_bf16 v[78:81], v[18:21], v[98:101], v[78:81]\n"
    "v_lshlrev_b32 v59, 4, v107\n"
    "ds_read_b128 v[18:21], v9 offset:14336\n"
    "s_waitcnt lgkmcnt(3)\n"
    "v_mfma_f32_16x16x32_bf16 v[82:85], v[22:25], v[98:101], v[82:85]\n"
    "v_lshl_add_u32 v59, v106, 2, v59\n"
    "ds_read_b128 v[22:25], v9 offset:15360\n"
    "s_waitcnt lgkmcnt(3)\n"
    "v_mfma_f32_16x16x32_bf16 v[86:89], v[10:13], v[98:101], v[86:89]\n"
    "v_add_u32 v59, s85, v59\n"
    "ds_read_b128 v[10:13], v9 offset:16384\n"
    "s_waitcnt lgkmcnt(3)\n"
    "v_mfma_f32_16x16x32_bf16 v[90:93], v[14:17], v[98:101], v[90:93]\n"
    "global_load_dwordx4 a[124:127], v2, s[38:39] offset:-3072\n"
    "ds_read_b128 v[14:17], v9 offset:17408\n"
    "s_waitcnt lgkmcnt(3)\n"
    "v_mfma_f32_16x16x32_bf16 v[94:97], v[18:21], v[98:101], v[94:97]\n"
    "v_add_u32 v58, s91, v59\n"
    "ds_read_b128 v[98:101], v3 offset:15232\n"
    "ds_read_b128 v[18:21], v9 offset:18432\n"
    "s_waitcnt lgkmcnt(4)\n"
    "v_mfma_f32_16x16x32_bf16 v[62:65], v[22:25], v[102:105], v[62:65]\n"
    "v_mfma_f32_16x16x32_bf16 v[78:81], v[46:49], v[34:37], v[78:81]\n"
    "ds_read_b128 v[22:25], v9 offset:19456\n"
    "s_waitcnt lgkmcnt(4)\n"
    "v_mfma_f32_16x16x32_bf16 v[66:69], v[10:13], v[102:105], v[66:69]\n"
    "v_mfma_f32_16x16x32_bf16 v[82:85], v[50:53], v[34:37], v[82:85]\n"
    "ds_read_b128 v[10:13], v9 offset:20480\n"
    "s_waitcnt lgkmcnt(4)\n"
    "v_mfma_f32_16x16x32_bf16 v[70:73], v[14:17], v[102:105], v[70:73]\n"
    "global_load_dwordx4 a[128:131], v2, s[38:39] offset:-2048\n"
    "v_mfma_f32_16x16x32_bf16 v[86:89], v[46:49], v[38:41], v[86:89]\n"
    "ds_read_b128 v[14:17], v9 offset:21504\n"
    "s_waitcnt lgkmcnt(3)\n"
    "v_mfma_f32_16x16x32_bf16 v[74:77], v[18:21], v[102:105], v[74:77]\n"
    "v_mfma_f32_16x16x32_bf16 v[90:93], v[50:53], v[38:41], v[90:93]\n"
    "ds_read_b128 v[18:21], v9 offset:22528\n"
    "s_waitcnt lgkmcnt(3)\n"
    "v_mfma_f32_16x16x32_bf16 v[78:81], v[22:25], v[102:105], v[78:81]\n"
    "v_mfma_f32_16x16x32_bf16 v[94:97], v[46:49], v[42:45], v[94:97]\n"
    "ds_read_b128 v[22:25], v9 offset:23552\n"
    "s_waitcnt lgkmcnt(3)\n"
    "v_mfma_f32_16x16x32_bf16 v[82:85], v[10:13], v[102:105], v[82:85]\n"
    "v_mfma_f32_16x16x32_bf16 v[62:65], v[46:49], v[26:29], v[62:65]\n"
    "ds_read_b128 v[10:13], v9 offset:24576\n"
    "s_waitcnt lgkmcnt(3)\n"
    "v_mfma_f32_16x16x32_bf16 v[86:89], v[14:17], v[102:105], v[86:89]\n"
    "global_load_dwordx4 a[132:135], v2, s[38:39] offset:-1024\n"
    "v_mfma_f32_16x16x32_bf16 v[66:69], v[50:53], v[26:29], v[66:69]\n"
    "s_waitcnt lgkmcnt(2)\n"
    "v_mfma_f32_16x16x32_bf16 v[90:93], v[18:21], v[102:105], v[90:93]\n"
    "v_mfma_f32_16x16x32_bf16 v[70:73], v[46:49], v[30:33], v[70:73]\n"
    "s_waitcnt lgkmcnt(1)\n"
    "v_mfma_f32_16x16x32_bf16 v[94:97], v[22:25], v[102:105], v[94:97]\n"
    "v_mfma_f32_16x16x32_bf16 v[74:77], v[50:53], v[30:33], v[74:77]\n"
    "ds_read_b128 v[102:105], v3 offset:16320\n"
    "s_waitcnt lgkmcnt(1)\n"
    "v_mfma_f32_16x16x32_bf16 v[62:65], v[10:13], v[98:101], v[62:65]\n"
    "global_load_dwordx4 a[136:139], v2, s[38:39] offset:0\n"
    "s_waitcnt vmcnt(35)\n"
    "v_mfma_f32_16x16x32_bf16 v[66:69], a[0:3], v[98:101], v[66:69]\n"
    "s_waitcnt vmcnt(22)\n"
    "v_cmp_gt_u32 vcc, 0x80, v158\n"
    "s_cbranch_vccnz 122f\n"
    "123:\n"
    "v_mfma_f32_16x16x32_bf16 v[70:73], a[4:7], v[98:101], v[70:73]\n"
    "buffer_load_dwordx4 v[112:115], v2, s[72:75], s87 offen offset:0 sc1\n"
    "v_mfma_f32_16x16x32_bf16 v[74:77], a[8:11], v[98:101], v[74:77]\n"
    "v_mfma_f32_16x16x32_bf16 v[78:81], a[12:15], v[98:101], v[78:81]\n"
    "v_mfma_f32_16x16x32_bf16 v[82:85], a[16:19], v[98:101], v[82:85]\n"
    "v_mfma_f32_16x16x32_bf16 v[86:89], a[20:23], v[98:101], v[86:89]\n"
    "buffer_load_dwordx4 v[116:119], v2, s[72:75], s87 offen offset:1024 sc1\n"
    "v_mfma_f32_16x16x32_bf16 v[90:93], a[24:27], v[98:101], v[90:93]\n"
    "v_mfma_f32_16x16x32_bf16 v[94:97], a[28:31], v[98:101], v[94:97]\n"
    "ds_read_b128 v[98:101], v3 offset:17408\n"
    "s_waitcnt lgkmcnt(1)\n"
    "v_mfma_f32_16x16x32_bf16 v[62:65], a[32:35], v[102:105], v[62:65]\n"
    "v_mfma_f32_16x16x32_bf16 v[66:69], a[36:39], v[102:105], v[66:69]\n"
    "buffer_load_dwordx4 v[120:123], v2, s[72:75], s87 offen offset:2048 sc1\n"
    "v_mfma_f32_16x16x32_bf16 v[70:73], a[40:43], v[102:105], v[70:73]\n"
    "v_mfma_f32_16x16x32_bf16 v[74:77], a[44:47], v[102:105], v[74:77]\n"
    "v_mfma_f32_16x16x32_bf16 v[78:81], a[48:51], v[102:105], v[78:81]\n"
    "s_waitcnt vmcnt(24)\n"
    "v_mfma_f32_16x16x32_bf16 v[82:85], a[52:55], v[102:105], v[82:85]\n"
    "buffer_load_dwordx4 v[124:127], v2, s[72:75], s87 offen offset:3072 sc1\n"
    "s_add_u32 s87, s87, 4096\n"
    "s_waitcnt vmcnt(24)\n"
    "v_mfma_f32_16x16x32_bf16 v[86:89], a[56:59], v[102:105], v[86:89]\n"
    "s_waitcnt vmcnt(23)\n"
    "v_mfma_f32_16x16x32_bf16 v[90:93], a[60:63], v[102:105], v[90:93]\n"
    "s_waitcnt vmcnt(22)\n"
    "v_mfma_f32_16x16x32_bf16 v[94:97], a[64:67], v[102:105], v[94:97]\n"
    "ds_read_b128 v[102:105], v3 offset:18496\n"
    "s_waitcnt vmcnt(21) lgkmcnt(1)\n"
    "v_mfma_f32_16x16x32_bf16 v[62:65], a[68:71], v[98:101], v[62:65]\n"
    "buffer_load_dwordx4 v[128:131], v2, s[72:75], s87 offen offset:0 sc1\n"
    "s_waitcnt vmcnt(21)\n"
    "v_mfma_f32_16x16x32_bf16 v[66:69], a[72:75], v[98:101], v[66:69]\n"
    "s_waitcnt vmcnt(20)\n"
    "v_mfma_f32_16x16x32_bf16 v[70:73], a[76:79], v[98:101], v[70:73]\n"
    "s_waitcnt vmcnt(19)\n"
    "v_mfma_f32_16x16x32_bf16 v[74:77], a[80:83], v[98:101], v[74:77]\n"
    "s_waitcnt vmcnt(18)\n"
    "v_mfma_f32_16x16x32_bf16 v[78:81], a[84:87], v[98:101], v[78:81]\n"
    "buffer_load_dwordx4 v[132:135], v2, s[72:75], s87 offen offset:1024 sc1\n"
    "s_waitcnt vmcnt(18)\n"
    "v_mfma_f32_16x16x32_bf16 v[82:85], a[88:91], v[98:101], v[82:85]\n"
    "s_waitcnt vmcnt(17)\n"
    "v_mfma_f32_16x16x32_bf16 v[86:89], a[92:95], v[98:101], v[86:89]\n"
    "v_min_u32 v10, 35, v159\n"
    "v_lshrrev_b32 v11, 2, v10\n"
    "s_waitcnt vmcnt(16)\n"
    "v_mfma_f32_16x16x32_bf16 v[90:93], a[96:99], v[98:101], v[90:93]\n"
    "v_and_b32 v10, 3, v159\n"
    "v_lshlrev_b32 v10, 3, v10\n"
    "s_waitcnt vmcnt(15)\n"
    "v_mfma_f32_16x16x32_bf16 v[94:97], a[100:103], v[98:101], v[94:97]\n"
    "v_lshl_add_u32 v11, v11, 7, v10\n"
    "s_lshl_b32 s90, s68, 7\n"
    "buffer_load_dwordx4 v[136:139], v2, s[72:75], s87 offen offset:2048 sc1\n"
    "s_waitcnt vmcnt(15) lgkmcnt(0)\n"
    "v_mfma_f32_16x16x32_bf16 v[62:65], a[104:107], v[102:105], v[62:65]\n"
    "v_lshl_add_u32 v16, v106, 3, s90\n"
    "s_waitcnt vmcnt(14)\n"
    "v_mfma_f32_16x16x32_bf16 v[66:69], a[108:111], v[102:105], v[66:69]\n"
    "s_waitcnt vmcnt(13)\n"
    "v_mfma_f32_16x16x32_bf16 v[70:73], a[112:115], v[102:105], v[70:73]\n"
    "s_waitcnt vmcnt(12)\n"
    "v_mfma_f32_16x16x32_bf16 v[74:77], a[116:119], v[102:105], v[74:77]\n"
    "buffer_load_dwordx4 v[140:143], v2, s[72:75], s87 offen offset:3072 sc1\n"
    "s_add_u32 s87, s87, 4096\n"
    "s_waitcnt vmcnt(12)\n"
    "v_mfma_f32_16x16x32_bf16 v[78:81], a[120:123], v[102:105], v[78:81]\n"
    "s_waitcnt vmcnt(11)\n"
    "v_mfma_f32_16x16x32_bf16 v[82:85], a[124:127], v[102:105], v[82:85]\n"
    "s_waitcnt vmcnt(10)\n"
    "v_mfma_f32_16x16x32_bf16 v[86:89], a[128:131], v[102:105], v[86:89]\n"
    "s_waitcnt vmcnt(9)\n"
    "v_mfma_f32_16x16x32_bf16 v[90:93], a[132:135], v[102:105], v[90:93]\n"
    "buffer_load_dwordx4 v[144:147], v2, s[72:75], s87 offen offset:0 sc1\n"
    "s_waitcnt vmcnt(9)\n"
    "v_mfma_f32_16x16x32_bf16 v[94:97], a[136:139], v[102:105], v[94:97]\n"
    "global_load_dwordx2 v[12:13], v11, s[76:77]\n"
    "global_load_dwordx2 v[14:15], v10, s[78:79] offset:1024\n"
    "global_load_dwordx2 v[16:17], v16, s[78:79]\n"
    "v_mov_b32 v36, 0\n"
    "v_mov_b32 v37, 0\n"
    "v_add_f32_dpp v62, v62, v62 row_ror:8 row_mask:0xf bank_mask:0x3\n"
    "v_add_f32_dpp v63, v63, v63 row_ror:8 row_mask:0xf bank_mask:0x3\n"
    "v_add_f32_dpp v64, v64, v64 row_ror:8 row_mask:0xf bank_mask:0x3\n"
    "v_add_f32_dpp v65, v65, v65 row_ror:8 row_mask:0xf bank_mask:0x3\n"
    "v_add_f32_dpp v62, v66, v66 row_ror:8 row_mask:0xf bank_mask:0xc\n"
    "v_add_f32_dpp v63, v67, v67 row_ror:8 row_mask:0xf bank_mask:0xc\n"
    "v_add_f32_dpp v64, v68, v68 row_ror:8 row_mask:0xf bank_mask:0xc\n"
    "v_add_f32_dpp v65, v69, v69 row_ror:8 row_mask:0xf bank_mask:0xc\n"
    "v_cvt_pk_bf16_f32 v26, v62, v63\n"
    "v_cvt_pk_bf16_f32 v27, v64, v65\n"
    "ds_write_b64 v54, v[26:27] offset:0\n"
    "v_add_f32_dpp v70, v70, v70 row_ror:8 row_mask:0xf bank_mask:0x3\n"
    "v_add_f32_dpp v71, v71, v71 row_ror:8 row_mask:0xf bank_mask:0x3\n"
    "v_add_f32_dpp v72, v72, v72 row_ror:8 row_mask:0xf bank_mask:0x3\n"
    "v_add_f32_dpp v73, v73, v73 row_ror:8 row_mask:0xf bank_mask:0x3\n"
    "v_add_f32_dpp v70, v74, v74 row_ror:8 row_mask:0xf bank_mask:0xc\n"
    "v_add_f32_dpp v71, v75, v75 row_ror:8 row_mask:0xf bank_mask:0xc\n"
    "v_add_f32_dpp v72, v76, v76 row_ror:8 row_mask:0xf bank_mask:0xc\n"
    "v_add_f32_dpp v73, v77, v77 row_ror:8 row_mask:0xf bank_mask:0xc\n"
    "v_cvt_pk_bf16_f32 v28, v70, v71\n"
    "v_cvt_pk_bf16_f32 v29, v72, v73\n"
    "ds_write_b64 v54, v[28:29] offset:2048\n"
    "buffer_load_dwordx4 v[148:151], v2, s[72:75], s87 offen offset:1024 sc1\n"
    "v_mfma_f32_16x16x32_bf16 v[18:21], v[108:111], v[26:29], 0\n"
    "v_mfma_f32_16x16x32_bf16 v[22:25], v[26:29], v[26:29], 0\n"
    "buffer_load_dwordx4 v[152:155], v2, s[72:75], s87 offen offset:2048 sc1\n"
    "buffer_load_dwordx4 v[156:159], v2, s[72:75], s87 offen offset:3072 sc1\n"
    "s_add_u32 s87, s87, 4096\n"
    "v_add_f32_dpp v78, v78, v78 row_ror:8 row_mask:0xf bank_mask:0x3\n"
    "v_add_f32_dpp v79, v79, v79 row_ror:8 row_mask:0xf bank_mask:0x3\n"
    "v_add_f32_dpp v80, v80, v80 row_ror:8 row_mask:0xf bank_mask:0x3\n"
    "v_add_f32_dpp v81, v81, v81 row_ror:8 row_mask:0xf bank_mask:0x3\n"
    "v_add_f32_dpp v78, v82, v82 row_ror:8 row_mask:0xf bank_mask:0xc\n"
    "v_add_f32_dpp v79, v83, v83 row_ror:8 row_mask:0xf bank_mask:0xc\n"
    "v_add_f32_dpp v80, v84, v84 row_ror:8 row_mask:0xf bank_mask:0xc\n"
    "v_add_f32_dpp v81, v85, v85 row_ror:8 row_mask:0xf bank_mask:0xc\n"
    "v_cvt_pk_bf16_f32 v30, v78, v79\n"
    "v_cvt_pk_bf16_f32 v31, v80, v81\n"
    "ds_write_b64 v54, v[30:31] offset:4096\n"
    "v_add_f32_dpp v86, v86, v86 row_ror:8 row_mask:0xf bank_mask:0x3\n"
    "v_add_f32_dpp v87, v87, v87 row_ror:8 row_mask:0xf bank_mask:0x3\n"
    "v_add_f32_dpp v88, v88, v88 row_ror:8 row_mask:0xf bank_mask:0x3\n"
    "v_add_f32_dpp v89, v89, v89 row_ror:8 row_mask:0xf bank_mask:0x3\n"
    "v_add_f32_dpp v86, v90, v90 row_ror:8 row_mask:0xf bank_mask:0xc\n"
    "v_add_f32_dpp v87, v91, v91 row_ror:8 row_mask:0xf bank_mask:0xc\n"
    "v_add_f32_dpp v88, v92, v92 row_ror:8 row_mask:0xf bank_mask:0xc\n"
    "v_add_f32_dpp v89, v93, v93 row_ror:8 row_mask:0xf bank_mask:0xc\n"
    "v_cvt_pk_bf16_f32 v32, v86, v87\n"
    "v_cvt_pk_bf16_f32 v33, v88, v89\n"
    "ds_write_b64 v54, v[32:33] offset:6144\n"
    "buffer_load_dwordx4 v[160:163], v2, s[72:75], s87 offen offset:0 sc1\n"
    "v_mfma_f32_16x16x32_bf16 v[18:21], v[108:111], v[30:33], v[18:21]\n"
    "v_mfma_f32_16x16x32_bf16 v[22:25], v[30:33], v[30:33], v[22:25]\n"
    "buffer_load_dwordx4 v[164:167], v2, s[72:75], s87 offen offset:1024 sc1\n"
    "buffer_load_dwordx4 v[168:171], v2, s[72:75], s87 offen offset:2048 sc1\n"
    "v_add_f32_dpp v94, v94, v94 row_ror:8 row_mask:0xf bank_mask:0x3\n"
    "v_add_f32_dpp v95, v95, v95 row_ror:8 row_mask:0xf bank_mask:0x3\n"
    "v_add_f32_dpp v96, v96, v96 row_ror:8 row_mask:0xf bank_mask:0x3\n"
    "v_add_f32_dpp v97, v97, v97 row_ror:8 row_mask:0xf bank_mask:0x3\n"
    "v_mov_b32_dpp v94, v36 row_ror:8 row_mask:0xf bank_mask:0xc\n"
    "v_mov_b32_dpp v95, v36 row_ror:8 row_mask:0xf bank_mask:0xc\n"
    "v_mov_b32_dpp v96, v36 row_ror:8 row_mask:0xf bank_mask:0xc\n"
    "v_mov_b32_dpp v97, v36 row_ror:8 row_mask:0xf bank_mask:0xc\n"
    "v_cvt_pk_bf16_f32 v34, v94, v95\n"
    "v_cvt_pk_bf16_f32 v35, v96, v97\n"
    "s_mov_b32 exec_lo, 0x00ff00ff\n"
    "s_mov_b32 exec_hi, 0x00ff00ff\n"
    "ds_write_b64 v54, v[34:35] offset:8192\n"
    "s_mov_b64 exec, -1\n"
    "v_mfma_f32_16x16x32_bf16 v[18:21], v[108:111], v[34:37], v[18:21]\n"
    "v_mfma_f32_16x16x32_bf16 v[22:25], v[34:37], v[34:37], v[22:25]\n"
    "buffer_load_dwordx4 v[172:175], v2, s[72:75], s87 offen offset:3072 sc1\n"
    "s_add_u32 s87, s87, 4096\n"
    "buffer_load_dwordx4 v[176:179], v2, s[72:75], s87 offen offset:0 sc1\n"
    "buffer_load_dwordx4 v[180:183], v2, s[72:75], s87 offen offset:1024 sc1\n"
    "s_add_u32 s87, s87, 2048\n"
    "buffer_load_dwordx4 v[184:187], v2, s[72:75], s87 offen offset:0 sc1\n"
    "s_nop 3\n"
    "v_cndmask_b32 v22, v22, v23, s[36:37]\n"
    "v_cndmask_b32 v24, v24, v25, s[36:37]\n"
    "v_cndmask_b32 v19, v22, v24, s[40:41]\n"
    "s_mov_b64 exec, s[94:95]\n"
    "ds_write_b64 v55, v[18:19]\n"
    "s_mov_b64 exec, -1\n"
    "s_waitcnt lgkmcnt(0)\n"
    "s_barrier\n"
    "ds_read_b128 v[62:65], v56 offset:0\n"
    "ds_read_b128 v[66:69], v56 offset:16\n"
    "ds_read_b128 v[70:73], v56 offset:32\n"
    "ds_read_b128 v[74:77], v56 offset:48\n"
    "ds_read_b128 v[24:27], v57 offset:0\n"
    "ds_read_b128 v[28:31], v57 offset:1024\n"
    "ds_read_b32 v32, v58\n"
    "buffer_load_dwordx4 v[188:191], v2, s[72:75], s87 offen offset:1024 sc1\n"
    "buffer_load_dwordx4 v[192:195], v2, s[72:75], s87 offen offset:2048 sc1\n"
    "s_waitcnt lgkmcnt(3)\n"
    "v_add_f32 v62, v62, v70\n"
    "v_add_f32 v63, v63, v71\n"
    "v_add_f32 v64, v64, v72\n"
    "v_add_f32 v65, v65, v73\n"
    "v_add_f32 v66, v66, v74\n"
    "v_add_f32 v67, v67, v75\n"
    "v_add_f32 v68, v68, v76\n"
    "v_add_f32 v69, v69, v77\n"
    "v_add_f32 v62, v62, v64\n"
    "v_add_f32 v63, v63, v65\n"
    "v_add_f32 v66, v66, v68\n"
    "v_add_f32 v67, v67, v69\n"
    "v_add_f32 v62, v62, v66\n"
    "v_add_f32 v63, v63, v67\n"
    "v_mul_f32 v62, 0x3ae38e39, v62\n"
    "v_mul_f32 v63, 0x3ae38e39, v63\n"
    "v_fma_f32 v63, -v62, v62, v63\n"
    "v_max_f32 v63, 0, v63\n"
    "v_cvt_pk_bf16_f32 v62, v62, v63\n"
    "v_and_b32 v63, 0xffff0000, v62\n"
    "v_lshlrev_b32 v62, 16, v62\n"
    "v_add_f32 v63, 0x3727c5ac, v63\n"
    "v_cvt_pk_bf16_f32 v63, v63, v63\n"
    "v_lshlrev_b32 v63, 16, v63\n"
    "v_sqrt_f32 v63, v63\n"
    "s_waitcnt lgkmcnt(0)\n"
    "v_cvt_pk_bf16_f32 v63, v63, v63\n"
    "v_lshlrev_b32 v63, 16, v63\n"
    "v_rcp_f32 v63, v63\n"
    "v_lshlrev_b32 v18, 16, v24\n"
    "v_and_b32 v19, 0xffff0000, v24\n"
    "v_sub_f32 v18, v18, v62\n"
    "v_sub_f32 v19, v19, v62\n"
    "v_cvt_pk_bf16_f32 v18, v18, v19\n"
    "v_and_b32 v19, 0xffff0000, v18\n"
    "v_lshlrev_b32 v18, 16, v18\n"
    "v_mul_f32 v18, v18, v63\n"
    "v_mul_f32 v19, v19, v63\n"
    "v_cvt_pk_bf16_f32 v24, v18, v19\n"
    "v_min_u32 v24, -2, v24\n"
    "v_lshlrev_b32 v18, 16, v25\n"
    "v_and_b32 v19, 0xffff0000, v25\n"
    "v_sub_f32 v18, v18, v62\n"
    "v_sub_f32 v19, v19, v62\n"
    "v_cvt_pk_bf16_f32 v18, v18, v19\n"
    "v_and_b32 v19, 0xffff0000, v18\n"
    "v_lshlrev_b32 v18, 16, v18\n"
    "v_mul_f32 v18, v18, v63\n"
    "v_mul_f32 v19, v19, v63\n"
    "v_cvt_pk_bf16_f32 v25, v18, v19\n"
    "v_min_u32 v25, -2, v25\n"
    "v_lshlrev_b32 v18, 16, v26\n"
    "v_and_b32 v19, 0xffff0000, v26\n"
    "v_sub_f32 v18, v18, v62\n"
    "v_sub_f32 v19, v19, v62\n"
    "v_cvt_pk_bf16_f32 v18, v18, v19\n"
    "v_and_b32 v19, 0xffff0000, v18\n"
    "v_lshlrev_b32 v18, 16, v18\n"
    "v_mul_f32 v18, v18, v63\n"
    "v_mul_f32 v19, v19, v63\n"
    "v_cvt_pk_bf16_f32 v26, v18, v19\n"
    "v_min_u32 v26, -2, v26\n"
    "v_lshlrev_b32 v18, 16, v27\n"
    "v_and_b32 v19, 0xffff0000, v27\n"
    "v_sub_f32 v18, v18, v62\n"
    "v_sub_f32 v19, v19, v62\n"
    "v_cvt_pk_bf16_f32 v18, v18, v19\n"
    "v_and_b32 v19, 0xffff0000, v18\n"
    "v_lshlrev_b32 v18, 16, v18\n"
    "v_mul_f32 v18, v18, v63\n"
    "v_mul_f32 v19, v19, v63\n"
    "v_cvt_pk_bf16_f32 v27, v18, v19\n"
    "v_min_u32 v27, -2, v27\n"
    "global_store_dwordx4 v60, v[24:27], s[62:63] offset:0 sc1\n"
    "v_lshlrev_b32 v18, 16, v28\n"
    "v_and_b32 v19, 0xffff0000, v28\n"
    "v_sub_f32 v18, v18, v62\n"
    "v_sub_f32 v19, v19, v62\n"
    "v_cvt_pk_bf16_f32 v18, v18, v19\n"
    "v_and_b32 v19, 0xffff0000, v18\n"
    "v_lshlrev_b32 v18, 16, v18\n"
    "v_mul_f32 v18, v18, v63\n"
    "v_mul_f32 v19, v19, v63\n"
    "v_cvt_pk_bf16_f32 v28, v18, v19\n"
    "v_min_u32 v28, -2, v28\n"
    "v_lshlrev_b32 v18, 16, v29\n"
    "v_and_b32 v19, 0xffff0000, v29\n"
    "v_sub_f32 v18, v18, v62\n"
    "v_sub_f32 v19, v19, v62\n"
    "v_cvt_pk_bf16_f32 v18, v18, v19\n"
    "v_and_b32 v19, 0xffff0000, v18\n"
    "v_lshlrev_b32 v18, 16, v18\n"
    "v_mul_f32 v18, v18, v63\n"
    "v_mul_f32 v19, v19, v63\n"
    "v_cvt_pk_bf16_f32 v29, v18, v19\n"
    "v_min_u32 v29, -2, v29\n"
    "v_lshlrev_b32 v18, 16, v30\n"
    "v_and_b32 v19, 0xffff0000, v30\n"
    "v_sub_f32 v18, v18, v62\n"
    "v_sub_f32 v19, v19, v62\n"
    "v_cvt_pk_bf16_f32 v18, v18, v19\n"
    "v_and_b32 v19, 0xffff0000, v18\n"
    "v_lshlrev_b32 v18, 16, v18\n"
    "v_mul_f32 v18, v18, v63\n"
    "v_mul_f32 v19, v19, v63\n"
    "v_cvt_pk_bf16_f32 v30, v18, v19\n"
    "v_min_u32 v30, -2, v30\n"
    "v_lshlrev_b32 v18, 16, v31\n"
    "v_and_b32 v19, 0xffff0000, v31\n"
    "v_sub_f32 v18, v18, v62\n"
    "v_sub_f32 v19, v19, v62\n"
    "v_cvt_pk_bf16_f32 v18, v18, v19\n"
    "v_and_b32 v19, 0xffff0000, v18\n"
    "v_lshlrev_b32 v18, 16, v18\n"
    "v_mul_f32 v18, v18, v63\n"
    "v_mul_f32 v19, v19, v63\n"
    "v_cvt_pk_bf16_f32 v31, v18, v19\n"
    "v_min_u32 v31, -2, v31\n"
    "global_store_dwordx4 v60, v[28:31], s[62:63] offset:1024 sc1\n"
    "v_lshlrev_b32 v18, 16, v32\n"
    "v_and_b32 v19, 0xffff0000, v32\n"
    "v_sub_f32 v18, v18, v62\n"
    "v_sub_f32 v19, v19, v62\n"
    "v_cvt_pk_bf16_f32 v18, v18, v19\n"
    "v_and_b32 v19, 0xffff0000, v18\n"
    "v_lshlrev_b32 v18, 16, v18\n"
    "v_mul_f32 v18, v18, v63\n"
    "v_mul_f32 v19, v19, v63\n"
    "v_cvt_pk_bf16_f32 v32, v18, v19\n"
    "v_min_u32 v32, -2, v32\n"
    "global_store_dword v59, v32, s[62:63] sc1\n"
    "v_bfe_u32 v18, v2, 6, 2\n"
    "v_cmp_eq_u32 vcc, v106, v18\n"
    "v_bfe_u32 v18, v2, 4, 1\n"
    "v_lshlrev_b32 v18, 4, v18\n"
    "buffer_load_dwordx4 v[196:199], v2, s[72:75], s87 offen offset:3072 sc1\n"
    "s_add_u32 s87, s87, 4096\n"
    "v_mov_b32 v45, 0x3f80\n"
    "v_lshlrev_b32 v45, v18, v45\n"
    "v_cndmask_b32 v45, 0, v45, vcc\n"
    "v_bfe_u32 v18, v2, 5, 1\n"
    "buffer_load_dwordx4 v[200:203], v2, s[72:75], s87 offen offset:0 sc1\n"
    "v_cmp_eq_u32 vcc, 0, v18\n"
    "v_mov_b32 v46, 0\n"
    "v_mov_b32 v47, 0\n"
    "v_cndmask_b32 v42, 0, v45, vcc\n"
    "buffer_load_dwordx4 v[204:207], v2, s[72:75], s87 offen offset:1024 sc1\n"
    "v_sub_u32 v43, v45, v42\n"
    "v_mov_b32 v44, 0\n"
    "v_mov_b32 v45, 0\n"
    "v_mov_b32 v48, v42\n"
    "buffer_load_dwordx4 v[208:211], v2, s[72:75], s87 offen offset:2048 sc1\n"
    "v_mov_b32 v49, v43\n"
    "s_bitcmp1_b32 s67, 0\n"
    "s_cselect_b64 vcc, -1, 0\n"
    "v_cndmask_b32 v76, v42, v46, vcc\n"
    "buffer_load_dwordx4 v[212:215], v2, s[72:75], s87 offen offset:3072 sc1\n"
    "s_add_u32 s87, s87, 4096\n"
    "v_cndmask_b32 v77, v43, v47, vcc\n"
    "v_cndmask_b32 v78, v44, v48, vcc\n"
    "v_cndmask_b32 v79, v45, v49, vcc\n"
    "v_lshrrev_b32 v20, 1, v107\n"
    "buffer_load_dwordx4 v[216:219], v2, s[72:75], s87 offen offset:0 sc1\n"
    "v_and_b32 v21, 1, v107\n"
    "v_mul_u32_u24 v3, 9248, v20\n"
    "v_lshl_add_u32 v3, v106, 8, v3\n"
    "v_lshl_add_u32 v3, v21, 4, v3\n"
    "buffer_load_dwordx4 v[220:223], v2, s[72:75], s87 offen offset:1024 sc1\n"
    "v_add_u32 v3, s66, v3\n"
    "s_lshr_b32 s84, s67, 1\n"
    "s_lshl_b32 s90, s84, 7\n"
    "s_add_u32 s90, s90, s91\n"
    "buffer_load_dwordx4 v[224:227], v2, s[72:75], s87 offen offset:2048 sc1\n"
    "v_lshlrev_b32 v9, 8, v106\n"
    "v_lshl_add_u32 v9, v21, 4, v9\n"
    "v_add_u32 v9, s90, v9\n"
    "v_cmp_eq_u32 s[94:95], s68, v20\n"
    "buffer_load_dwordx4 v[228:231], v2, s[72:75], s87 offen offset:3072 sc1\n"
    "s_add_u32 s87, s87, 4096\n"
    "s_mul_i32 s90, s67, 18496\n"
    "s_add_u32 s90, s90, s66\n"
    "v_add_u32 v108, s90, v2\n"
    "s_lshl_b32 s90, s68, 11\n"
    "buffer_load_dwordx4 v[232:235], v2, s[72:75], s87 offen offset:0 sc1\n"
    "s_add_u32 s50, s82, s90\n"
    "s_addc_u32 s51, s83, 0\n"
    "s_lshl_b32 s90, s68, 16\n"
    "s_add_u32 s52, s82, s90\n"
    "s_addc_u32 s53, s83, 0\n"
    "buffer_load_dwordx4 v[236:239], v2, s[72:75], s87 offen offset:1024 sc1\n"
    "s_mov_b64 s[36:37], s[46:47]\n"
    "s_add_u32 s38, s46, 8192\n"
    "s_addc_u32 s39, s47, 0\n"
    "v_add_u32 v75, 8192, v9\n"
    "buffer_load_dwordx4 v[240:243], v2, s[72:75], s87 offen offset:2048 sc1\n"
    "s_mov_b32 s89, 0\n"
    "buffer_load_dwordx4 v[244:247], v2, s[72:75], s87 offen offset:3072 sc1\n"
    "s_add_u32 s87, s87, 4096\n"
    "buffer_load_dwordx4 v[248:251], v2, s[72:75], s87 offen offset:0 sc1\n"
    "buffer_load_dwordx4 v[252:255], v2, s[72:75], s87 offen offset:1024 sc1\n"
    "s_add_u32 s87, s87, 2048\n"
    "s_waitcnt vmcnt(30)\n"
    "v_lshrrev_b32 v18, 4, v2\n"
    "v_cmp_le_u32 vcc, 36, v18\n"
    "s_cmp_eq_u32 s68, 0\n"
    "s_cselect_b32 s90, -1, 0\n"
    "v_and_b32 v14, s90, v14\n"
    "v_and_b32 v15, s90, v15\n"
    "v_cndmask_b32 v12, v12, v14, vcc\n"
    "v_cndmask_b32 v13, v13, v15, vcc\n"
    "v_add_u32 v11, s88, v2\n"
    "v_lshlrev_b32 v20, 16, v12\n"
    "v_and_b32 v21, 0xffff0000, v12\n"
    "v_lshlrev_b32 v22, 16, v13\n"
    "v_and_b32 v23, 0xffff0000, v13\n"
    "ds_write_b128 v11, v[20:23]\n"
    "v_lshlrev_b32 v54, 16, v16\n"
    "v_and_b32 v55, 0xffff0000, v16\n"
    "v_lshlrev_b32 v56, 16, v17\n"
    "v_and_b32 v57, 0xffff0000, v17\n"
    "s_waitcnt lgkmcnt(0)\n"
    "124:\n"
    "s_add_u32 s40, s64, 4096\n"
    "s_addc_u32 s41, s65, 0\n"
    "global_load_dwordx4 v[10:13], v2, s[40:41] offset:-4096 sc1\n"
    "global_load_dwordx4 v[14:17], v2, s[40:41] offset:-3072 sc1\n"
    "global_load_dwordx4 v[18:21], v2, s[40:41] offset:-2048 sc1\n"
    "global_load_dwordx4 v[22:25], v2, s[40:41] offset:-1024 sc1\n"
    "global_load_dwordx4 v[26:29], v2, s[40:41] offset:0 sc1\n"
    "global_load_dwordx4 v[30:33], v2, s[40:41] offset:1024 sc1\n"
    "global_load_dwordx4 v[34:37], v2, s[40:41] offset:2048 sc1\n"
    "global_load_dwordx4 v[38:41], v2, s[40:41] offset:3072 sc1\n"
    "s_add_u32 s40, s40, 8192\n"
    "s_addc_u32 s41, s41, 0\n"
    "global_load_dwordx4 v[58:61], v2, s[40:41] offset:-4096 sc1\n"
    "global_load_dwordx4 v[62:65], v2, s[40:41] offset:-3072 sc1\n"
    "global_load_dwordx4 v[66:69], v2, s[40:41] offset:-2048 sc1\n"
    "global_load_dwordx4 v[70:73], v2, s[40:41] offset:-1024 sc1\n"
    "global_load_dwordx4 v[80:83], v2, s[40:41] offset:0 sc1\n"
    "global_load_dwordx4 v[84:87], v2, s[40:41] offset:1024 sc1\n"
    "global_load_dwordx4 v[88:91], v2, s[40:41] offset:2048 sc1\n"
    "global_load_dwordx4 v[92:95], v2, s[40:41] offset:3072 sc1\n"
    "s_add_u32 s40, s40, 8192\n"
    "s_addc_u32 s41, s41, 0\n"
    "global_load_dwordx4 v[96:99], v2, s[40:41] offset:-4096 sc1\n"
    "global_load_dwordx4 v[100:103], v2, s[40:41] offset:-3072 sc1\n"
    "127:\n"
    "s_waitcnt vmcnt(17)\n"
    "ds_write_b128 v108, v[10:13] offset:0\n"
    "v_max_u32 v110, v10, v11\n"
    "v_max3_u32 v110, v110, v12, v13\n"
    "s_waitcnt vmcnt(16)\n"
    "ds_write_b128 v108, v[14:17] offset:1024\n"
    "v_max3_u32 v110, v110, v14, v15\n"
    "v_max3_u32 v110, v110, v16, v17\n"
    "s_waitcnt vmcnt(15)\n"
    "ds_write_b128 v108, v[18:21] offset:2048\n"
    "v_max3_u32 v110, v110, v18, v19\n"
    "v_max3_u32 v110, v110, v20, v21\n"
    "s_waitcnt vmcnt(14)\n"
    "ds_write_b128 v108, v[22:25] offset:3072\n"
    "v_max3_u32 v110, v110, v22, v23\n"
    "v_max3_u32 v110, v110, v24, v25\n"
    "s_waitcnt vmcnt(13)\n"
    "ds_write_b128 v108, v[26:29] offset:4096\n"
    "v_max3_u32 v110, v110, v26, v27\n"
    "v_max3_u32 v110, v110, v28, v29\n"
    "s_waitcnt vmcnt(12)\n"
    "ds_write_b128 v108, v[30:33] offset:5120\n"
    "v_max3_u32 v110, v110, v30, v31\n"
    "v_max3_u32 v110, v110, v32, v33\n"
    "s_waitcnt vmcnt(11)\n"
    "ds_write_b128 v108, v[34:37] offset:6144\n"
    "v_max3_u32 v110, v110, v34, v35\n"
    "v_max3_u32 v110, v110, v36, v37\n"
    "s_waitcnt vmcnt(10)\n"
    "ds_write_b128 v108, v[38:41] offset:7168\n"
    "v_max3_u32 v110, v110, v38, v39\n"
    "v_max3_u32 v110, v110, v40, v41\n"
    "s_waitcnt vmcnt(9)\n"
    "ds_write_b128 v108, v[58:61] offset:8192\n"
    "v_max3_u32 v110, v110, v58, v59\n"
    "v_max3_u32 v110, v110, v60, v61\n"
    "s_waitcnt vmcnt(8)\n"
    "ds_write_b128 v108, v[62:65] offset:9248\n"
    "v_max3_u32 v110, v110, v62, v63\n"
    "v_max3_u32 v110, v110, v64, v65\n"
    "s_waitcnt vmcnt(7)\n"
    "ds_write_b128 v108, v[66:69] offset:10272\n"
    "v_max3_u32 v110, v110, v66, v67\n"
    "v_max3_u32 v110, v110, v68, v69\n"
    "s_waitcnt vmcnt(6)\n"
    "ds_write_b128 v108, v[70:73] offset:11296\n"
    "v_max3_u32 v110, v110, v70, v71\n"
    "v_max3_u32 v110, v110, v72, v73\n"
    "s_waitcnt vmcnt(5)\n"
    "ds_write_b128 v108, v[80:83] offset:12320\n"
    "v_max3_u32 v110, v110, v80, v81\n"
    "v_max3_u32 v110, v110, v82, v83\n"
    "s_waitcnt vmcnt(4)\n"
    "ds_write_b128 v108, v[84:87] offset:13344\n"
    "v_max3_u32 v110, v110, v84, v85\n"
    "v_max3_u32 v110, v110, v86, v87\n"
    "s_waitcnt vmcnt(3)\n"
    "ds_write_b128 v108, v[88:91] offset:14368\n"
    "v_max3_u32 v110, v110, v88, v89\n"
    "v_max3_u32 v110, v110, v90, v91\n"
    "s_waitcnt vmcnt(2)\n"
    "ds_write_b128 v108, v[92:95] offset:15392\n"
    "v_max3_u32 v110, v110, v92, v93\n"
    "v_max3_u32 v110, v110, v94, v95\n"
    "s_waitcnt vmcnt(1)\n"
    "ds_write_b128 v108, v[96:99] offset:16416\n"
    "v_max3_u32 v110, v110, v96, v97\n"
    "v_max3_u32 v110, v110, v98, v99\n"
    "s_waitcnt vmcnt(0)\n"
    "ds_write_b128 v108, v[100:103] offset:17440\n"
    "v_max3_u32 v110, v110, v100, v101\n"
    "v_max3_u32 v110, v110, v102, v103\n"
    "v_cmp_eq_u32 vcc, -1, v110\n"
    "s_cbranch_vccnz 128f\n"
    "129:\n"
    "s_waitcnt lgkmcnt(0)\n"
    "s_barrier\n"
    "ds_read_b128 v[50:53], v5\n"
    "ds_read_b128 a[0:3], v3 offset:0\n"
    "ds_read_b128 a[4:7], v3 offset:32\n"
    "ds_read_b128 a[8:11], v3 offset:64\n"
    "ds_read_b128 a[12:15], v3 offset:96\n"
    "ds_read_b128 a[16:19], v3 offset:128\n"
    "ds_read_b128 a[20:23], v3 offset:160\n"
    "ds_read_b128 a[24:27], v3 offset:192\n"
    "ds_read_b128 a[28:31], v3 offset:224\n"
    "ds_read_b128 a[32:35], v3 offset:1024\n"
    "ds_read_b128 a[36:39], v3 offset:1056\n"
    "ds_read_b128 a[40:43], v3 offset:1088\n"
    "ds_read_b128 a[44:47], v3 offset:1120\n"
    "s_waitcnt vmcnt(35) lgkmcnt(8)\n"
    "v_mfma_f32_16x16x32_bf16 v[10:13], v[112:115], a[0:3], v[50:53]\n"
    "ds_read_b128 a[48:51], v3 offset:1152\n"
    "v_mfma_f32_16x16x32_bf16 v[14:17], v[112:115], a[4:7], v[50:53]\n"
    "ds_read_b128 a[52:55], v3 offset:1184\n"
    "v_mfma_f32_16x16x32_bf16 v[18:21], v[112:115], a[8:11], v[50:53]\n"
    "ds_read_b128 a[56:59], v3 offset:1216\n"
    "v_mfma_f32_16x16x32_bf16 v[22:25], v[112:115], a[12:15], v[50:53]\n"
    "ds_read_b128 a[60:63], v3 offset:1248\n"
    "buffer_load_dwordx4 v[112:115], v2, s[72:75], s87 offen offset:0 sc1\n"
    "s_waitcnt vmcnt(35) lgkmcnt(8)\n"
    "v_mfma_f32_16x16x32_bf16 v[10:13], v[116:119], a[16:19], v[10:13]\n"
    "ds_read_b128 a[64:67], v3 offset:2048\n"
    "v_mfma_f32_16x16x32_bf16 v[14:17], v[116:119], a[20:23], v[14:17]\n"
    "ds_read_b128 a[68:71], v3 offset:2080\n"
    "v_mfma_f32_16x16x32_bf16 v[18:21], v[116:119], a[24:27], v[18:21]\n"
    "ds_read_b128 a[72:75], v3 offset:2112\n"
    "v_mov_b32 v58, 0\n"
    "v_mfma_f32_16x16x32_bf16 v[22:25], v[116:119], a[28:31], v[22:25]\n"
    "ds_read_b128 a[76:79], v3 offset:2144\n"
    "buffer_load_dwordx4 v[116:119], v2, s[72:75], s87 offen offset:1024 sc1\n"
    "s_waitcnt vmcnt(35) lgkmcnt(8)\n"
    "v_mfma_f32_16x16x32_bf16 v[10:13], v[120:123], a[32:35], v[10:13]\n"
    "ds_read_b128 a[80:83], v3 offset:2176\n"
    "v_mov_b32 v59, 0\n"
    "v_mfma_f32_16x16x32_bf16 v[14:17], v[120:123], a[36:39], v[14:17]\n"
    "ds_read_b128 a[84:87], v3 offset:2208\n"
    "v_mov_b32 v60, 0\n"
    "v_mfma_f32_16x16x32_bf16 v[18:21], v[120:123], a[40:43], v[18:21]\n"
    "ds_read_b128 a[88:91], v3 offset:2240\n"
    "v_mov_b32 v61, 0\n"
    "v_mfma_f32_16x16x32_bf16 v[22:25], v[120:123], a[44:47], v[22:25]\n"
    "ds_read_b128 a[92:95], v3 offset:2272\n"
    "buffer_load_dwordx4 v[120:123], v2, s[72:75], s87 offen offset:2048 sc1\n"
    "s_waitcnt vmcnt(35) lgkmcnt(8)\n"
    "v_mfma_f32_16x16x32_bf16 v[10:13], v[124:127], a[48:51], v[10:13]\n"
    "ds_read_b128 a[96:99], v3 offset:3072\n"
    "v_mov_b32 v62, 0\n"
    "v_mfma_f32_16x16x32_bf16 v[14:17], v[124:127], a[52:55], v[14:17]\n"
    "ds_read_b128 a[100:103], v3 offset:3104\n"
    "v_mov_b32 v63, 0\n"
    "v_mfma_f32_16x16x32_bf16 v[18:21], v[124:127], a[56:59], v[18:21]\n"
    "ds_read_b128 a[104:107], v3 offset:3136\n"
    "v_mov_b32 v64, 0\n"
    "v_mfma_f32_16x16x32_bf16 v[22:25], v[124:127], a[60:63], v[22:25]\n"
    "ds_read_b128 a[108:111], v3 offset:3168\n"
    "buffer_load_dwordx4 v[124:127], v2, s[72:75], s87 offen offset:3072 sc1\n"
    "s_add_u32 s87, s87, 4096\n"
    "s_waitcnt vmcnt(35) lgkmcnt(8)\n"
    "v_mfma_f32_16x16x32_bf16 v[10:13], v[128:131], a[64:67], v[10:13]\n"
    "ds_read_b128 a[112:115], v3 offset:3200\n"
    "v_mov_b32 v65, 0\n"
    "v_mfma_f32_16x16x32_bf16 v[14:17], v[128:131], a[68:71], v[14:17]\n"
    "ds_read_b128 a[116:119], v3 offset:3232\n"
    "v_mov_b32 v66, 0\n"
    "v_mfma_f32_16x16x32_bf16 v[18:21], v[128:131], a[72:75], v[18:21]\n"
    "ds_read_b128 a[120:123], v3 offset:3264\n"
    "v_mov_b32 v67, 0\n"
    "v_mfma_f32_16x16x32_bf16 v[22:25], v[128:131], a[76:79], v[22:25]\n"
    "ds_read_b128 a[124:127], v3 offset:3296\n"
    "buffer_load_dwordx4 v[128:131], v2, s[72:75], s87 offen offset:0 sc1\n"
    "s_waitcnt vmcnt(35) lgkmcnt(8)\n"
    "v_mfma_f32_16x16x32_bf16 v[10:13], v[132:135], a[80:83], v[10:13]\n"
    "ds_read_b128 a[128:131], v3 offset:4096\n"
    "v_mov_b32 v68, 0\n"
    "v_mfma_f32_16x16x32_bf16 v[14:17], v[132:135], a[84:87], v[14:17]\n"
    "ds_read_b128 a[132:135], v3 offset:4128\n"
    "v_mov_b32 v69, 0\n"
    "v_mfma_f32_16x16x32_bf16 v[18:21], v[132:135], a[88:91], v[18:21]\n"
    "ds_read_b128 a[136:139], v3 offset:4160\n"
    "v_mov_b32 v70, 0\n"
    "v_mfma_f32_16x16x32_bf16 v[22:25], v[132:135], a[92:95], v[22:25]\n"
    "ds_read_b128 a[140:143], v3 offset:4192\n"
    "buffer_load_dwordx4 v[132:135], v2, s[72:75], s87 offen offset:1024 sc1\n"
    "s_waitcnt vmcnt(35) lgkmcnt(8)\n"
    "v_mfma_f32_16x16x32_bf16 v[10:13], v[136:139], a[96:99], v[10:13]\n"
    "ds_read_b128 a[144:147], v3 offset:4224\n"
    "v_mov_b32 v71, 0\n"
    "v_mfma_f32_16x16x32_bf16 v[14:17], v[136:139], a[100:103], v[14:17]\n"
    "ds_read_b128 a[148:151], v3 offset:4256\n"
    "v_mov_b32 v72, 0\n"
    "v_mfma_f32_16x16x32_bf16 v[18:21], v[136:139], a[104:107], v[18:21]\n"
    "ds_read_b128 a[152:155], v3 offset:4288\n"
    "v_mov_b32 v73, 0\n"
    "v_mfma_f32_16x16x32_bf16 v[22:25], v[136:139], a[108:111], v[22:25]\n"
    "ds_read_b128 a[156:159], v3 offset:4320\n"
    "buffer_load_dwordx4 v[136:139], v2, s[72:75], s87 offen offset:2048 sc1\n"
    "s_waitcnt vmcnt(35) lgkmcnt(8)\n"
    "v_mfma_f32_16x16x32_bf16 v[10:13], v[140:143], a[112:115], v[10:13]\n"
    "ds_read_b128 a[160:163], v3 offset:5120\n"
    "v_mfma_f32_16x16x32_bf16 v[14:17], v[140:143], a[116:119], v[14:17]\n"
    "ds_read_b128 a[164:167], v3 offset:5152\n"
    "v_mfma_f32_16x16x32_bf16 v[18:21], v[140:143], a[120:123], v[18:21]\n"
    "ds_read_b128 a[168:171], v3 offset:5184\n"
    "v_mfma_f32_16x16x32_bf16 v[22:25], v[140:143], a[124:127], v[22:25]\n"
    "ds_read_b128 a[172:175], v3 offset:5216\n"
    "buffer_load_dwordx4 v[140:143], v2, s[72:75], s87 offen offset:3072 sc1\n"
    "s_add_u32 s87, s87, 4096\n"
    "s_waitcnt vmcnt(35) lgkmcnt(8)\n"
    "v_mfma_f32_16x16x32_bf16 v[10:13], v[144:147], a[128:131], v[10:13]\n"
    "ds_read_b128 a[176:179], v3 offset:5248\n"
    "v_mfma_f32_16x16x32_bf16 v[14:17], v[144:147], a[132:135], v[14:17]\n"
    "ds_read_b128 a[180:183], v3 offset:5280\n"
    "v_mfma_f32_16x16x32_bf16 v[18:21], v[144:147], a[136:139], v[18:21]\n"
    "ds_read_b128 a[184:187], v3 offset:5312\n"
    "v_mfma_f32_16x16x32_bf16 v[22:25], v[144:147], a[140:143], v[22:25]\n"
    "ds_read_b128 a[188:191], v3 offset:5344\n"
    "buffer_load_dwordx4 v[144:147], v2, s[72:75], s87 offen offset:0 sc1\n"
    "s_waitcnt vmcnt(35) lgkmcnt(8)\n"
    "v_mfma_f32_16x16x32_bf16 v[10:13], v[148:151], a[144:147], v[10:13]\n"
    "ds_read_b128 a[192:195], v3 offset:6144\n"
    "v_mfma_f32_16x16x32_bf16 v[14:17], v[148:151], a[148:151], v[14:17]\n"
    "ds_read_b128 a[196:199], v3 offset:6176\n"
    "v_mfma_f32_16x16x32_bf16 v[18:21], v[148:151], a[152:155], v[18:21]\n"
    "ds_read_b128 a[200:203], v3 offset:6208\n"
    "v_mfma_f32_16x16x32_bf16 v[22:25], v[148:151], a[156:159], v[22:25]\n"
    "ds_read_b128 a[204:207], v3 offset:6240\n"
    "buffer_load_dwordx4 v[148:151], v2, s[72:75], s87 offen offset:1024 sc1\n"
    "s_waitcnt vmcnt(35) lgkmcnt(8)\n"
    "v_mfma_f32_16x16x32_bf16 v[10:13], v[152:155], a[160:163], v[10:13]\n"
    "ds_read_b128 a[208:211], v3 offset:6272\n"
    "v_mfma_f32_16x16x32_bf16 v[14:17], v[152:155], a[164:167], v[14:17]\n"
    "ds_read_b128 a[212:215], v3 offset:6304\n"
    "v_mfma_f32_16x16x32_bf16 v[18:21], v[152:155], a[168:171], v[18:21]\n"
    "ds_read_b128 a[216:219], v3 offset:6336\n"
    "v_mfma_f32_16x16x32_bf16 v[22:25], v[152:155], a[172:175], v[22:25]\n"
    "ds_read_b128 a[220:223], v3 offset:6368\n"
    "buffer_load_dwordx4 v[152:155], v2, s[72:75], s87 offen offset:2048 sc1\n"
    "s_waitcnt vmcnt(35) lgkmcnt(8)\n"
    "v_mfma_f32_16x16x32_bf16 v[10:13], v[156:159], a[176:179], v[10:13]\n"
    "ds_read_b128 a[224:227], v3 offset:7168\n"
    "v_mfma_f32_16x16x32_bf16 v[14:17], v[156:159], a[180:183], v[14:17]\n"
    "ds_read_b128 a[228:231], v3 offset:7200\n"
    "v_mfma_f32_16x16x32_bf16 v[18:21], v[156:159], a[184:187], v[18:21]\n"
    "ds_read_b128 a[232:235], v3 offset:7232\n"
    "v_mfma_f32_16x16x32_bf16 v[22:25], v[156:159], a[188:191], v[22:25]\n"
    "ds_read_b128 a[236:239], v3 offset:7264\n"
    "buffer_load_dwordx4 v[156:159], v2, s[72:75], s87 offen offset:3072 sc1\n"
    "s_add_u32 s87, s87, 4096\n"
    "s_waitcnt vmcnt(35) lgkmcnt(8)\n"
    "v_mfma_f32_16x16x32_bf16 v[10:13], v[160:163], a[192:195], v[10:13]\n"
    "ds_read_b128 a[240:243], v3 offset:7296\n"
    "v_mfma_f32_16x16x32_bf16 v[14:17], v[160:163], a[196:199], v[14:17]\n"
    "ds_read_b128 a[244:247], v3 offset:7328\n"
    "v_mfma_f32_16x16x32_bf16 v[18:21], v[160:163], a[200:203], v[18:21]\n"
    "ds_read_b128 a[248:251], v3 offset:7360\n"
    "v_mfma_f32_16x16x32_bf16 v[22:25], v[160:163], a[204:207], v[22:25]\n"
    "ds_read_b128 a[252:255], v3 offset:7392\n"
    "buffer_load_dwordx4 v[160:163], v2, s[72:75], s87 offen offset:0 sc1\n"
    "s_waitcnt vmcnt(35) lgkmcnt(8)\n"
    "v_mfma_f32_16x16x32_bf16 v[10:13], v[164:167], a[208:211], v[10:13]\n"
    "ds_read_b128 v[80:83], v3 offset:8192\n"
    "v_mfma_f32_16x16x32_bf16 v[14:17], v[164:167], a[212:215], v[14:17]\n"
    "ds_read_b128 v[84:87], v3 offset:8224\n"
    "v_mfma_f32_16x16x32_bf16 v[18:21], v[164:167], a[216:219], v[18:21]\n"
    "ds_read_b128 v[88:91], v3 offset:8256\n"
    "v_mfma_f32_16x16x32_bf16 v[22:25], v[164:167], a[220:223], v[22:25]\n"
    "ds_read_b128 v[92:95], v3 offset:8288\n"
    "buffer_load_dwordx4 v[164:167], v2, s[72:75], s87 offen offset:1024 sc1\n"
    "s_waitcnt vmcnt(35) lgkmcnt(8)\n"
    "v_mfma_f32_16x16x32_bf16 v[10:13], v[168:171], a[224:227], v[10:13]\n"
    "ds_read_b128 v[96:99], v3 offset:8320\n"
    "v_mfma_f32_16x16x32_bf16 v[14:17], v[168:171], a[228:231], v[14:17]\n"
    "ds_read_b128 v[100:103], v3 offset:8352\n"
    "v_mfma_f32_16x16x32_bf16 v[18:21], v[168:171], a[232:235], v[18:21]\n"
    "ds_read_b128 v[104:107], v3 offset:8384\n"
    "v_mfma_f32_16x16x32_bf16 v[22:25], v[168:171], a[236:239], v[22:25]\n"
    "ds_read_b128 v[108:111], v3 offset:8416\n"
    "buffer_load_dwordx4 v[168:171], v2, s[72:75], s87 offen offset:2048 sc1\n"
    "s_waitcnt vmcnt(35) lgkmcnt(8)\n"
    "v_mfma_f32_16x16x32_bf16 v[10:13], v[172:175], a[240:243], v[10:13]\n"
    "v_mfma_f32_16x16x32_bf16 v[14:17], v[172:175], a[244:247], v[14:17]\n"
    "v_mfma_f32_16x16x32_bf16 v[18:21], v[172:175], a[248:251], v[18:21]\n"
    "v_mfma_f32_16x16x32_bf16 v[22:25], v[172:175], a[252:255], v[22:25]\n"
    "buffer_load_dwordx4 v[172:175], v2, s[72:75], s87 offen offset:3072 sc1\n"
    "s_add_u32 s87, s87, 4096\n"
    "s_waitcnt vmcnt(35) lgkmcnt(4)\n"
    "v_mfma_f32_16x16x32_bf16 v[10:13], v[176:179], v[80:83], v[10:13]\n"
    "v_add_u32 v5, 64, v5\n"
    "v_mfma_f32_16x16x32_bf16 v[14:17], v[176:179], v[84:87], v[14:17]\n"
    "ds_read_b128 v[50:53], v5\n"
    "v_mfma_f32_16x16x32_bf16 v[18:21], v[176:179], v[88:91], v[18:21]\n"
    "s_mov_b64 exec, s[94:95]\n"
    "ds_read_b128 v[58:61], v75 offset:0\n"
    "ds_read_b128 v[62:65], v75 offset:32\n"
    "ds_read_b128 v[66:69], v75 offset:64\n"
    "ds_read_b128 v[70:73], v75 offset:96\n"
    "s_mov_b64 exec, -1\n"
    "v_mfma_f32_16x16x32_bf16 v[22:25], v[176:179], v[92:95], v[22:25]\n"
    "buffer_load_dwordx4 v[176:179], v2, s[72:75], s87 offen offset:0 sc1\n"
    "s_waitcnt vmcnt(35) lgkmcnt(0)\n"
    "v_mfma_f32_16x16x32_bf16 v[26:29], v[180:183], v[96:99], v[10:13]\n"
    "v_add_u32 v3, 36864, v4\n"
    "v_mfma_f32_16x16x32_bf16 v[30:33], v[180:183], v[100:103], v[14:17]\n"
    "v_mfma_f32_16x16x32_bf16 v[34:37], v[180:183], v[104:107], v[18:21]\n"
    "v_mfma_f32_16x16x32_bf16 v[38:41], v[180:183], v[108:111], v[22:25]\n"
    "buffer_load_dwordx4 v[180:183], v2, s[72:75], s87 offen offset:1024 sc1\n"
    "s_add_u32 s87, s87, 2048\n"
    "s_mov_b32 s88, 4\n"
    "10:\n"
    "s_waitcnt vmcnt(35) lgkmcnt(0)\n"
    "v_mfma_f32_16x16x32_bf16 v[10:13], v[184:187], a[0:3], v[50:53]\n"
    "v_mfma_f32_16x16x32_bf16 v[14:17], v[184:187], a[4:7], v[50:53]\n"
    "v_mfma_f32_16x16x32_bf16 v[18:21], v[184:187], a[8:11], v[50:53]\n"
    "v_mfma_f32_16x16x32_bf16 v[22:25], v[184:187], a[12:15], v[50:53]\n"
    "buffer_load_dwordx4 v[184:187], v2, s[72:75], s87 offen offset:0 sc1\n"
    "s_waitcnt vmcnt(35)\n"
    "v_mfma_f32_16x16x32_bf16 v[10:13], v[188:191], a[16:19], v[10:13]\n"
    "v_mfma_f32_16x16x32_bf16 v[14:17], v[188:191], a[20:23], v[14:17]\n"
    "v_mfma_f32_16x16x32_bf16 v[18:21], v[188:191], a[24:27], v[18:21]\n"
    "v_add_u32 v5, 64, v5\n"
    "v_mfma_f32_16x16x32_bf16 v[22:25], v[188:191], a[28:31], v[22:25]\n"
    "buffer_load_dwordx4 v[188:191], v2, s[72:75], s87 offen offset:1024 sc1\n"
    "s_waitcnt vmcnt(35)\n"
    "v_mfma_f32_16x16x32_bf16 v[10:13], v[192:195], a[32:35], v[10:13]\n"
    "ds_read_b128 v[50:53], v5\n"
    "v_max_f32 v26, 0, v26\n"
    "v_mfma_f32_16x16x32_bf16 v[14:17], v[192:195], a[36:39], v[14:17]\n"
    "v_max_f32 v27, 0, v27\n"
    "v_mfma_f32_16x16x32_bf16 v[18:21], v[192:195], a[40:43], v[18:21]\n"
    "v_max_f32 v28, 0, v28\n"
    "v_mfma_f32_16x16x32_bf16 v[22:25], v[192:195], a[44:47], v[22:25]\n"
    "buffer_load_dwordx4 v[192:195], v2, s[72:75], s87 offen offset:2048 sc1\n"
    "s_waitcnt vmcnt(35)\n"
    "v_mfma_f32_16x16x32_bf16 v[10:13], v[196:199], a[48:51], v[10:13]\n"
    "v_max_f32 v29, 0, v29\n"
    "v_mul_f32 v26, v26, v26\n"
    "v_mfma_f32_16x16x32_bf16 v[14:17], v[196:199], a[52:55], v[14:17]\n"
    "v_mul_f32 v27, v27, v27\n"
    "v_mfma_f32_16x16x32_bf16 v[18:21], v[196:199], a[56:59], v[18:21]\n"
    "v_mul_f32 v28, v28, v28\n"
    "v_mfma_f32_16x16x32_bf16 v[22:25], v[196:199], a[60:63], v[22:25]\n"
    "buffer_load_dwordx4 v[196:199], v2, s[72:75], s87 offen offset:3072 sc1\n"
    "s_add_u32 s87, s87, 4096\n"
    "s_waitcnt vmcnt(35)\n"
    "v_mfma_f32_16x16x32_bf16 v[10:13], v[200:203], a[64:67], v[10:13]\n"
    "v_mul_f32 v29, v29, v29\n"
    "v_cvt_pk_bf16_f32 v26, v26, v27\n"
    "v_mfma_f32_16x16x32_bf16 v[14:17], v[200:203], a[68:71], v[14:17]\n"
    "v_cvt_pk_bf16_f32 v27, v28, v29\n"
    "v_mfma_f32_16x16x32_bf16 v[18:21], v[200:203], a[72:75], v[18:21]\n"
    "ds_write_b64 v8, v[26:27] offset:0\n"
    "v_mfma_f32_16x16x32_bf16 v[22:25], v[200:203], a[76:79], v[22:25]\n"
    "buffer_load_dwordx4 v[200:203], v2, s[72:75], s87 offen offset:0 sc1\n"
    "s_waitcnt vmcnt(35)\n"
    "v_mfma_f32_16x16x32_bf16 v[10:13], v[204:207], a[80:83], v[10:13]\n"
    "v_max_f32 v30, 0, v30\n"
    "v_max_f32 v31, 0, v31\n"
    "v_mfma_f32_16x16x32_bf16 v[14:17], v[204:207], a[84:87], v[14:17]\n"
    "v_max_f32 v32, 0, v32\n"
    "v_mfma_f32_16x16x32_bf16 v[18:21], v[204:207], a[88:91], v[18:21]\n"
    "v_max_f32 v33, 0, v33\n"
    "v_mfma_f32_16x16x32_bf16 v[22:25], v[204:207], a[92:95], v[22:25]\n"
    "buffer_load_dwordx4 v[204:207], v2, s[72:75], s87 offen offset:1024 sc1\n"
    "s_waitcnt vmcnt(35)\n"
    "v_mfma_f32_16x16x32_bf16 v[10:13], v[208:211], a[96:99], v[10:13]\n"
    "v_mul_f32 v30, v30, v30\n"
    "v_mul_f32 v31, v31, v31\n"
    "v_mfma_f32_16x16x32_bf16 v[14:17], v[208:211], a[100:103], v[14:17]\n"
    "v_mul_f32 v32, v32, v32\n"
    "v_mfma_f32_16x16x32_bf16 v[18:21], v[208:211], a[104:107], v[18:21]\n"
    "v_mul_f32 v33, v33, v33\n"
    "v_mfma_f32_16x16x32_bf16 v[22:25], v[208:211], a[108:111], v[22:25]\n"
    "buffer_load_dwordx4 v[208:211], v2, s[72:75], s87 offen offset:2048 sc1\n"
    "s_waitcnt vmcnt(35)\n"
    "v_mfma_f32_16x16x32_bf16 v[10:13], v[212:215], a[112:115], v[10:13]\n"
    "v_cvt_pk_bf16_f32 v30, v30, v31\n"
    "v_cvt_pk_bf16_f32 v31, v32, v33\n"
    "v_mfma_f32_16x16x32_bf16 v[14:17], v[212:215], a[116:119], v[14:17]\n"
    "ds_write_b64 v8, v[30:31] offset:1024\n"
    "v_mfma_f32_16x16x32_bf16 v[18:21], v[212:215], a[120:123], v[18:21]\n"
    "v_max_f32 v34, 0, v34\n"
    "v_mfma_f32_16x16x32_bf16 v[22:25], v[212:215], a[124:127], v[22:25]\n"
    "buffer_load_dwordx4 v[212:215], v2, s[72:75], s87 offen offset:3072 sc1\n"
    "s_add_u32 s87, s87, 4096\n"
    "s_waitcnt vmcnt(35)\n"
    "v_mfma_f32_16x16x32_bf16 v[10:13], v[216:219], a[128:131], v[10:13]\n"
    "v_max_f32 v35, 0, v35\n"
    "v_max_f32 v36, 0, v36\n"
    "v_mfma_f32_16x16x32_bf16 v[14:17], v[216:219], a[132:135], v[14:17]\n"
    "v_max_f32 v37, 0, v37\n"
    "v_mfma_f32_16x16x32_bf16 v[18:21], v[216:219], a[136:139], v[18:21]\n"
    "v_mul_f32 v34, v34, v34\n"
    "v_mfma_f32_16x16x32_bf16 v[22:25], v[216:219], a[140:143], v[22:25]\n"
    "buffer_load_dwordx4 v[216:219], v2, s[72:75], s87 offen offset:0 sc1\n"
    "s_waitcnt vmcnt(35)\n"
    "v_mfma_f32_16x16x32_bf16 v[10:13], v[220:223], a[144:147], v[10:13]\n"
    "v_mul_f32 v35, v35, v35\n"
    "v_mul_f32 v36, v36, v36\n"
    "v_mfma_f32_16x16x32_bf16 v[14:17], v[220:223], a[148:151], v[14:17]\n"
    "v_mul_f32 v37, v37, v37\n"
    "v_mfma_f32_16x16x32_bf16 v[18:21], v[220:223], a[152:155], v[18:21]\n"
    "v_cvt_pk_bf16_f32 v34, v34, v35\n"
    "v_mfma_f32_16x16x32_bf16 v[22:25], v[220:223], a[156:159], v[22:25]\n"
    "buffer_load_dwordx4 v[220:223], v2, s[72:75], s87 offen offset:1024 sc1\n"
    "s_waitcnt vmcnt(35)\n"
    "v_mfma_f32_16x16x32_bf16 v[10:13], v[224:227], a[160:163], v[10:13]\n"
    "v_cvt_pk_bf16_f32 v35, v36, v37\n"
    "ds_write_b64 v8, v[34:35] offset:2048\n"
    "v_mfma_f32_16x16x32_bf16 v[14:17], v[224:227], a[164:167], v[14:17]\n"
    "v_max_f32 v38, 0, v38\n"
    "v_mfma_f32_16x16x32_bf16 v[18:21], v[224:227], a[168:171], v[18:21]\n"
    "v_max_f32 v39, 0, v39\n"
    "v_mfma_f32_16x16x32_bf16 v[22:25], v[224:227], a[172:175], v[22:25]\n"
    "buffer_load_dwordx4 v[224:227], v2, s[72:75], s87 offen offset:2048 sc1\n"
    "s_waitcnt vmcnt(35)\n"
    "v_mfma_f32_16x16x32_bf16 v[10:13], v[228:231], a[176:179], v[10:13]\n"
    "v_max_f32 v40, 0, v40\n"
    "v_max_f32 v41, 0, v41\n"
    "v_mfma_f32_16x16x32_bf16 v[14:17], v[228:231], a[180:183], v[14:17]\n"
    "v_mul_f32 v38, v38, v38\n"
    "v_mfma_f32_16x16x32_bf16 v[18:21], v[228:231], a[184:187], v[18:21]\n"
    "v_mul_f32 v39, v39, v39\n"
    "v_mfma_f32_16x16x32_bf16 v[22:25], v[228:231], a[188:191], v[22:25]\n"
    "buffer_load_dwordx4 v[228:231], v2, s[72:75], s87 offen offset:3072 sc1\n"
    "s_add_u32 s87, s87, 4096\n"
    "s_waitcnt vmcnt(35)\n"
    "v_mfma_f32_16x16x32_bf16 v[10:13], v[232:235], a[192:195], v[10:13]\n"
    "v_mul_f32 v40, v40, v40\n"
    "v_mul_f32 v41, v41, v41\n"
    "v_mfma_f32_16x16x32_bf16 v[14:17], v[232:235], a[196:199], v[14:17]\n"
    "v_cvt_pk_bf16_f32 v38, v38, v39\n"
    "v_mfma_f32_16x16x32_bf16 v[18:21], v[232:235], a[200:203], v[18:21]\n"
    "v_cvt_pk_bf16_f32 v39, v40, v41\n"
    "v_mfma_f32_16x16x32_bf16 v[22:25], v[232:235], a[204:207], v[22:25]\n"
    "buffer_load_dwordx4 v[232:235], v2, s[72:75], s87 offen offset:0 sc1\n"
    "s_waitcnt vmcnt(35)\n"
    "v_mfma_f32_16x16x32_bf16 v[10:13], v[236:239], a[208:211], v[10:13]\n"
    "ds_write_b64 v8, v[38:39] offset:3072\n"
    "v_add_u32 v8, 8192, v8\n"
    "v_mfma_f32_16x16x32_bf16 v[14:17], v[236:239], a[212:215], v[14:17]\n"
    "v_mfma_f32_16x16x32_bf16 v[18:21], v[236:239], a[216:219], v[18:21]\n"
    "v_mfma_f32_16x16x32_bf16 v[22:25], v[236:239], a[220:223], v[22:25]\n"
    "buffer_load_dwordx4 v[236:239], v2, s[72:75], s87 offen offset:1024 sc1\n"
    "s_waitcnt vmcnt(35)\n"
    "v_mfma_f32_16x16x32_bf16 v[10:13], v[240:243], a[224:227], v[10:13]\n"
    "v_mfma_f32_16x16x32_bf16 v[14:17], v[240:243], a[228:231], v[14:17]\n"
    "v_mfma_f32_16x16x32_bf16 v[18:21], v[240:243], a[232:235], v[18:21]\n"
    "v_mfma_f32_16x16x32_bf16 v[22:25], v[240:243], a[236:239], v[22:25]\n"
    "buffer_load_dwordx4 v[240:243], v2, s[72:75], s87 offen offset:2048 sc1\n"
    "s_waitcnt vmcnt(35)\n"
    "v_mfma_f32_16x16x32_bf16 v[10:13], v[244:247], a[240:243], v[10:13]\n"
    "v_mfma_f32_16x16x32_bf16 v[14:17], v[244:247], a[244:247], v[14:17]\n"
    "v_mfma_f32_16x16x32_bf16 v[18:21], v[244:247], a[248:251], v[18:21]\n"
    "v_mfma_f32_16x16x32_bf16 v[22:25], v[244:247], a[252:255], v[22:25]\n"
    "buffer_load_dwordx4 v[244:247], v2, s[72:75], s87 offen offset:3072 sc1\n"
    "s_add_u32 s87, s87, 4096\n"
    "s_waitcnt vmcnt(35)\n"
    "v_mfma_f32_16x16x32_bf16 v[10:13], v[248:251], v[80:83], v[10:13]\n"
    "v_mfma_f32_16x16x32_bf16 v[14:17], v[248:251], v[84:87], v[14:17]\n"
    "v_mfma_f32_16x16x32_bf16 v[18:21], v[248:251], v[88:91], v[18:21]\n"
    "v_mfma_f32_16x16x32_bf16 v[22:25], v[248:251], v[92:95], v[22:25]\n"
    "buffer_load_dwordx4 v[248:251], v2, s[72:75], s87 offen offset:0 sc1\n"
    "s_waitcnt vmcnt(35)\n"
    "v_mfma_f32_16x16x32_bf16 v[26:29], v[252:255], v[96:99], v[10:13]\n"
    "v_mfma_f32_16x16x32_bf16 v[30:33], v[252:255], v[100:103], v[14:17]\n"
    "v_mfma_f32_16x16x32_bf16 v[34:37], v[252:255], v[104:107], v[18:21]\n"
    "v_mfma_f32_16x16x32_bf16 v[38:41], v[252:255], v[108:111], v[22:25]\n"
    "buffer_load_dwordx4 v[252:255], v2, s[72:75], s87 offen offset:1024 sc1\n"
    "s_add_u32 s87, s87, 2048\n"
    "s_waitcnt vmcnt(35) lgkmcnt(0)\n"
    "v_mfma_f32_16x16x32_bf16 v[10:13], v[112:115], a[0:3], v[50:53]\n"
    "v_mfma_f32_16x16x32_bf16 v[14:17], v[112:115], a[4:7], v[50:53]\n"
    "v_mfma_f32_16x16x32_bf16 v[18:21], v[112:115], a[8:11], v[50:53]\n"
    "v_mfma_f32_16x16x32_bf16 v[22:25], v[112:115], a[12:15], v[50:53]\n"
    "buffer_load_dwordx4 v[112:115], v2, s[72:75], s87 offen offset:0 sc1\n"
    "s_waitcnt vmcnt(35)\n"
    "v_mfma_f32_16x16x32_bf16 v[10:13], v[116:119], a[16:19], v[10:13]\n"
    "v_mfma_f32_16x16x32_bf16 v[14:17], v[116:119], a[20:23], v[14:17]\n"
    "v_mfma_f32_16x16x32_bf16 v[18:21], v[116:119], a[24:27], v[18:21]\n"
    "v_add_u32 v5, 64, v5\n"
    "v_mfma_f32_16x16x32_bf16 v[22:25], v[116:119], a[28:31], v[22:25]\n"
    "buffer_load_dwordx4 v[116:119], v2, s[72:75], s87 offen offset:1024 sc1\n"
    "s_waitcnt vmcnt(35)\n"
    "v_mfma_f32_16x16x32_bf16 v[10:13], v[120:123], a[32:35], v[10:13]\n"
    "ds_read_b128 v[50:53], v5\n"
    "v_max_f32 v26, 0, v26\n"
    "v_mfma_f32_16x16x32_bf16 v[14:17], v[120:123], a[36:39], v[14:17]\n"
    "v_max_f32 v27, 0, v27\n"
    "v_mfma_f32_16x16x32_bf16 v[18:21], v[120:123], a[40:43], v[18:21]\n"
    "v_max_f32 v28, 0, v28\n"
    "v_mfma_f32_16x16x32_bf16 v[22:25], v[120:123], a[44:47], v[22:25]\n"
    "buffer_load_dwordx4 v[120:123], v2, s[72:75], s87 offen offset:2048 sc1\n"
    "s_waitcnt vmcnt(35)\n"
    "v_mfma_f32_16x16x32_bf16 v[10:13], v[124:127], a[48:51], v[10:13]\n"
    "v_max_f32 v29, 0, v29\n"
    "v_mul_f32 v26, v26, v26\n"
    "v_mfma_f32_16x16x32_bf16 v[14:17], v[124:127], a[52:55], v[14:17]\n"
    "v_mul_f32 v27, v27, v27\n"
    "v_mfma_f32_16x16x32_bf16 v[18:21], v[124:127], a[56:59], v[18:21]\n"
    "v_mul_f32 v28, v28, v28\n"
    "v_mfma_f32_16x16x32_bf16 v[22:25], v[124:127], a[60:63], v[22:25]\n"
    "buffer_load_dwordx4 v[124:127], v2, s[72:75], s87 offen offset:3072 sc1\n"
    "s_add_u32 s87, s87, 4096\n"
    "s_waitcnt vmcnt(35)\n"
    "v_mfma_f32_16x16x32_bf16 v[10:13], v[128:131], a[64:67], v[10:13]\n"
    "v_mul_f32 v29, v29, v29\n"
    "v_cvt_pk_bf16_f32 v26, v26, v27\n"
    "v_mfma_f32_16x16x32_bf16 v[14:17], v[128:131], a[68:71], v[14:17]\n"
    "v_cvt_pk_bf16_f32 v27, v28, v29\n"
    "v_mfma_f32_16x16x32_bf16 v[18:21], v[128:131], a[72:75], v[18:21]\n"
    "ds_write_b64 v8, v[26:27] offset:0\n"
    "v_mfma_f32_16x16x32_bf16 v[22:25], v[128:131], a[76:79], v[22:25]\n"
    "buffer_load_dwordx4 v[128:131], v2, s[72:75], s87 offen offset:0 sc1\n"
    "s_waitcnt vmcnt(35)\n"
    "v_mfma_f32_16x16x32_bf16 v[10:13], v[132:135], a[80:83], v[10:13]\n"
    "v_max_f32 v30, 0, v30\n"
    "v_max_f32 v31, 0, v31\n"
    "v_mfma_f32_16x16x32_bf16 v[14:17], v[132:135], a[84:87], v[14:17]\n"
    "v_max_f32 v32, 0, v32\n"
    "v_mfma_f32_16x16x32_bf16 v[18:21], v[132:135], a[88:91], v[18:21]\n"
    "v_max_f32 v33, 0, v33\n"
    "v_mfma_f32_16x16x32_bf16 v[22:25], v[132:135], a[92:95], v[22:25]\n"
    "buffer_load_dwordx4 v[132:135], v2, s[72:75], s87 offen offset:1024 sc1\n"
    "s_waitcnt vmcnt(35)\n"
    "v_mfma_f32_16x16x32_bf16 v[10:13], v[136:139], a[96:99], v[10:13]\n"
    "v_mul_f32 v30, v30, v30\n"
    "v_mul_f32 v31, v31, v31\n"
    "v_mfma_f32_16x16x32_bf16 v[14:17], v[136:139], a[100:103], v[14:17]\n"
    "v_mul_f32 v32, v32, v32\n"
    "v_mfma_f32_16x16x32_bf16 v[18:21], v[136:139], a[104:107], v[18:21]\n"
    "v_mul_f32 v33, v33, v33\n"
    "v_mfma_f32_16x16x32_bf16 v[22:25], v[136:139], a[108:111], v[22:25]\n"
    "buffer_load_dwordx4 v[136:139], v2, s[72:75], s87 offen offset:2048 sc1\n"
    "s_waitcnt vmcnt(35)\n"
    "v_mfma_f32_16x16x32_bf16 v[10:13], v[140:143], a[112:115], v[10:13]\n"
    "v_cvt_pk_bf16_f32 v30, v30, v31\n"
    "v_cvt_pk_bf16_f32 v31, v32, v33\n"
    "v_mfma_f32_16x16x32_bf16 v[14:17], v[140:143], a[116:119], v[14:17]\n"
    "ds_write_b64 v8, v[30:31] offset:1024\n"
    "v_mfma_f32_16x16x32_bf16 v[18:21], v[140:143], a[120:123], v[18:21]\n"
    "v_max_f32 v34, 0, v34\n"
    "v_mfma_f32_16x16x32_bf16 v[22:25], v[140:143], a[124:127], v[22:25]\n"
    "buffer_load_dwordx4 v[140:143], v2, s[72:75], s87 offen offset:3072 sc1\n"
    "s_add_u32 s87, s87, 4096\n"
    "s_waitcnt vmcnt(35)\n"
    "v_mfma_f32_16x16x32_bf16 v[10:13], v[144:147], a[128:131], v[10:13]\n"
    "v_max_f32 v35, 0, v35\n"
    "v_max_f32 v36, 0, v36\n"
    "v_mfma_f32_16x16x32_bf16 v[14:17], v[144:147], a[132:135], v[14:17]\n"
    "v_max_f32 v37, 0, v37\n"
    "v_mfma_f32_16x16x32_bf16 v[18:21], v[144:147], a[136:139], v[18:21]\n"
    "v_mul_f32 v34, v34, v34\n"
    "v_mfma_f32_16x16x32_bf16 v[22:25], v[144:147], a[140:143], v[22:25]\n"
    "buffer_load_dwordx4 v[144:147], v2, s[72:75], s87 offen offset:0 sc1\n"
    "s_waitcnt vmcnt(35)\n"
    "v_mfma_f32_16x16x32_bf16 v[10:13], v[148:151], a[144:147], v[10:13]\n"
    "v_mul_f32 v35, v35, v35\n"
    "v_mul_f32 v36, v36, v36\n"
    "v_mfma_f32_16x16x32_bf16 v[14:17], v[148:151], a[148:151], v[14:17]\n"
    "v_mul_f32 v37, v37, v37\n"
    "v_mfma_f32_16x16x32_bf16 v[18:21], v[148:151], a[152:155], v[18:21]\n"
    "v_cvt_pk_bf16_f32 v34, v34, v35\n"
    "v_mfma_f32_16x16x32_bf16 v[22:25], v[148:151], a[156:159], v[22:25]\n"
    "buffer_load_dwordx4 v[148:151], v2, s[72:75], s87 offen offset:1024 sc1\n"
    "s_waitcnt vmcnt(35)\n"
    "v_mfma_f32_16x16x32_bf16 v[10:13], v[152:155], a[160:163], v[10:13]\n"
    "v_cvt_pk_bf16_f32 v35, v36, v37\n"
    "ds_write_b64 v8, v[34:35] offset:2048\n"
    "v_mfma_f32_16x16x32_bf16 v[14:17], v[152:155], a[164:167], v[14:17]\n"
    "v_max_f32 v38, 0, v38\n"
    "v_mfma_f32_16x16x32_bf16 v[18:21], v[152:155], a[168:171], v[18:21]\n"
    "v_max_f32 v39, 0, v39\n"
    "v_mfma_f32_16x16x32_bf16 v[22:25], v[152:155], a[172:175], v[22:25]\n"
    "buffer_load_dwordx4 v[152:155], v2, s[72:75], s87 offen offset:2048 sc1\n"
    "s_waitcnt vmcnt(35)\n"
    "v_mfma_f32_16x16x32_bf16 v[10:13], v[156:159], a[176:179], v[10:13]\n"
    "v_max_f32 v40, 0, v40\n"
    "v_max_f32 v41, 0, v41\n"
    "v_mfma_f32_16x16x32_bf16 v[14:17], v[156:159], a[180:183], v[14:17]\n"
    "v_mul_f32 v38, v38, v38\n"
    "v_mfma_f32_16x16x32_bf16 v[18:21], v[156:159], a[184:187], v[18:21]\n"
    "v_mul_f32 v39, v39, v39\n"
    "v_mfma_f32_16x16x32_bf16 v[22:25], v[156:159], a[188:191], v[22:25]\n"
    "buffer_load_dwordx4 v[156:159], v2, s[72:75], s87 offen offset:3072 sc1\n"
    "s_add_u32 s87, s87, 4096\n"
    "s_waitcnt vmcnt(35)\n"
    "v_mfma_f32_16x16x32_bf16 v[10:13], v[160:163], a[192:195], v[10:13]\n"
    "v_mul_f32 v40, v40, v40\n"
    "v_mul_f32 v41, v41, v41\n"
    "v_mfma_f32_16x16x32_bf16 v[14:17], v[160:163], a[196:199], v[14:17]\n"
    "v_cvt_pk_bf16_f32 v38, v38, v39\n"
    "v_mfma_f32_16x16x32_bf16 v[18:21], v[160:163], a[200:203], v[18:21]\n"
    "v_cvt_pk_bf16_f32 v39, v40, v41\n"
    "v_mfma_f32_16x16x32_bf16 v[22:25], v[160:163], a[204:207], v[22:25]\n"
    "buffer_load_dwordx4 v[160:163], v2, s[72:75], s87 offen offset:0 sc1\n"
    "s_waitcnt vmcnt(35)\n"
    "v_mfma_f32_16x16x32_bf16 v[10:13], v[164:167], a[208:211], v[10:13]\n"
    "ds_write_b64 v8, v[38:39] offset:3072\n"
    "v_add_u32 v8, 8192, v8\n"
    "v_mfma_f32_16x16x32_bf16 v[14:17], v[164:167], a[212:215], v[14:17]\n"
    "v_mfma_f32_16x16x32_bf16 v[18:21], v[164:167], a[216:219], v[18:21]\n"
    "v_mfma_f32_16x16x32_bf16 v[22:25], v[164:167], a[220:223], v[22:25]\n"
    "buffer_load_dwordx4 v[164:167], v2, s[72:75], s87 offen offset:1024 sc1\n"
    "s_waitcnt vmcnt(35)\n"
    "v_mfma_f32_16x16x32_bf16 v[10:13], v[168:171], a[224:227], v[10:13]\n"
    "v_mfma_f32_16x16x32_bf16 v[14:17], v[168:171], a[228:231], v[14:17]\n"
    "v_mfma_f32_16x16x32_bf16 v[18:21], v[168:171], a[232:235], v[18:21]\n"
    "v_mfma_f32_16x16x32_bf16 v[22:25], v[168:171], a[236:239], v[22:25]\n"
    "buffer_load_dwordx4 v[168:171], v2, s[72:75], s87 offen offset:2048 sc1\n"
    "s_waitcnt vmcnt(35)\n"
    "v_mfma_f32_16x16x32_bf16 v[10:13], v[172:175], a[240:243], v[10:13]\n"
    "v_mfma_f32_16x16x32_bf16 v[14:17], v[172:175], a[244:247], v[14:17]\n"
    "v_mfma_f32_16x16x32_bf16 v[18:21], v[172:175], a[248:251], v[18:21]\n"
    "v_mfma_f32_16x16x32_bf16 v[22:25], v[172:175], a[252:255], v[22:25]\n"
    "buffer_load_dwordx4 v[172:175], v2, s[72:75], s87 offen offset:3072 sc1\n"
    "s_add_u32 s87, s87, 4096\n"
    "s_waitcnt vmcnt(35)\n"
    "v_mfma_f32_16x16x32_bf16 v[10:13], v[176:179], v[80:83], v[10:13]\n"
    "v_mfma_f32_16x16x32_bf16 v[14:17], v[176:179], v[84:87], v[14:17]\n"
    "v_mfma_f32_16x16x32_bf16 v[18:21], v[176:179], v[88:91], v[18:21]\n"
    "v_mfma_f32_16x16x32_bf16 v[22:25], v[176:179], v[92:95], v[22:25]\n"
    "buffer_load_dwordx4 v[176:179], v2, s[72:75], s87 offen offset:0 sc1\n"
    "s_waitcnt vmcnt(35)\n"
    "v_mfma_f32_16x16x32_bf16 v[26:29], v[180:183], v[96:99], v[10:13]\n"
    "v_mfma_f32_16x16x32_bf16 v[30:33], v[180:183], v[100:103], v[14:17]\n"
    "v_mfma_f32_16x16x32_bf16 v[34:37], v[180:183], v[104:107], v[18:21]\n"
    "v_mfma_f32_16x16x32_bf16 v[38:41], v[180:183], v[108:111], v[22:25]\n"
    "buffer_load_dwordx4 v[180:183], v2, s[72:75], s87 offen offset:1024 sc1\n"
    "s_add_u32 s87, s87, 2048\n"
    "s_sub_u32 s88, s88, 1\n"
    "s_cmp_lg_u32 s88, 0\n"
    "s_cbranch_scc1 10b\n"
    "s_nop 2\n"
    "v_max_f32 v26, 0, v26\n"
    "v_max_f32 v27, 0, v27\n"
    "v_max_f32 v28, 0, v28\n"
    "v_max_f32 v29, 0, v29\n"
    "v_mul_f32 v26, v26, v26\n"
    "v_mul_f32 v27, v27, v27\n"
    "v_mul_f32 v28, v28, v28\n"
    "v_mul_f32 v29, v29, v29\n"
    "v_cvt_pk_bf16_f32 v26, v26, v27\n"
    "v_cvt_pk_bf16_f32 v27, v28, v29\n"
    "ds_write_b64 v8, v[26:27] offset:0\n"
    "v_max_f32 v30, 0, v30\n"
    "v_max_f32 v31, 0, v31\n"
    "v_max_f32 v32, 0, v32\n"
    "v_max_f32 v33, 0, v33\n"
    "v_mul_f32 v30, v30, v30\n"
    "v_mul_f32 v31, v31, v31\n"
    "v_mul_f32 v32, v32, v32\n"
    "v_mul_f32 v33, v33, v33\n"
    "v_cvt_pk_bf16_f32 v30, v30, v31\n"
    "v_cvt_pk_bf16_f32 v31, v32, v33\n"
    "ds_write_b64 v8, v[30:31] offset:1024\n"
    "v_max_f32 v34, 0, v34\n"
    "v_max_f32 v35, 0, v35\n"
    "v_max_f32 v36, 0, v36\n"
    "v_max_f32 v37, 0, v37\n"
    "v_mul_f32 v34, v34, v34\n"
    "v_mul_f32 v35, v35, v35\n"
    "v_mul_f32 v36, v36, v36\n"
    "v_mul_f32 v37, v37, v37\n"
    "v_cvt_pk_bf16_f32 v34, v34, v35\n"
    "v_cvt_pk_bf16_f32 v35, v36, v37\n"
    "ds_write_b64 v8, v[34:35] offset:2048\n"
    "v_max_f32 v38, 0, v38\n"
    "v_max_f32 v39, 0, v39\n"
    "v_max_f32 v40, 0, v40\n"
    "v_max_f32 v41, 0, v41\n"
    "v_mul_f32 v38, v38, v38\n"
    "v_mul_f32 v39, v39, v39\n"
    "v_mul_f32 v40, v40, v40\n"
    "v_mul_f32 v41, v41, v41\n"
    "v_cvt_pk_bf16_f32 v38, v38, v39\n"
    "v_cvt_pk_bf16_f32 v39, v40, v41\n"
    "ds_write_b64 v8, v[38:39] offset:3072\n"
    "s_waitcnt lgkmcnt(0)\n"
    "s_barrier\n"
    "ds_read_b128 a[0:3], v4 offset:0\n"
    "ds_read_b128 a[4:7], v4 offset:1024\n"
    "ds_read_b128 a[8:11], v4 offset:2048\n"
    "ds_read_b128 a[12:15], v4 offset:3072\n"
    "ds_read_b128 a[16:19], v4 offset:4096\n"
    "ds_read_b128 a[20:23], v4 offset:5120\n"
    "ds_read_b128 a[24:27], v4 offset:6144\n"
    "ds_read_b128 a[28:31], v4 offset:7168\n"
    "ds_read_b128 a[32:35], v4 offset:8192\n"
    "ds_read_b128 a[36:39], v4 offset:9216\n"
    "ds_read_b128 a[40:43], v4 offset:10240\n"
    "ds_read_b128 a[44:47], v4 offset:11264\n"
    "v_mfma_f32_16x16x32_bf16 v[10:13], v[76:79], v[58:61], v[50:53]\n"
    "v_mfma_f32_16x16x32_bf16 v[14:17], v[76:79], v[62:65], v[50:53]\n"
    "v_mfma_f32_16x16x32_bf16 v[18:21], v[76:79], v[66:69], v[50:53]\n"
    "v_mfma_f32_16x16x32_bf16 v[22:25], v[76:79], v[70:73], v[50:53]\n"
    "s_waitcnt vmcnt(35) lgkmcnt(8)\n"
    "v_mfma_f32_16x16x32_bf16 v[10:13], v[184:187], a[0:3], v[10:13]\n"
    "ds_read_b128 a[48:51], v4 offset:12288\n"
    "v_mfma_f32_16x16x32_bf16 v[14:17], v[184:187], a[4:7], v[14:17]\n"
    "ds_read_b128 a[52:55], v4 offset:13312\n"
    "v_mfma_f32_16x16x32_bf16 v[18:21], v[184:187], a[8:11], v[18:21]\n"
    "ds_read_b128 a[56:59], v4 offset:14336\n"
    "v_mfma_f32_16x16x32_bf16 v[22:25], v[184:187], a[12:15], v[22:25]\n"
    "ds_read_b128 a[60:63], v4 offset:15360\n"
    "buffer_load_dwordx4 v[184:187], v2, s[72:75], s87 offen offset:0 sc1\n"
    "s_waitcnt vmcnt(35) lgkmcnt(8)\n"
    "v_mfma_f32_16x16x32_bf16 v[10:13], v[188:191], a[16:19], v[10:13]\n"
    "ds_read_b128 a[64:67], v4 offset:16384\n"
    "v_mfma_f32_16x16x32_bf16 v[14:17], v[188:191], a[20:23], v[14:17]\n"
    "ds_read_b128 a[68:71], v4 offset:17408\n"
    "v_mfma_f32_16x16x32_bf16 v[18:21], v[188:191], a[24:27], v[18:21]\n"
    "ds_read_b128 a[72:75], v4 offset:18432\n"
    "v_mfma_f32_16x16x32_bf16 v[22:25], v[188:191], a[28:31], v[22:25]\n"
    "ds_read_b128 a[76:79], v4 offset:19456\n"
    "buffer_load_dwordx4 v[188:191], v2, s[72:75], s87 offen offset:1024 sc1\n"
    "s_waitcnt vmcnt(35) lgkmcnt(8)\n"
    "v_mfma_f32_16x16x32_bf16 v[10:13], v[192:195], a[32:35], v[10:13]\n"
    "ds_read_b128 a[80:83], v4 offset:20480\n"
    "v_mfma_f32_16x16x32_bf16 v[14:17], v[192:195], a[36:39], v[14:17]\n"
    "ds_read_b128 a[84:87], v4 offset:21504\n"
    "v_mfma_f32_16x16x32_bf16 v[18:21], v[192:195], a[40:43], v[18:21]\n"
    "ds_read_b128 a[88:91], v4 offset:22528\n"
    "v_mfma_f32_16x16x32_bf16 v[22:25], v[192:195], a[44:47], v[22:25]\n"
    "ds_read_b128 a[92:95], v4 offset:23552\n"
    "buffer_load_dwordx4 v[192:195], v2, s[72:75], s87 offen offset:2048 sc1\n"
    "s_waitcnt vmcnt(35) lgkmcnt(8)\n"
    "v_mfma_f32_16x16x32_bf16 v[10:13], v[196:199], a[48:51], v[10:13]\n"
    "ds_read_b128 a[96:99], v4 offset:24576\n"
    "v_mfma_f32_16x16x32_bf16 v[14:17], v[196:199], a[52:55], v[14:17]\n"
    "ds_read_b128 a[100:103], v4 offset:25600\n"
    "v_mfma_f32_16x16x32_bf16 v[18:21], v[196:199], a[56:59], v[18:21]\n"
    "ds_read_b128 a[104:107], v4 offset:26624\n"
    "v_mfma_f32_16x16x32_bf16 v[22:25], v[196:199], a[60:63], v[22:25]\n"
    "ds_read_b128 a[108:111], v4 offset:27648\n"
    "buffer_load_dwordx4 v[196:199], v2, s[72:75], s87 offen offset:3072 sc1\n"
    "s_add_u32 s87, s87, 4096\n"
    "s_waitcnt vmcnt(35) lgkmcnt(8)\n"
    "v_mfma_f32_16x16x32_bf16 v[10:13], v[200:203], a[64:67], v[10:13]\n"
    "ds_read_b128 a[112:115], v4 offset:28672\n"
    "v_mfma_f32_16x16x32_bf16 v[14:17], v[200:203], a[68:71], v[14:17]\n"
    "ds_read_b128 a[116:119], v4 offset:29696\n"
    "v_mfma_f32_16x16x32_bf16 v[18:21], v[200:203], a[72:75], v[18:21]\n"
    "ds_read_b128 a[120:123], v4 offset:30720\n"
    "v_mfma_f32_16x16x32_bf16 v[22:25], v[200:203], a[76:79], v[22:25]\n"
    "ds_read_b128 a[124:127], v4 offset:31744\n"
    "buffer_load_dwordx4 v[200:203], v2, s[72:75], s87 offen offset:0 sc1\n"
    "s_waitcnt vmcnt(35) lgkmcnt(8)\n"
    "v_mfma_f32_16x16x32_bf16 v[10:13], v[204:207], a[80:83], v[10:13]\n"
    "ds_read_b128 a[128:131], v4 offset:32768\n"
    "v_mfma_f32_16x16x32_bf16 v[14:17], v[204:207], a[84:87], v[14:17]\n"
    "ds_read_b128 a[132:135], v4 offset:33792\n"
    "v_mfma_f32_16x16x32_bf16 v[18:21], v[204:207], a[88:91], v[18:21]\n"
    "ds_read_b128 a[136:139], v4 offset:34816\n"
    "v_mfma_f32_16x16x32_bf16 v[22:25], v[204:207], a[92:95], v[22:25]\n"
    "ds_read_b128 a[140:143], v4 offset:35840\n"
    "buffer_load_dwordx4 v[204:207], v2, s[72:75], s87 offen offset:1024 sc1\n"
    "s_waitcnt vmcnt(35) lgkmcnt(8)\n"
    "v_mfma_f32_16x16x32_bf16 v[10:13], v[208:211], a[96:99], v[10:13]\n"
    "ds_read_b128 a[144:147], v3 offset:0\n"
    "v_mfma_f32_16x16x32_bf16 v[14:17], v[208:211], a[100:103], v[14:17]\n"
    "ds_read_b128 a[148:151], v3 offset:1024\n"
    "v_mfma_f32_16x16x32_bf16 v[18:21], v[208:211], a[104:107], v[18:21]\n"
    "ds_read_b128 a[152:155], v3 offset:2048\n"
    "v_mfma_f32_16x16x32_bf16 v[22:25], v[208:211], a[108:111], v[22:25]\n"
    "ds_read_b128 a[156:159], v3 offset:3072\n"
    "buffer_load_dwordx4 v[208:211], v2, s[72:75], s87 offen offset:2048 sc1\n"
    "s_waitcnt vmcnt(35) lgkmcnt(8)\n"
    "v_mfma_f32_16x16x32_bf16 v[10:13], v[212:215], a[112:115], v[10:13]\n"
    "ds_read_b128 a[160:163], v3 offset:4096\n"
    "v_mfma_f32_16x16x32_bf16 v[14:17], v[212:215], a[116:119], v[14:17]\n"
    "ds_read_b128 a[164:167], v3 offset:5120\n"
    "v_mfma_f32_16x16x32_bf16 v[18:21], v[212:215], a[120:123], v[18:21]\n"
    "ds_read_b128 a[168:171], v3 offset:6144\n"
    "v_mfma_f32_16x16x32_bf16 v[22:25], v[212:215], a[124:127], v[22:25]\n"
    "ds_read_b128 a[172:175], v3 offset:7168\n"
    "buffer_load_dwordx4 v[212:215], v2, s[72:75], s87 offen offset:3072 sc1\n"
    "s_add_u32 s87, s87, 4096\n"
    "s_waitcnt vmcnt(35) lgkmcnt(8)\n"
    "v_mfma_f32_16x16x32_bf16 v[10:13], v[216:219], a[128:131], v[10:13]\n"
    "ds_read_b128 a[176:179], v3 offset:8192\n"
    "v_mfma_f32_16x16x32_bf16 v[14:17], v[216:219], a[132:135], v[14:17]\n"
    "ds_read_b128 a[180:183], v3 offset:9216\n"
    "v_mfma_f32_16x16x32_bf16 v[18:21], v[216:219], a[136:139], v[18:21]\n"
    "ds_read_b128 a[184:187], v3 offset:10240\n"
    "v_mfma_f32_16x16x32_bf16 v[22:25], v[216:219], a[140:143], v[22:25]\n"
    "ds_read_b128 a[188:191], v3 offset:11264\n"
    "buffer_load_dwordx4 v[216:219], v2, s[72:75], s87 offen offset:0 sc1\n"
    "s_waitcnt vmcnt(35) lgkmcnt(8)\n"
    "v_mfma_f32_16x16x32_bf16 v[10:13], v[220:223], a[144:147], v[10:13]\n"
    "ds_read_b128 a[192:195], v3 offset:12288\n"
    "v_mfma_f32_16x16x32_bf16 v[14:17], v[220:223], a[148:151], v[14:17]\n"
    "ds_read_b128 a[196:199], v3 offset:13312\n"
    "v_mfma_f32_16x16x32_bf16 v[18:21], v[220:223], a[152:155], v[18:21]\n"
    "ds_read_b128 a[200:203], v3 offset:14336\n"
    "v_mfma_f32_16x16x32_bf16 v[22:25], v[220:223], a[156:159], v[22:25]\n"
    "ds_read_b128 a[204:207], v3 offset:15360\n"
    "buffer_load_dwordx4 v[220:223], v2, s[72:75], s87 offen offset:1024 sc1\n"
    "s_waitcnt vmcnt(35) lgkmcnt(8)\n"
    "v_mfma_f32_16x16x32_bf16 v[10:13], v[224:227], a[160:163], v[10:13]\n"
    "ds_read_b128 a[208:211], v3 offset:16384\n"
    "v_mfma_f32_16x16x32_bf16 v[14:17], v[224:227], a[164:167], v[14:17]\n"
    "ds_read_b128 a[212:215], v3 offset:17408\n"
    "v_mfma_f32_16x16x32_bf16 v[18:21], v[224:227], a[168:171], v[18:21]\n"
    "ds_read_b128 a[216:219], v3 offset:18432\n"
    "v_mfma_f32_16x16x32_bf16 v[22:25], v[224:227], a[172:175], v[22:25]\n"
    "ds_read_b128 a[220:223], v3 offset:19456\n"
    "buffer_load_dwordx4 v[224:227], v2, s[72:75], s87 offen offset:2048 sc1\n"
    "s_waitcnt vmcnt(35) lgkmcnt(8)\n"
    "v_mfma_f32_16x16x32_bf16 v[10:13], v[228:231], a[176:179], v[10:13]\n"
    "ds_read_b128 a[224:227], v3 offset:20480\n"
    "v_mfma_f32_16x16x32_bf16 v[14:17], v[228:231], a[180:183], v[14:17]\n"
    "ds_read_b128 a[228:231], v3 offset:21504\n"
    "v_mfma_f32_16x16x32_bf16 v[18:21], v[228:231], a[184:187], v[18:21]\n"
    "ds_read_b128 a[232:235], v3 offset:22528\n"
    "v_mfma_f32_16x16x32_bf16 v[22:25], v[228:231], a[188:191], v[22:25]\n"
    "ds_read_b128 a[236:239], v3 offset:23552\n"
    "buffer_load_dwordx4 v[228:231], v2, s[72:75], s87 offen offset:3072 sc1\n"
    "s_add_u32 s87, s87, 4096\n"
    "s_waitcnt vmcnt(35) lgkmcnt(8)\n"
    "v_mfma_f32_16x16x32_bf16 v[10:13], v[232:235], a[192:195], v[10:13]\n"
    "ds_read_b128 a[240:243], v3 offset:24576\n"
    "v_mfma_f32_16x16x32_bf16 v[14:17], v[232:235], a[196:199], v[14:17]\n"
    "ds_read_b128 a[244:247], v3 offset:25600\n"
    "v_mfma_f32_16x16x32_bf16 v[18:21], v[232:235], a[200:203], v[18:21]\n"
    "ds_read_b128 a[248:251], v3 offset:26624\n"
    "v_mfma_f32_16x16x32_bf16 v[22:25], v[232:235], a[204:207], v[22:25]\n"
    "ds_read_b128 a[252:255], v3 offset:27648\n"
    "buffer_load_dwordx4 v[232:235], v2, s[72:75], s87 offen offset:0 sc1\n"
    "s_waitcnt vmcnt(35) lgkmcnt(8)\n"
    "v_mfma_f32_16x16x32_bf16 v[10:13], v[236:239], a[208:211], v[10:13]\n"
    "ds_read_b128 v[80:83], v3 offset:28672\n"
    "v_mfma_f32_16x16x32_bf16 v[14:17], v[236:239], a[212:215], v[14:17]\n"
    "ds_read_b128 v[84:87], v3 offset:29696\n"
    "v_mfma_f32_16x16x32_bf16 v[18:21], v[236:239], a[216:219], v[18:21]\n"
    "ds_read_b128 v[88:91], v3 offset:30720\n"
    "v_mfma_f32_16x16x32_bf16 v[22:25], v[236:239], a[220:223], v[22:25]\n"
    "ds_read_b128 v[92:95], v3 offset:31744\n"
    "buffer_load_dwordx4 v[236:239], v2, s[72:75], s87 offen offset:1024 sc1\n"
    "s_waitcnt vmcnt(35) lgkmcnt(8)\n"
    "v_mfma_f32_16x16x32_bf16 v[10:13], v[240:243], a[224:227], v[10:13]\n"
    "ds_read_b128 v[96:99], v3 offset:32768\n"
    "v_mfma_f32_16x16x32_bf16 v[14:17], v[240:243], a[228:231], v[14:17]\n"
    "ds_read_b128 v[100:103], v3 offset:33792\n"
    "v_mfma_f32_16x16x32_bf16 v[18:21], v[240:243], a[232:235], v[18:21]\n"
    "ds_read_b128 v[104:107], v3 offset:34816\n"
    "v_mfma_f32_16x16x32_bf16 v[22:25], v[240:243], a[236:239], v[22:25]\n"
    "ds_read_b128 v[108:111], v3 offset:35840\n"
    "buffer_load_dwordx4 v[240:243], v2, s[72:75], s87 offen offset:2048 sc1\n"
    "s_waitcnt vmcnt(35) lgkmcnt(8)\n"
    "v_mfma_f32_16x16x32_bf16 v[10:13], v[244:247], a[240:243], v[10:13]\n"
    "s_add_u32 s89, s89, 1\n"
    "s_add_u32 s90, s68, s89\n"
    "v_mfma_f32_16x16x32_bf16 v[14:17], v[244:247], a[244:247], v[14:17]\n"
    "s_and_b32 s90, s90, 7\n"
    "s_lshl_b32 s90, s90, 10\n"
    "v_mfma_f32_16x16x32_bf16 v[18:21], v[244:247], a[248:251], v[18:21]\n"
    "v_add_u32 v75, s90, v9\n"
    "s_mov_b64 exec, s[94:95]\n"
    "ds_read_b128 v[58:61], v75 offset:0\n"
    "ds_read_b128 v[62:65], v75 offset:32\n"
    "ds_read_b128 v[66:69], v75 offset:64\n"
    "ds_read_b128 v[70:73], v75 offset:96\n"
    "s_mov_b64 exec, -1\n"
    "v_mfma_f32_16x16x32_bf16 v[22:25], v[244:247], a[252:255], v[22:25]\n"
    "buffer_load_dwordx4 v[244:247], v2, s[72:75], s87 offen offset:3072 sc1\n"
    "s_add_u32 s87, s87, 4096\n"
    "s_waitcnt vmcnt(35) lgkmcnt(4)\n"
    "v_mfma_f32_16x16x32_bf16 v[10:13], v[248:251], v[80:83], v[10:13]\n"
    "v_mfma_f32_16x16x32_bf16 v[14:17], v[248:251], v[84:87], v[14:17]\n"
    "v_mfma_f32_16x16x32_bf16 v[18:21], v[248:251], v[88:91], v[18:21]\n"
    "v_mfma_f32_16x16x32_bf16 v[22:25], v[248:251], v[92:95], v[22:25]\n"
    "buffer_load_dwordx4 v[248:251], v2, s[72:75], s87 offen offset:0 sc1\n"
    "s_waitcnt vmcnt(35) lgkmcnt(0)\n"
    "v_mfma_f32_16x16x32_bf16 v[26:29], v[252:255], v[96:99], v[10:13]\n"
    "v_mfma_f32_16x16x32_bf16 v[30:33], v[252:255], v[100:103], v[14:17]\n"
    "v_mfma_f32_16x16x32_bf16 v[34:37], v[252:255], v[104:107], v[18:21]\n"
    "v_mfma_f32_16x16x32_bf16 v[38:41], v[252:255], v[108:111], v[22:25]\n"
    "buffer_load_dwordx4 v[252:255], v2, s[72:75], s87 offen offset:1024 sc1\n"
    "s_add_u32 s87, s87, 2048\n"
    "s_mov_b32 s88, 3\n"
    "s_mul_i32 s84, s67, 14336\n"
    "s_add_u32 m0, s84, s66\n"
    "11:\n"
    "s_waitcnt lgkmcnt(0)\n"
    "v_mfma_f32_16x16x32_bf16 v[10:13], v[76:79], v[58:61], 0\n"
    "v_mfma_f32_16x16x32_bf16 v[14:17], v[76:79], v[62:65], 0\n"
    "v_mfma_f32_16x16x32_bf16 v[18:21], v[76:79], v[66:69], 0\n"
    "v_mfma_f32_16x16x32_bf16 v[22:25], v[76:79], v[70:73], 0\n"
    "s_waitcnt vmcnt(35)\n"
    "v_mfma_f32_16x16x32_bf16 v[10:13], v[112:115], a[0:3], v[10:13]\n"
    "v_mfma_f32_16x16x32_bf16 v[14:17], v[112:115], a[4:7], v[14:17]\n"
    "v_mfma_f32_16x16x32_bf16 v[18:21], v[112:115], a[8:11], v[18:21]\n"
    "v_mfma_f32_16x16x32_bf16 v[22:25], v[112:115], a[12:15], v[22:25]\n"
    "buffer_load_dwordx4 v[112:115], v2, s[72:75], s87 offen offset:0 sc1\n"
    "s_waitcnt vmcnt(35)\n"
    "v_mfma_f32_16x16x32_bf16 v[10:13], v[116:119], a[16:19], v[10:13]\n"
    "v_mfma_f32_16x16x32_bf16 v[14:17], v[116:119], a[20:23], v[14:17]\n"
    "v_mfma_f32_16x16x32_bf16 v[18:21], v[116:119], a[24:27], v[18:21]\n"
    "v_cvt_pk_bf16_f32 v26, v26, v27\n"
    "v_mfma_f32_16x16x32_bf16 v[22:25], v[116:119], a[28:31], v[22:25]\n"
    "buffer_load_dwordx4 v[116:119], v2, s[72:75], s87 offen offset:1024 sc1\n"
    "s_waitcnt vmcnt(35)\n"
    "v_mfma_f32_16x16x32_bf16 v[10:13], v[120:123], a[32:35], v[10:13]\n"
    "v_cvt_pk_bf16_f32 v27, v28, v29\n"
    "v_cvt_pk_bf16_f32 v28, v30, v31\n"
    "v_mfma_f32_16x16x32_bf16 v[14:17], v[120:123], a[36:39], v[14:17]\n"
    "v_cvt_pk_bf16_f32 v29, v32, v33\n"
    "v_mfma_f32_16x16x32_bf16 v[18:21], v[120:123], a[40:43], v[18:21]\n"
    "v_min_u32 v26, -2, v26\n"
    "v_mfma_f32_16x16x32_bf16 v[22:25], v[120:123], a[44:47], v[22:25]\n"
    "buffer_load_dwordx4 v[120:123], v2, s[72:75], s87 offen offset:2048 sc1\n"
    "s_waitcnt vmcnt(35)\n"
    "v_mfma_f32_16x16x32_bf16 v[10:13], v[124:127], a[48:51], v[10:13]\n"
    "v_min_u32 v27, -2, v27\n"
    "v_min_u32 v28, -2, v28\n"
    "v_mfma_f32_16x16x32_bf16 v[14:17], v[124:127], a[52:55], v[14:17]\n"
    "v_min_u32 v29, -2, v29\n"
    "v_mfma_f32_16x16x32_bf16 v[18:21], v[124:127], a[56:59], v[18:21]\n"
    "global_store_dwordx4 v6, v[26:29], s[36:37] sc1\n"
    "v_mfma_f32_16x16x32_bf16 v[22:25], v[124:127], a[60:63], v[22:25]\n"
    "buffer_load_dwordx4 v[124:127], v2, s[72:75], s87 offen offset:3072 sc1\n"
    "s_add_u32 s87, s87, 4096\n"
    "s_waitcnt vmcnt(36)\n"
    "v_mfma_f32_16x16x32_bf16 v[10:13], v[128:131], a[64:67], v[10:13]\n"
    "v_cvt_pk_bf16_f32 v34, v34, v35\n"
    "v_cvt_pk_bf16_f32 v35, v36, v37\n"
    "v_mfma_f32_16x16x32_bf16 v[14:17], v[128:131], a[68:71], v[14:17]\n"
    "v_cvt_pk_bf16_f32 v36, v38, v39\n"
    "v_mfma_f32_16x16x32_bf16 v[18:21], v[128:131], a[72:75], v[18:21]\n"
    "v_cvt_pk_bf16_f32 v37, v40, v41\n"
    "v_mfma_f32_16x16x32_bf16 v[22:25], v[128:131], a[76:79], v[22:25]\n"
    "buffer_load_dwordx4 v[128:131], v2, s[72:75], s87 offen offset:0 sc1\n"
    "s_waitcnt vmcnt(36)\n"
    "v_mfma_f32_16x16x32_bf16 v[10:13], v[132:135], a[80:83], v[10:13]\n"
    "v_min_u32 v34, -2, v34\n"
    "v_min_u32 v35, -2, v35\n"
    "v_mfma_f32_16x16x32_bf16 v[14:17], v[132:135], a[84:87], v[14:17]\n"
    "v_min_u32 v36, -2, v36\n"
    "v_mfma_f32_16x16x32_bf16 v[18:21], v[132:135], a[88:91], v[18:21]\n"
    "v_min_u32 v37, -2, v37\n"
    "v_mfma_f32_16x16x32_bf16 v[22:25], v[132:135], a[92:95], v[22:25]\n"
    "buffer_load_dwordx4 v[132:135], v2, s[72:75], s87 offen offset:1024 sc1\n"
    "s_waitcnt vmcnt(36)\n"
    "v_mfma_f32_16x16x32_bf16 v[10:13], v[136:139], a[96:99], v[10:13]\n"
    "global_store_dwordx4 v6, v[34:37], s[38:39] sc1\n"
    "s_add_u32 s90, s68, s89\n"
    "v_mfma_f32_16x16x32_bf16 v[14:17], v[136:139], a[100:103], v[14:17]\n"
    "s_and_b32 s84, s90, 7\n"
    "v_mfma_f32_16x16x32_bf16 v[18:21], v[136:139], a[104:107], v[18:21]\n"
    "s_lshl_b32 s84, s84, 16\n"
    "v_mfma_f32_16x16x32_bf16 v[22:25], v[136:139], a[108:111], v[22:25]\n"
    "buffer_load_dwordx4 v[136:139], v2, s[72:75], s87 offen offset:2048 sc1\n"
    "s_waitcnt vmcnt(37)\n"
    "v_mfma_f32_16x16x32_bf16 v[10:13], v[140:143], a[112:115], v[10:13]\n"
    "s_add_u32 s36, s50, s84\n"
    "s_addc_u32 s37, s51, 0\n"
    "v_mfma_f32_16x16x32_bf16 v[14:17], v[140:143], a[116:119], v[14:17]\n"
    "s_add_u32 s38, s36, 1024\n"
    "s_addc_u32 s39, s37, 0\n"
    "v_mfma_f32_16x16x32_bf16 v[18:21], v[140:143], a[120:123], v[18:21]\n"
    "s_add_u32 s89, s89, 1\n"
    "v_mfma_f32_16x16x32_bf16 v[22:25], v[140:143], a[124:127], v[22:25]\n"
    "buffer_load_dwordx4 v[140:143], v2, s[72:75], s87 offen offset:3072 sc1\n"
    "s_add_u32 s87, s87, 4096\n"
    "s_waitcnt vmcnt(37)\n"
    "v_mfma_f32_16x16x32_bf16 v[10:13], v[144:147], a[128:131], v[10:13]\n"
    "s_add_u32 s90, s68, s89\n"
    "s_and_b32 s90, s90, 7\n"
    "v_mfma_f32_16x16x32_bf16 v[14:17], v[144:147], a[132:135], v[14:17]\n"
    "s_lshl_b32 s90, s90, 10\n"
    "v_mfma_f32_16x16x32_bf16 v[18:21], v[144:147], a[136:139], v[18:21]\n"
    "v_add_u32 v75, s90, v9\n"
    "v_mfma_f32_16x16x32_bf16 v[22:25], v[144:147], a[140:143], v[22:25]\n"
    "buffer_load_dwordx4 v[144:147], v2, s[72:75], s87 offen offset:0 sc1\n"
    "s_waitcnt vmcnt(37)\n"
    "v_mfma_f32_16x16x32_bf16 v[10:13], v[148:151], a[144:147], v[10:13]\n"
    "s_mov_b64 exec, s[94:95]\n"
    "ds_read_b128 v[58:61], v75 offset:0\n"
    "ds_read_b128 v[62:65], v75 offset:32\n"
    "ds_read_b128 v[66:69], v75 offset:64\n"
    "ds_read_b128 v[70:73], v75 offset:96\n"
    "s_mov_b64 exec, -1\n"
    "v_mfma_f32_16x16x32_bf16 v[14:17], v[148:151], a[148:151], v[14:17]\n"
    "s_sub_u32 s91, s68, s89\n"
    "v_mfma_f32_16x16x32_bf16 v[18:21], v[148:151], a[152:155], v[18:21]\n"
    "s_add_u32 s91, s91, 3\n"
    "v_mfma_f32_16x16x32_bf16 v[22:25], v[148:151], a[156:159], v[22:25]\n"
    "buffer_load_dwordx4 v[148:151], v2, s[72:75], s87 offen offset:1024 sc1\n"
    "s_waitcnt vmcnt(37)\n"
    "v_mfma_f32_16x16x32_bf16 v[10:13], v[152:155], a[160:163], v[10:13]\n"
    "s_and_b32 s91, s91, 7\n"
    "s_lshl_b32 s91, s91, 11\n"
    "v_mfma_f32_16x16x32_bf16 v[14:17], v[152:155], a[164:167], v[14:17]\n"
    "s_add_u32 s40, s52, s91\n"
    "s_addc_u32 s41, s53, 0\n"
    "v_mfma_f32_16x16x32_bf16 v[18:21], v[152:155], a[168:171], v[18:21]\n"
    "s_add_u32 m0, m0, 2048\n"
    "v_mfma_f32_16x16x32_bf16 v[22:25], v[152:155], a[172:175], v[22:25]\n"
    "buffer_load_dwordx4 v[152:155], v2, s[72:75], s87 offen offset:2048 sc1\n"
    "s_waitcnt vmcnt(37)\n"
    "v_mfma_f32_16x16x32_bf16 v[10:13], v[156:159], a[176:179], v[10:13]\n"
    "v_mfma_f32_16x16x32_bf16 v[14:17], v[156:159], a[180:183], v[14:17]\n"
    "v_mfma_f32_16x16x32_bf16 v[18:21], v[156:159], a[184:187], v[18:21]\n"
    "v_mfma_f32_16x16x32_bf16 v[22:25], v[156:159], a[188:191], v[22:25]\n"
    "buffer_load_dwordx4 v[156:159], v2, s[72:75], s87 offen offset:3072 sc1\n"
    "s_add_u32 s87, s87, 4096\n"
    "s_waitcnt vmcnt(37)\n"
    "v_mfma_f32_16x16x32_bf16 v[10:13], v[160:163], a[192:195], v[10:13]\n"
    "v_mfma_f32_16x16x32_bf16 v[14:17], v[160:163], a[196:199], v[14:17]\n"
    "v_mfma_f32_16x16x32_bf16 v[18:21], v[160:163], a[200:203], v[18:21]\n"
    "v_mfma_f32_16x16x32_bf16 v[22:25], v[160:163], a[204:207], v[22:25]\n"
    "buffer_load_dwordx4 v[160:163], v2, s[72:75], s87 offen offset:0 sc1\n"
    "s_waitcnt vmcnt(37)\n"
    "v_mfma_f32_16x16x32_bf16 v[10:13], v[164:167], a[208:211], v[10:13]\n"
    "v_mfma_f32_16x16x32_bf16 v[14:17], v[164:167], a[212:215], v[14:17]\n"
    "v_mfma_f32_16x16x32_bf16 v[18:21], v[164:167], a[216:219], v[18:21]\n"
    "v_mfma_f32_16x16x32_bf16 v[22:25], v[164:167], a[220:223], v[22:25]\n"
    "buffer_load_dwordx4 v[164:167], v2, s[72:75], s87 offen offset:1024 sc1\n"
    "s_waitcnt vmcnt(37)\n"
    "v_mfma_f32_16x16x32_bf16 v[10:13], v[168:171], a[224:227], v[10:13]\n"
    "v_mfma_f32_16x16x32_bf16 v[14:17], v[168:171], a[228:231], v[14:17]\n"
    "v_mfma_f32_16x16x32_bf16 v[18:21], v[168:171], a[232:235], v[18:21]\n"
    "v_mfma_f32_16x16x32_bf16 v[22:25], v[168:171], a[236:239], v[22:25]\n"
    "buffer_load_dwordx4 v[168:171], v2, s[72:75], s87 offen offset:2048 sc1\n"
    "s_waitcnt vmcnt(37)\n"
    "v_mfma_f32_16x16x32_bf16 v[10:13], v[172:175], a[240:243], v[10:13]\n"
    "v_mfma_f32_16x16x32_bf16 v[14:17], v[172:175], a[244:247], v[14:17]\n"
    "v_mfma_f32_16x16x32_bf16 v[18:21], v[172:175], a[248:251], v[18:21]\n"
    "v_mfma_f32_16x16x32_bf16 v[22:25], v[172:175], a[252:255], v[22:25]\n"
    "buffer_load_dwordx4 v[172:175], v2, s[72:75], s87 offen offset:3072 sc1\n"
    "s_add_u32 s87, s87, 4096\n"
    "s_waitcnt vmcnt(37)\n"
    "v_mfma_f32_16x16x32_bf16 v[10:13], v[176:179], v[80:83], v[10:13]\n"
    "s_cmp_lt_u32 s89, 4\n"
    "s_cbranch_scc1 130f\n"
    "global_load_lds_dwordx4 v6, s[40:41] sc1\n"
    "global_load_lds_dwordx4 v6, s[40:41] offset:1024 sc1\n"
    "130:\n"
    "v_mfma_f32_16x16x32_bf16 v[14:17], v[176:179], v[84:87], v[14:17]\n"
    "v_mfma_f32_16x16x32_bf16 v[18:21], v[176:179], v[88:91], v[18:21]\n"
    "v_mfma_f32_16x16x32_bf16 v[22:25], v[176:179], v[92:95], v[22:25]\n"
    "buffer_load_dwordx4 v[176:179], v2, s[72:75], s87 offen offset:0 sc1\n"
    "s_waitcnt vmcnt(37)\n"
    "v_mfma_f32_16x16x32_bf16 v[26:29], v[180:183], v[96:99], v[10:13]\n"
    "v_mfma_f32_16x16x32_bf16 v[30:33], v[180:183], v[100:103], v[14:17]\n"
    "v_mfma_f32_16x16x32_bf16 v[34:37], v[180:183], v[104:107], v[18:21]\n"
    "v_mfma_f32_16x16x32_bf16 v[38:41], v[180:183], v[108:111], v[22:25]\n"
    "buffer_load_dwordx4 v[180:183], v2, s[72:75], s87 offen offset:1024 sc1\n"
    "s_add_u32 s87, s87, 2048\n"
    "s_waitcnt lgkmcnt(0)\n"
    "v_mfma_f32_16x16x32_bf16 v[10:13], v[76:79], v[58:61], 0\n"
    "v_mfma_f32_16x16x32_bf16 v[14:17], v[76:79], v[62:65], 0\n"
    "v_mfma_f32_16x16x32_bf16 v[18:21], v[76:79], v[66:69], 0\n"
    "v_mfma_f32_16x16x32_bf16 v[22:25], v[76:79], v[70:73], 0\n"
    "s_waitcnt vmcnt(35)\n"
    "v_mfma_f32_16x16x32_bf16 v[10:13], v[184:187], a[0:3], v[10:13]\n"
    "v_mfma_f32_16x16x32_bf16 v[14:17], v[184:187], a[4:7], v[14:17]\n"
    "v_mfma_f32_16x16x32_bf16 v[18:21], v[184:187], a[8:11], v[18:21]\n"
    "v_mfma_f32_16x16x32_bf16 v[22:25], v[184:187], a[12:15], v[22:25]\n"
    "buffer_load_dwordx4 v[184:187], v2, s[72:75], s87 offen offset:0 sc1\n"
    "s_waitcnt vmcnt(35)\n"
    "v_mfma_f32_16x16x32_bf16 v[10:13], v[188:191], a[16:19], v[10:13]\n"
    "v_mfma_f32_16x16x32_bf16 v[14:17], v[188:191], a[20:23], v[14:17]\n"
    "v_mfma_f32_16x16x32_bf16 v[18:21], v[188:191], a[24:27], v[18:21]\n"
    "v_cvt_pk_bf16_f32 v26, v26, v27\n"
    "v_mfma_f32_16x16x32_bf16 v[22:25], v[188:191], a[28:31], v[22:25]\n"
    "buffer_load_dwordx4 v[188:191], v2, s[72:75], s87 offen offset:1024 sc1\n"
    "s_waitcnt vmcnt(35)\n"
    "v_mfma_f32_16x16x32_bf16 v[10:13], v[192:195], a[32:35], v[10:13]\n"
    "v_cvt_pk_bf16_f32 v27, v28, v29\n"
    "v_cvt_pk_bf16_f32 v28, v30, v31\n"
    "v_mfma_f32_16x16x32_bf16 v[14:17], v[192:195], a[36:39], v[14:17]\n"
    "v_cvt_pk_bf16_f32 v29, v32, v33\n"
    "v_mfma_f32_16x16x32_bf16 v[18:21], v[192:195], a[40:43], v[18:21]\n"
    "v_min_u32 v26, -2, v26\n"
    "v_mfma_f32_16x16x32_bf16 v[22:25], v[192:195], a[44:47], v[22:25]\n"
    "buffer_load_dwordx4 v[192:195], v2, s[72:75], s87 offen offset:2048 sc1\n"
    "s_waitcnt vmcnt(35)\n"
    "v_mfma_f32_16x16x32_bf16 v[10:13], v[196:199], a[48:51], v[10:13]\n"
    "v_min_u32 v27, -2, v27\n"
    "v_min_u32 v28, -2, v28\n"
    "v_mfma_f32_16x16x32_bf16 v[14:17], v[196:199], a[52:55], v[14:17]\n"
    "v_min_u32 v29, -2, v29\n"
    "v_mfma_f32_16x16x32_bf16 v[18:21], v[196:199], a[56:59], v[18:21]\n"
    "global_store_dwordx4 v6, v[26:29], s[36:37] sc1\n"
    "v_mfma_f32_16x16x32_bf16 v[22:25], v[196:199], a[60:63], v[22:25]\n"
    "buffer_load_dwordx4 v[196:199], v2, s[72:75], s87 offen offset:3072 sc1\n"
    "s_add_u32 s87, s87, 4096\n"
    "s_waitcnt vmcnt(36)\n"
    "v_mfma_f32_16x16x32_bf16 v[10:13], v[200:203], a[64:67], v[10:13]\n"
    "v_cvt_pk_bf16_f32 v34, v34, v35\n"
    "v_cvt_pk_bf16_f32 v35, v36, v37\n"
    "v_mfma_f32_16x16x32_bf16 v[14:17], v[200:203], a[68:71], v[14:17]\n"
    "v_cvt_pk_bf16_f32 v36, v38, v39\n"
    "v_mfma_f32_16x16x32_bf16 v[18:21], v[200:203], a[72:75], v[18:21]\n"
    "v_cvt_pk_bf16_f32 v37, v40, v41\n"
    "v_mfma_f32_16x16x32_bf16 v[22:25], v[200:203], a[76:79], v[22:25]\n"
    "buffer_load_dwordx4 v[200:203], v2, s[72:75], s87 offen offset:0 sc1\n"
    "s_waitcnt vmcnt(36)\n"
    "v_mfma_f32_16x16x32_bf16 v[10:13], v[204:207], a[80:83], v[10:13]\n"
    "v_min_u32 v34, -2, v34\n"
    "v_min_u32 v35, -2, v35\n"
    "v_mfma_f32_16x16x32_bf16 v[14:17], v[204:207], a[84:87], v[14:17]\n"
    "v_min_u32 v36, -2, v36\n"
    "v_mfma_f32_16x16x32_bf16 v[18:21], v[204:207], a[88:91], v[18:21]\n"
    "v_min_u32 v37, -2, v37\n"
    "v_mfma_f32_16x16x32_bf16 v[22:25], v[204:207], a[92:95], v[22:25]\n"
    "buffer_load_dwordx4 v[204:207], v2, s[72:75], s87 offen offset:1024 sc1\n"
    "s_waitcnt vmcnt(36)\n"
    "v_mfma_f32_16x16x32_bf16 v[10:13], v[208:211], a[96:99], v[10:13]\n"
    "global_store_dwordx4 v6, v[34:37], s[38:39] sc1\n"
    "s_add_u32 s90, s68, s89\n"
    "v_mfma_f32_16x16x32_bf16 v[14:17], v[208:211], a[100:103], v[14:17]\n"
    "s_and_b32 s84, s90, 7\n"
    "v_mfma_f32_16x16x32_bf16 v[18:21], v[208:211], a[104:107], v[18:21]\n"
    "s_lshl_b32 s84, s84, 16\n"
    "v_mfma_f32_16x16x32_bf16 v[22:25], v[208:211], a[108:111], v[22:25]\n"
    "buffer_load_dwordx4 v[208:211], v2, s[72:75], s87 offen offset:2048 sc1\n"
    "s_waitcnt vmcnt(37)\n"
    "v_mfma_f32_16x16x32_bf16 v[10:13], v[212:215], a[112:115], v[10:13]\n"
    "s_add_u32 s36, s50, s84\n"
    "s_addc_u32 s37, s51, 0\n"
    "v_mfma_f32_16x16x32_bf16 v[14:17], v[212:215], a[116:119], v[14:17]\n"
    "s_add_u32 s38, s36, 1024\n"
    "s_addc_u32 s39, s37, 0\n"
    "v_mfma_f32_16x16x32_bf16 v[18:21], v[212:215], a[120:123], v[18:21]\n"
    "s_add_u32 s89, s89, 1\n"
    "v_mfma_f32_16x16x32_bf16 v[22:25], v[212:215], a[124:127], v[22:25]\n"
    "buffer_load_dwordx4 v[212:215], v2, s[72:75], s87 offen offset:3072 sc1\n"
    "s_add_u32 s87, s87, 4096\n"
    "s_waitcnt vmcnt(37)\n"
    "v_mfma_f32_16x16x32_bf16 v[10:13], v[216:219], a[128:131], v[10:13]\n"
    "s_add_u32 s90, s68, s89\n"
    "s_and_b32 s90, s90, 7\n"
    "v_mfma_f32_16x16x32_bf16 v[14:17], v[216:219], a[132:135], v[14:17]\n"
    "s_lshl_b32 s90, s90, 10\n"
    "v_mfma_f32_16x16x32_bf16 v[18:21], v[216:219], a[136:139], v[18:21]\n"
    "v_add_u32 v75, s90, v9\n"
    "v_mfma_f32_16x16x32_bf16 v[22:25], v[216:219], a[140:143], v[22:25]\n"
    "buffer_load_dwordx4 v[216:219], v2, s[72:75], s87 offen offset:0 sc1\n"
    "s_waitcnt vmcnt(37)\n"
    "v_mfma_f32_16x16x32_bf16 v[10:13], v[220:223], a[144:147], v[10:13]\n"
    "s_mov_b64 exec, s[94:95]\n"
    "ds_read_b128 v[58:61], v75 offset:0\n"
    "ds_read_b128 v[62:65], v75 offset:32\n"
    "ds_read_b128 v[66:69], v75 offset:64\n"
    "ds_read_b128 v[70:73], v75 offset:96\n"
    "s_mov_b64 exec, -1\n"
    "v_mfma_f32_16x16x32_bf16 v[14:17], v[220:223], a[148:151], v[14:17]\n"
    "s_sub_u32 s91, s68, s89\n"
    "v_mfma_f32_16x16x32_bf16 v[18:21], v[220:223], a[152:155], v[18:21]\n"
    "s_add_u32 s91, s91, 3\n"
    "v_mfma_f32_16x16x32_bf16 v[22:25], v[220:223], a[156:159], v[22:25]\n"
    "buffer_load_dwordx4 v[220:223], v2, s[72:75], s87 offen offset:1024 sc1\n"
    "s_waitcnt vmcnt(37)\n"
    "v_mfma_f32_16x16x32_bf16 v[10:13], v[224:227], a[160:163], v[10:13]\n"
    "s_and_b32 s91, s91, 7\n"
    "s_lshl_b32 s91, s91, 11\n"
    "v_mfma_f32_16x16x32_bf16 v[14:17], v[224:227], a[164:167], v[14:17]\n"
    "s_add_u32 s40, s52, s91\n"
    "s_addc_u32 s41, s53, 0\n"
    "v_mfma_f32_16x16x32_bf16 v[18:21], v[224:227], a[168:171], v[18:21]\n"
    "s_add_u32 m0, m0, 2048\n"
    "v_mfma_f32_16x16x32_bf16 v[22:25], v[224:227], a[172:175], v[22:25]\n"
    "buffer_load_dwordx4 v[224:227], v2, s[72:75], s87 offen offset:2048 sc1\n"
    "s_waitcnt vmcnt(37)\n"
    "v_mfma_f32_16x16x32_bf16 v[10:13], v[228:231], a[176:179], v[10:13]\n"
    "v_mfma_f32_16x16x32_bf16 v[14:17], v[228:231], a[180:183], v[14:17]\n"
    "v_mfma_f32_16x16x32_bf16 v[18:21], v[228:231], a[184:187], v[18:21]\n"
    "v_mfma_f32_16x16x32_bf16 v[22:25], v[228:231], a[188:191], v[22:25]\n"
    "buffer_load_dwordx4 v[228:231], v2, s[72:75], s87 offen offset:3072 sc1\n"
    "s_add_u32 s87, s87, 4096\n"
    "s_waitcnt vmcnt(37)\n"
    "v_mfma_f32_16x16x32_bf16 v[10:13], v[232:235], a[192:195], v[10:13]\n"
    "v_mfma_f32_16x16x32_bf16 v[14:17], v[232:235], a[196:199], v[14:17]\n"
    "v_mfma_f32_16x16x32_bf16 v[18:21], v[232:235], a[200:203], v[18:21]\n"
    "v_mfma_f32_16x16x32_bf16 v[22:25], v[232:235], a[204:207], v[22:25]\n"
    "buffer_load_dwordx4 v[232:235], v2, s[72:75], s87 offen offset:0 sc1\n"
    "s_waitcnt vmcnt(37)\n"
    "v_mfma_f32_16x16x32_bf16 v[10:13], v[236:239], a[208:211], v[10:13]\n"
    "v_mfma_f32_16x16x32_bf16 v[14:17], v[236:239], a[212:215], v[14:17]\n"
    "v_mfma_f32_16x16x32_bf16 v[18:21], v[236:239], a[216:219], v[18:21]\n"
    "v_mfma_f32_16x16x32_bf16 v[22:25], v[236:239], a[220:223], v[22:25]\n"
    "buffer_load_dwordx4 v[236:239], v2, s[72:75], s87 offen offset:1024 sc1\n"
    "s_waitcnt vmcnt(37)\n"
    "v_mfma_f32_16x16x32_bf16 v[10:13], v[240:243], a[224:227], v[10:13]\n"
    "v_mfma_f32_16x16x32_bf16 v[14:17], v[240:243], a[228:231], v[14:17]\n"
    "v_mfma_f32_16x16x32_bf16 v[18:21], v[240:243], a[232:235], v[18:21]\n"
    "v_mfma_f32_16x16x32_bf16 v[22:25], v[240:243], a[236:239], v[22:25]\n"
    "buffer_load_dwordx4 v[240:243], v2, s[72:75], s87 offen offset:2048 sc1\n"
    "s_waitcnt vmcnt(37)\n"
    "v_mfma_f32_16x16x32_bf16 v[10:13], v[244:247], a[240:243], v[10:13]\n"
    "v_mfma_f32_16x16x32_bf16 v[14:17], v[244:247], a[244:247], v[14:17]\n"
    "v_mfma_f32_16x16x32_bf16 v[18:21], v[244:247], a[248:251], v[18:21]\n"
    "v_mfma_f32_16x16x32_bf16 v[22:25], v[244:247], a[252:255], v[22:25]\n"
    "buffer_load_dwordx4 v[244:247], v2, s[72:75], s87 offen offset:3072 sc1\n"
    "s_add_u32 s87, s87, 4096\n"
    "s_waitcnt vmcnt(37)\n"
    "v_mfma_f32_16x16x32_bf16 v[10:13], v[248:251], v[80:83], v[10:13]\n"
    "s_cmp_lt_u32 s89, 4\n"
    "s_cbranch_scc1 132f\n"
    "global_load_lds_dwordx4 v6, s[40:41] sc1\n"
    "global_load_lds_dwordx4 v6, s[40:41] offset:1024 sc1\n"
    "132:\n"
    "v_mfma_f32_16x16x32_bf16 v[14:17], v[248:251], v[84:87], v[14:17]\n"
    "v_mfma_f32_16x16x32_bf16 v[18:21], v[248:251], v[88:91], v[18:21]\n"
    "v_mfma_f32_16x16x32_bf16 v[22:25], v[248:251], v[92:95], v[22:25]\n"
    "buffer_load_dwordx4 v[248:251], v2, s[72:75], s87 offen offset:0 sc1\n"
    "s_waitcnt vmcnt(37)\n"
    "v_mfma_f32_16x16x32_bf16 v[26:29], v[252:255], v[96:99], v[10:13]\n"
    "v_mfma_f32_16x16x32_bf16 v[30:33], v[252:255], v[100:103], v[14:17]\n"
    "v_mfma_f32_16x16x32_bf16 v[34:37], v[252:255], v[104:107], v[18:21]\n"
    "v_mfma_f32_16x16x32_bf16 v[38:41], v[252:255], v[108:111], v[22:25]\n"
    "buffer_load_dwordx4 v[252:255], v2, s[72:75], s87 offen offset:1024 sc1\n"
    "s_add_u32 s87, s87, 2048\n"
    "s_sub_u32 s88, s88, 1\n"
    "s_cmp_lg_u32 s88, 0\n"
    "s_cbranch_scc1 11b\n"
    "s_waitcnt lgkmcnt(0)\n"
    "v_mfma_f32_16x16x32_bf16 v[10:13], v[76:79], v[58:61], 0\n"
    "v_mfma_f32_16x16x32_bf16 v[14:17], v[76:79], v[62:65], 0\n"
    "v_mfma_f32_16x16x32_bf16 v[18:21], v[76:79], v[66:69], 0\n"
    "v_mfma_f32_16x16x32_bf16 v[22:25], v[76:79], v[70:73], 0\n"
    "s_waitcnt vmcnt(39)\n"
    "v_mfma_f32_16x16x32_bf16 v[10:13], v[112:115], a[0:3], v[10:13]\n"
    "v_mfma_f32_16x16x32_bf16 v[14:17], v[112:115], a[4:7], v[14:17]\n"
    "v_mfma_f32_16x16x32_bf16 v[18:21], v[112:115], a[8:11], v[18:21]\n"
    "v_mfma_f32_16x16x32_bf16 v[22:25], v[112:115], a[12:15], v[22:25]\n"
    "s_waitcnt vmcnt(38)\n"
    "v_mfma_f32_16x16x32_bf16 v[10:13], v[116:119], a[16:19], v[10:13]\n"
    "v_mfma_f32_16x16x32_bf16 v[14:17], v[116:119], a[20:23], v[14:17]\n"
    "v_mfma_f32_16x16x32_bf16 v[18:21], v[116:119], a[24:27], v[18:21]\n"
    "v_cvt_pk_bf16_f32 v26, v26, v27\n"
    "v_cvt_pk_bf16_f32 v27, v28, v29\n"
    "v_mfma_f32_16x16x32_bf16 v[22:25], v[116:119], a[28:31], v[22:25]\n"
    "v_cvt_pk_bf16_f32 v28, v30, v31\n"
    "s_waitcnt vmcnt(37)\n"
    "v_mfma_f32_16x16x32_bf16 v[10:13], v[120:123], a[32:35], v[10:13]\n"
    "v_cvt_pk_bf16_f32 v29, v32, v33\n"
    "v_min_u32 v26, -2, v26\n"
    "v_mfma_f32_16x16x32_bf16 v[14:17], v[120:123], a[36:39], v[14:17]\n"
    "v_min_u32 v27, -2, v27\n"
    "v_min_u32 v28, -2, v28\n"
    "v_mfma_f32_16x16x32_bf16 v[18:21], v[120:123], a[40:43], v[18:21]\n"
    "v_min_u32 v29, -2, v29\n"
    "global_store_dwordx4 v6, v[26:29], s[36:37] sc1\n"
    "v_mfma_f32_16x16x32_bf16 v[22:25], v[120:123], a[44:47], v[22:25]\n"
    "v_cvt_pk_bf16_f32 v34, v34, v35\n"
    "s_waitcnt vmcnt(36)\n"
    "v_mfma_f32_16x16x32_bf16 v[10:13], v[124:127], a[48:51], v[10:13]\n"
    "v_cvt_pk_bf16_f32 v35, v36, v37\n"
    "v_cvt_pk_bf16_f32 v36, v38, v39\n"
    "v_mfma_f32_16x16x32_bf16 v[14:17], v[124:127], a[52:55], v[14:17]\n"
    "v_cvt_pk_bf16_f32 v37, v40, v41\n"
    "v_min_u32 v34, -2, v34\n"
    "v_mfma_f32_16x16x32_bf16 v[18:21], v[124:127], a[56:59], v[18:21]\n"
    "v_min_u32 v35, -2, v35\n"
    "v_min_u32 v36, -2, v36\n"
    "v_mfma_f32_16x16x32_bf16 v[22:25], v[124:127], a[60:63], v[22:25]\n"
    "v_min_u32 v37, -2, v37\n"
    "s_waitcnt vmcnt(35)\n"
    "v_mfma_f32_16x16x32_bf16 v[10:13], v[128:131], a[64:67], v[10:13]\n"
    "global_store_dwordx4 v6, v[34:37], s[38:39] sc1\n"
    "s_add_u32 s90, s68, s89\n"
    "v_mfma_f32_16x16x32_bf16 v[14:17], v[128:131], a[68:71], v[14:17]\n"
    "s_and_b32 s84, s90, 7\n"
    "s_lshl_b32 s84, s84, 16\n"
    "v_mfma_f32_16x16x32_bf16 v[18:21], v[128:131], a[72:75], v[18:21]\n"
    "s_add_u32 s36, s50, s84\n"
    "s_addc_u32 s37, s51, 0\n"
    "v_mfma_f32_16x16x32_bf16 v[22:25], v[128:131], a[76:79], v[22:25]\n"
    "s_add_u32 s38, s36, 1024\n"
    "s_addc_u32 s39, s37, 0\n"
    "s_waitcnt vmcnt(35)\n"
    "v_mfma_f32_16x16x32_bf16 v[10:13], v[132:135], a[80:83], v[10:13]\n"
    "s_add_u32 s89, s89, 1\n"
    "s_add_u32 s90, s68, s89\n"
    "v_mfma_f32_16x16x32_bf16 v[14:17], v[132:135], a[84:87], v[14:17]\n"
    "s_and_b32 s90, s90, 7\n"
    "s_lshl_b32 s90, s90, 10\n"
    "v_mfma_f32_16x16x32_bf16 v[18:21], v[132:135], a[88:91], v[18:21]\n"
    "v_add_u32 v75, s90, v9\n"
    "s_mov_b64 exec, s[94:95]\n"
    "ds_read_b128 v[58:61], v75 offset:0\n"
    "ds_read_b128 v[62:65], v75 offset:32\n"
    "ds_read_b128 v[66:69], v75 offset:64\n"
    "ds_read_b128 v[70:73], v75 offset:96\n"
    "s_mov_b64 exec, -1\n"
    "v_mfma_f32_16x16x32_bf16 v[22:25], v[132:135], a[92:95], v[22:25]\n"
    "s_mul_i32 s84, s67, 16128\n"
    "s_waitcnt vmcnt(33)\n"
    "v_mfma_f32_16x16x32_bf16 v[10:13], v[136:139], a[96:99], v[10:13]\n"
    "s_mul_i32 s85, s68, 7168\n"
    "s_sub_u32 s46, s46, s84\n"
    "s_subb_u32 s47, s47, 0\n"
    "v_mfma_f32_16x16x32_bf16 v[14:17], v[136:139], a[100:103], v[14:17]\n"
    "s_add_u32 s46, s46, s85\n"
    "s_addc_u32 s47, s47, 0\n"
    "v_mfma_f32_16x16x32_bf16 v[18:21], v[136:139], a[104:107], v[18:21]\n"
    "v_lshrrev_b32 v74, 8, v2\n"
    "v_mul_u32_u24 v74, 768, v74\n"
    "v_mfma_f32_16x16x32_bf16 v[22:25], v[136:139], a[108:111], v[22:25]\n"
    "s_waitcnt vmcnt(32)\n"
    "v_mfma_f32_16x16x32_bf16 v[10:13], v[140:143], a[112:115], v[10:13]\n"
    "v_mfma_f32_16x16x32_bf16 v[14:17], v[140:143], a[116:119], v[14:17]\n"
    "v_mfma_f32_16x16x32_bf16 v[18:21], v[140:143], a[120:123], v[18:21]\n"
    "v_mfma_f32_16x16x32_bf16 v[22:25], v[140:143], a[124:127], v[22:25]\n"
    "s_waitcnt vmcnt(31)\n"
    "v_mfma_f32_16x16x32_bf16 v[10:13], v[144:147], a[128:131], v[10:13]\n"
    "v_mfma_f32_16x16x32_bf16 v[14:17], v[144:147], a[132:135], v[14:17]\n"
    "v_mfma_f32_16x16x32_bf16 v[18:21], v[144:147], a[136:139], v[18:21]\n"
    "v_mfma_f32_16x16x32_bf16 v[22:25], v[144:147], a[140:143], v[22:25]\n"
    "s_waitcnt vmcnt(30)\n"
    "v_mfma_f32_16x16x32_bf16 v[10:13], v[148:151], a[144:147], v[10:13]\n"
    "v_mfma_f32_16x16x32_bf16 v[14:17], v[148:151], a[148:151], v[14:17]\n"
    "v_mfma_f32_16x16x32_bf16 v[18:21], v[148:151], a[152:155], v[18:21]\n"
    "v_mfma_f32_16x16x32_bf16 v[22:25], v[148:151], a[156:159], v[22:25]\n"
    "s_waitcnt vmcnt(29)\n"
    "v_mfma_f32_16x16x32_bf16 v[10:13], v[152:155], a[160:163], v[10:13]\n"
    "v_mfma_f32_16x16x32_bf16 v[14:17], v[152:155], a[164:167], v[14:17]\n"
    "v_mfma_f32_16x16x32_bf16 v[18:21], v[152:155], a[168:171], v[18:21]\n"
    "v_mfma_f32_16x16x32_bf16 v[22:25], v[152:155], a[172:175], v[22:25]\n"
    "s_waitcnt vmcnt(28)\n"
    "v_mfma_f32_16x16x32_bf16 v[10:13], v[156:159], a[176:179], v[10:13]\n"
    "v_mfma_f32_16x16x32_bf16 v[14:17], v[156:159], a[180:183], v[14:17]\n"
    "v_mfma_f32_16x16x32_bf16 v[18:21], v[156:159], a[184:187], v[18:21]\n"
    "v_mfma_f32_16x16x32_bf16 v[22:25], v[156:159], a[188:191], v[22:25]\n"
    "s_waitcnt vmcnt(27)\n"
    "v_mfma_f32_16x16x32_bf16 v[10:13], v[160:163], a[192:195], v[10:13]\n"
    "v_mfma_f32_16x16x32_bf16 v[14:17], v[160:163], a[196:199], v[14:17]\n"
    "v_mfma_f32_16x16x32_bf16 v[18:21], v[160:163], a[200:203], v[18:21]\n"
    "v_mfma_f32_16x16x32_bf16 v[22:25], v[160:163], a[204:207], v[22:25]\n"
    "s_waitcnt vmcnt(26)\n"
    "v_mfma_f32_16x16x32_bf16 v[10:13], v[164:167], a[208:211], v[10:13]\n"
    "v_mfma_f32_16x16x32_bf16 v[14:17], v[164:167], a[212:215], v[14:17]\n"
    "v_mfma_f32_16x16x32_bf16 v[18:21], v[164:167], a[216:219], v[18:21]\n"
    "v_mfma_f32_16x16x32_bf16 v[22:25], v[164:167], a[220:223], v[22:25]\n"
    "s_waitcnt vmcnt(25)\n"
    "v_mfma_f32_16x16x32_bf16 v[10:13], v[168:171], a[224:227], v[10:13]\n"
    "v_add_u32 v164, v74, v2\n"
    "v_add_u32 v165, 4096, v164\n"
    "v_mfma_f32_16x16x32_bf16 v[14:17], v[168:171], a[228:231], v[14:17]\n"
    "global_load_dwordx4 v[152:155], v164, s[46:47] sc1\n"
    "global_load_dwordx4 v[156:159], v165, s[46:47] sc1\n"
    "v_mfma_f32_16x16x32_bf16 v[18:21], v[168:171], a[232:235], v[18:21]\n"
    "s_sub_u32 s91, s68, 5\n"
    "s_and_b32 s91, s91, 7\n"
    "v_mfma_f32_16x16x32_bf16 v[22:25], v[168:171], a[236:239], v[22:25]\n"
    "s_lshl_b32 s91, s91, 11\n"
    "s_waitcnt vmcnt(26)\n"
    "v_mfma_f32_16x16x32_bf16 v[10:13], v[172:175], a[240:243], v[10:13]\n"
    "s_add_u32 s40, s52, s91\n"
    "s_addc_u32 s41, s53, 0\n"
    "v_mfma_f32_16x16x32_bf16 v[14:17], v[172:175], a[244:247], v[14:17]\n"
    "global_load_dwordx4 v[144:147], v6, s[40:41] sc1\n"
    "global_load_dwordx4 v[148:151], v6, s[40:41] offset:1024 sc1\n"
    "v_mfma_f32_16x16x32_bf16 v[18:21], v[172:175], a[248:251], v[18:21]\n"
    "v_mfma_f32_16x16x32_bf16 v[22:25], v[172:175], a[252:255], v[22:25]\n"
    "s_waitcnt vmcnt(27)\n"
    "v_mfma_f32_16x16x32_bf16 v[10:13], v[176:179], v[80:83], v[10:13]\n"
    "v_mfma_f32_16x16x32_bf16 v[14:17], v[176:179], v[84:87], v[14:17]\n"
    "v_mfma_f32_16x16x32_bf16 v[18:21], v[176:179], v[88:91], v[18:21]\n"
    "v_mfma_f32_16x16x32_bf16 v[22:25], v[176:179], v[92:95], v[22:25]\n"
    "s_waitcnt vmcnt(26)\n"
    "v_mfma_f32_16x16x32_bf16 v[26:29], v[180:183], v[96:99], v[10:13]\n"
    "v_mfma_f32_16x16x32_bf16 v[30:33], v[180:183], v[100:103], v[14:17]\n"
    "v_mfma_f32_16x16x32_bf16 v[34:37], v[180:183], v[104:107], v[18:21]\n"
    "v_mfma_f32_16x16x32_bf16 v[38:41], v[180:183], v[108:111], v[22:25]\n"
    "s_waitcnt lgkmcnt(0)\n"
    "v_mfma_f32_16x16x32_bf16 v[10:13], v[76:79], v[58:61], v[54:57]\n"
    "v_mfma_f32_16x16x32_bf16 v[14:17], v[76:79], v[62:65], v[54:57]\n"
    "v_mfma_f32_16x16x32_bf16 v[18:21], v[76:79], v[66:69], v[54:57]\n"
    "v_mfma_f32_16x16x32_bf16 v[22:25], v[76:79], v[70:73], v[54:57]\n"
    "s_waitcnt vmcnt(25)\n"
    "v_mfma_f32_16x16x32_bf16 v[10:13], v[184:187], a[0:3], v[10:13]\n"
    "v_mfma_f32_16x16x32_bf16 v[14:17], v[184:187], a[4:7], v[14:17]\n"
    "v_mfma_f32_16x16x32_bf16 v[18:21], v[184:187], a[8:11], v[18:21]\n"
    "v_mfma_f32_16x16x32_bf16 v[22:25], v[184:187], a[12:15], v[22:25]\n"
    "s_waitcnt vmcnt(24)\n"
    "v_mfma_f32_16x16x32_bf16 v[10:13], v[188:191], a[16:19], v[10:13]\n"
    "v_mfma_f32_16x16x32_bf16 v[14:17], v[188:191], a[20:23], v[14:17]\n"
    "v_mfma_f32_16x16x32_bf16 v[18:21], v[188:191], a[24:27], v[18:21]\n"
    "v_cvt_pk_bf16_f32 v26, v26, v27\n"
    "v_cvt_pk_bf16_f32 v27, v28, v29\n"
    "v_mfma_f32_16x16x32_bf16 v[22:25], v[188:191], a[28:31], v[22:25]\n"
    "v_cvt_pk_bf16_f32 v28, v30, v31\n"
    "s_waitcnt vmcnt(23)\n"
    "v_mfma_f32_16x16x32_bf16 v[10:13], v[192:195], a[32:35], v[10:13]\n"
    "v_cvt_pk_bf16_f32 v29, v32, v33\n"
    "v_min_u32 v26, -2, v26\n"
    "v_mfma_f32_16x16x32_bf16 v[14:17], v[192:195], a[36:39], v[14:17]\n"
    "v_min_u32 v27, -2, v27\n"
    "v_min_u32 v28, -2, v28\n"
    "v_mfma_f32_16x16x32_bf16 v[18:21], v[192:195], a[40:43], v[18:21]\n"
    "v_min_u32 v29, -2, v29\n"
    "global_store_dwordx4 v6, v[26:29], s[36:37] sc1\n"
    "v_mfma_f32_16x16x32_bf16 v[22:25], v[192:195], a[44:47], v[22:25]\n"
    "v_cvt_pk_bf16_f32 v34, v34, v35\n"
    "s_waitcnt vmcnt(22)\n"
    "v_mfma_f32_16x16x32_bf16 v[10:13], v[196:199], a[48:51], v[10:13]\n"
    "v_cvt_pk_bf16_f32 v35, v36, v37\n"
    "v_cvt_pk_bf16_f32 v36, v38, v39\n"
    "v_mfma_f32_16x16x32_bf16 v[14:17], v[196:199], a[52:55], v[14:17]\n"
    "v_cvt_pk_bf16_f32 v37, v40, v41\n"
    "v_min_u32 v34, -2, v34\n"
    "v_mfma_f32_16x16x32_bf16 v[18:21], v[196:199], a[56:59], v[18:21]\n"
    "v_min_u32 v35, -2, v35\n"
    "v_min_u32 v36, -2, v36\n"
    "v_mfma_f32_16x16x32_bf16 v[22:25], v[196:199], a[60:63], v[22:25]\n"
    "v_min_u32 v37, -2, v37\n"
    "s_waitcnt vmcnt(21)\n"
    "v_mfma_f32_16x16x32_bf16 v[10:13], v[200:203], a[64:67], v[10:13]\n"
    "global_store_dwordx4 v6, v[34:37], s[38:39] sc1\n"
    "v_bfe_u32 v74, v2, 4, 1\n"
    "v_mfma_f32_16x16x32_bf16 v[14:17], v[200:203], a[68:71], v[14:17]\n"
    "v_lshlrev_b32 v74, 4, v74\n"
    "v_mov_b32 v166, 0x3f80\n"
    "v_mfma_f32_16x16x32_bf16 v[18:21], v[200:203], a[72:75], v[18:21]\n"
    "v_lshlrev_b32 v166, v74, v166\n"
    "v_bfe_u32 v74, v2, 5, 3\n"
    "v_mfma_f32_16x16x32_bf16 v[22:25], v[200:203], a[76:79], v[22:25]\n"
    "v_cmp_eq_u32 vcc, 0, v74\n"
    "s_waitcnt vmcnt(21)\n"
    "v_mfma_f32_16x16x32_bf16 v[10:13], v[204:207], a[80:83], v[10:13]\n"
    "s_nop 1\n"
    "v_cndmask_b32 v160, 0, v166, vcc\n"
    "v_mfma_f32_16x16x32_bf16 v[14:17], v[204:207], a[84:87], v[14:17]\n"
    "v_cmp_eq_u32 vcc, 1, v74\n"
    "s_nop 1\n"
    "v_mfma_f32_16x16x32_bf16 v[18:21], v[204:207], a[88:91], v[18:21]\n"
    "v_cndmask_b32 v161, 0, v166, vcc\n"
    "v_cmp_eq_u32 vcc, 2, v74\n"
    "v_mfma_f32_16x16x32_bf16 v[22:25], v[204:207], a[92:95], v[22:25]\n"
    "s_nop 1\n"
    "s_waitcnt vmcnt(19)\n"
    "v_mfma_f32_16x16x32_bf16 v[10:13], v[208:211], a[96:99], v[10:13]\n"
    "v_cndmask_b32 v162, 0, v166, vcc\n"
    "v_cmp_eq_u32 vcc, 3, v74\n"
    "v_mfma_f32_16x16x32_bf16 v[14:17], v[208:211], a[100:103], v[14:17]\n"
    "s_nop 1\n"
    "v_cndmask_b32 v163, 0, v166, vcc\n"
    "v_mfma_f32_16x16x32_bf16 v[18:21], v[208:211], a[104:107], v[18:21]\n"
    "v_lshrrev_b32 v74, 8, v2\n"
    "v_mul_u32_u24 v74, 1144, v74\n"
    "v_mfma_f32_16x16x32_bf16 v[22:25], v[208:211], a[108:111], v[22:25]\n"
    "v_add_u32 v166, v74, v7\n"
    "s_waitcnt vmcnt(18)\n"
    "v_mfma_f32_16x16x32_bf16 v[10:13], v[212:215], a[112:115], v[10:13]\n"
    "s_and_b32 s84, s68, 1\n"
    "s_mul_i32 s84, s84, 2304\n"
    "v_mfma_f32_16x16x32_bf16 v[14:17], v[212:215], a[116:119], v[14:17]\n"
    "s_lshr_b32 s85, s68, 1\n"
    "s_lshl_b32 s85, s85, 5\n"
    "v_mfma_f32_16x16x32_bf16 v[18:21], v[212:215], a[120:123], v[18:21]\n"
    "s_add_u32 s84, s84, s85\n"
    "s_add_u32 s84, s84, 1024\n"
    "v_mfma_f32_16x16x32_bf16 v[22:25], v[212:215], a[124:127], v[22:25]\n"
    "s_lshl_b32 s85, s68, 7\n"
    "s_waitcnt vmcnt(17)\n"
    "v_mfma_f32_16x16x32_bf16 v[10:13], v[216:219], a[128:131], v[10:13]\n"
    "s_sub_u32 s84, s84, s85\n"
    "s_mul_i32 s85, s67, 24\n"
    "v_mfma_f32_16x16x32_bf16 v[14:17], v[216:219], a[132:135], v[14:17]\n"
    "s_sub_u32 s84, s84, s85\n"
    "s_add_u32 s36, s80, s84\n"
    "s_addc_u32 s37, s81, 0\n"
    "v_mfma_f32_16x16x32_bf16 v[18:21], v[216:219], a[136:139], v[18:21]\n"
    "v_bfe_u32 v58, v2, 4, 4\n"
    "v_mul_u32_u24 v58, 544, v58\n"
    "v_mfma_f32_16x16x32_bf16 v[22:25], v[216:219], a[140:143], v[22:25]\n"
    "v_lshrrev_b32 v59, 8, v2\n"
    "s_waitcnt vmcnt(16)\n"
    "v_mfma_f32_16x16x32_bf16 v[10:13], v[220:223], a[144:147], v[10:13]\n"
    "v_lshl_add_u32 v58, v59, 3, v58\n"
    "s_add_u32 s88, s66, 57344\n"
    "v_mfma_f32_16x16x32_bf16 v[14:17], v[220:223], a[148:151], v[14:17]\n"
    "s_lshl_b32 s89, s67, 5\n"
    "s_add_u32 s89, s89, s88\n"
    "v_mfma_f32_16x16x32_bf16 v[18:21], v[220:223], a[152:155], v[18:21]\n"
    "v_add_u32 v58, s89, v58\n"
    "v_lshrrev_b32 v61, 7, v2\n"
    "v_mfma_f32_16x16x32_bf16 v[22:25], v[220:223], a[156:159], v[22:25]\n"
    "v_and_b32 v62, 0x70, v2\n"
    "s_waitcnt vmcnt(15)\n"
    "v_mfma_f32_16x16x32_bf16 v[10:13], v[224:227], a[160:163], v[10:13]\n"
    "s_mul_i32 s89, s67, 2176\n"
    "s_add_u32 s89, s89, s88\n"
    "v_mfma_f32_16x16x32_bf16 v[14:17], v[224:227], a[164:167], v[14:17]\n"
    "v_mul_u32_u24 v59, 136, v61\n"
    "v_add3_u32 v59, v59, v62, s89\n"
    "v_mfma_f32_16x16x32_bf16 v[18:21], v[224:227], a[168:171], v[18:21]\n"
    "s_mul_i32 s89, s67, 18400\n"
    "v_mul_u32_u24 v60, 1152, v61\n"
    "v_mfma_f32_16x16x32_bf16 v[22:25], v[224:227], a[172:175], v[22:25]\n"
    "v_add3_u32 v60, v60, v62, s89\n"
    "s_waitcnt vmcnt(14)\n"
    "v_mfma_f32_16x16x32_bf16 v[10:13], v[228:231], a[176:179], v[10:13]\n"
    "v_add_u32 v61, 9216, v60\n"
    "v_mfma_f32_16x16x32_bf16 v[14:17], v[228:231], a[180:183], v[14:17]\n"
    "v_mfma_f32_16x16x32_bf16 v[18:21], v[228:231], a[184:187], v[18:21]\n"
    "v_mfma_f32_16x16x32_bf16 v[22:25], v[228:231], a[188:191], v[22:25]\n"
    "s_waitcnt vmcnt(13)\n"
    "v_mfma_f32_16x16x32_bf16 v[10:13], v[232:235], a[192:195], v[10:13]\n"
    "s_sub_u32 s91, s68, 6\n"
    "s_and_b32 s91, s91, 7\n"
    "v_mfma_f32_16x16x32_bf16 v[14:17], v[232:235], a[196:199], v[14:17]\n"
    "s_lshl_b32 s91, s91, 11\n"
    "s_add_u32 s40, s52, s91\n"
    "s_addc_u32 s41, s53, 0\n"
    "v_mfma_f32_16x16x32_bf16 v[18:21], v[232:235], a[200:203], v[18:21]\n"
    "global_load_dwordx4 v[184:187], v6, s[40:41] sc1\n"
    "global_load_dwordx4 v[188:191], v6, s[40:41] offset:1024 sc1\n"
    "v_mfma_f32_16x16x32_bf16 v[22:25], v[232:235], a[204:207], v[22:25]\n"
    "s_mul_i32 s90, s67, 14336\n"
    "s_waitcnt vmcnt(14)\n"
    "v_mfma_f32_16x16x32_bf16 v[10:13], v[236:239], a[208:211], v[10:13]\n"
    "s_add_u32 s90, s90, s66\n"
    "v_add_u32 v75, s90, v2\n"
    "v_mfma_f32_16x16x32_bf16 v[14:17], v[236:239], a[212:215], v[14:17]\n"
    "s_waitcnt vmcnt(10)\n"
    "ds_read_b128 v[112:115], v75 offset:6144\n"
    "ds_read_b128 v[116:119], v75 offset:7168\n"
    "v_mfma_f32_16x16x32_bf16 v[18:21], v[236:239], a[216:219], v[18:21]\n"
    "ds_read_b128 v[120:123], v75 offset:8192\n"
    "ds_read_b128 v[124:127], v75 offset:9216\n"
    "v_mfma_f32_16x16x32_bf16 v[22:25], v[236:239], a[220:223], v[22:25]\n"
    "ds_read_b128 v[128:131], v75 offset:10240\n"
    "s_waitcnt vmcnt(13)\n"
    "v_mfma_f32_16x16x32_bf16 v[10:13], v[240:243], a[224:227], v[10:13]\n"
    "ds_read_b128 v[132:135], v75 offset:11264\n"
    "ds_read_b128 v[136:139], v75 offset:12288\n"
    "v_mfma_f32_16x16x32_bf16 v[14:17], v[240:243], a[228:231], v[14:17]\n"
    "ds_read_b128 v[140:143], v75 offset:13312\n"
    "v_mfma_f32_16x16x32_bf16 v[18:21], v[240:243], a[232:235], v[18:21]\n"
    "v_mfma_f32_16x16x32_bf16 v[22:25], v[240:243], a[236:239], v[22:25]\n"
    "s_waitcnt vmcnt(12)\n"
    "v_mfma_f32_16x16x32_bf16 v[10:13], v[244:247], a[240:243], v[10:13]\n"
    "v_mfma_f32_16x16x32_bf16 v[14:17], v[244:247], a[244:247], v[14:17]\n"
    "v_mfma_f32_16x16x32_bf16 v[18:21], v[244:247], a[248:251], v[18:21]\n"
    "v_mfma_f32_16x16x32_bf16 v[22:25], v[244:247], a[252:255], v[22:25]\n"
    "s_waitcnt vmcnt(11)\n"
    "v_mfma_f32_16x16x32_bf16 v[10:13], v[248:251], v[80:83], v[10:13]\n"
    "v_mfma_f32_16x16x32_bf16 v[14:17], v[248:251], v[84:87], v[14:17]\n"
    "v_mfma_f32_16x16x32_bf16 v[18:21], v[248:251], v[88:91], v[18:21]\n"
    "v_mfma_f32_16x16x32_bf16 v[22:25], v[248:251], v[92:95], v[22:25]\n"
    "s_waitcnt vmcnt(10)\n"
    "v_mfma_f32_16x16x32_bf16 v[10:13], v[252:255], v[96:99], v[10:13]\n"
    "v_mfma_f32_16x16x32_bf16 v[14:17], v[252:255], v[100:103], v[14:17]\n"
    "v_mfma_f32_16x16x32_bf16 v[18:21], v[252:255], v[104:107], v[18:21]\n"
    "v_mfma_f32_16x16x32_bf16 v[22:25], v[252:255], v[108:111], v[22:25]\n"
    "s_sub_u32 s91, s68, 7\n"
    "s_and_b32 s91, s91, 7\n"
    "s_lshl_b32 s91, s91, 11\n"
    "s_add_u32 s40, s52, s91\n"
    "s_addc_u32 s41, s53, 0\n"
    "global_load_dwordx4 v[192:195], v6, s[40:41] sc1\n"
    "global_load_dwordx4 v[196:199], v6, s[40:41] offset:1024 sc1\n"
    "s_waitcnt lgkmcnt(0)\n"
    "v_max3_u32 v74, v112, v113, v114\n"
    "v_max3_u32 v74, v74, v115, v116\n"
    "v_max3_u32 v74, v74, v117, v118\n"
    "v_max3_u32 v74, v74, v119, v120\n"
    "v_max3_u32 v74, v74, v121, v122\n"
    "v_max3_u32 v74, v74, v123, v124\n"
    "v_max3_u32 v74, v74, v125, v126\n"
    "v_max_u32 v74, v74, v127\n"
    "v_cmp_eq_u32 vcc, -1, v74\n"
    "s_cbranch_vccnz 134f\n"
    "135:\n"
    "v_mfma_f32_16x16x32_bf16 v[10:13], v[42:45], v[112:115], v[10:13]\n"
    "v_max3_u32 v74, v128, v129, v130\n"
    "v_max3_u32 v74, v74, v131, v132\n"
    "v_mfma_f32_16x16x32_bf16 v[14:17], v[46:49], v[112:115], v[14:17]\n"
    "v_max3_u32 v74, v74, v133, v134\n"
    "v_max3_u32 v74, v74, v135, v136\n"
    "v_mfma_f32_16x16x32_bf16 v[18:21], v[42:45], v[116:119], v[18:21]\n"
    "v_max3_u32 v74, v74, v137, v138\n"
    "v_max3_u32 v74, v74, v139, v140\n"
    "v_mfma_f32_16x16x32_bf16 v[22:25], v[46:49], v[116:119], v[22:25]\n"
    "v_max3_u32 v74, v74, v141, v142\n"
    "v_max_u32 v74, v74, v143\n"
    "v_mfma_f32_16x16x32_bf16 v[10:13], v[42:45], v[120:123], v[10:13]\n"
    "v_cmp_eq_u32 vcc, -1, v74\n"
    "s_cbranch_vccnz 136f\n"
    "137:\n"
    "v_mfma_f32_16x16x32_bf16 v[14:17], v[46:49], v[120:123], v[14:17]\n"
    "v_mfma_f32_16x16x32_bf16 v[18:21], v[42:45], v[124:127], v[18:21]\n"
    "v_mfma_f32_16x16x32_bf16 v[22:25], v[46:49], v[124:127], v[22:25]\n"
    "v_mfma_f32_16x16x32_bf16 v[10:13], v[42:45], v[128:131], v[10:13]\n"
    "v_mfma_f32_16x16x32_bf16 v[14:17], v[46:49], v[128:131], v[14:17]\n"
    "v_mfma_f32_16x16x32_bf16 v[18:21], v[42:45], v[132:135], v[18:21]\n"
    "v_mfma_f32_16x16x32_bf16 v[22:25], v[46:49], v[132:135], v[22:25]\n"
    "v_mfma_f32_16x16x32_bf16 v[10:13], v[42:45], v[136:139], v[10:13]\n"
    "v_mfma_f32_16x16x32_bf16 v[14:17], v[46:49], v[136:139], v[14:17]\n"
    "v_mfma_f32_16x16x32_bf16 v[18:21], v[42:45], v[140:143], v[18:21]\n"
    "v_mfma_f32_16x16x32_bf16 v[22:25], v[46:49], v[140:143], v[22:25]\n"
    "s_waitcnt vmcnt(6)\n"
    "v_max3_u32 v74, v152, v153, v154\n"
    "v_max3_u32 v74, v74, v155, v156\n"
    "v_max3_u32 v74, v74, v157, v158\n"
    "v_max3_u32 v74, v74, v159, v144\n"
    "v_max3_u32 v74, v74, v145, v146\n"
    "v_max3_u32 v74, v74, v147, v148\n"
    "v_max3_u32 v74, v74, v149, v150\n"
    "v_max_u32 v74, v74, v151\n"
    "v_cmp_eq_u32 vcc, -1, v74\n"
    "s_cbranch_vccnz 138f\n"
    "139:\n"
    "v_mfma_f32_16x16x32_bf16 v[26:29], v[160:163], v[152:155], 0\n"
    "v_mfma_f32_16x16x32_bf16 v[26:29], v[160:163], v[156:159], v[26:29]\n"
    "v_mfma_f32_16x16x32_bf16 v[10:13], v[42:45], v[144:147], v[10:13]\n"
    "v_mfma_f32_16x16x32_bf16 v[14:17], v[46:49], v[144:147], v[14:17]\n"
    "v_mfma_f32_16x16x32_bf16 v[18:21], v[42:45], v[148:151], v[18:21]\n"
    "v_mfma_f32_16x16x32_bf16 v[22:25], v[46:49], v[148:151], v[22:25]\n"
    "s_waitcnt vmcnt(2)\n"
    "v_max3_u32 v74, v184, v185, v186\n"
    "v_max3_u32 v74, v74, v187, v188\n"
    "v_max3_u32 v74, v74, v189, v190\n"
    "v_max_u32 v74, v74, v191\n"
    "v_cmp_eq_u32 vcc, -1, v74\n"
    "s_cbranch_vccnz 140f\n"
    "141:\n"
    "v_cvt_pk_bf16_f32 v26, v26, v27\n"
    "v_cvt_pk_bf16_f32 v27, v28, v29\n"
    "s_mov_b32 exec_hi, 0\n"
    "global_store_dwordx2 v166, v[26:27], s[36:37] sc1\n"
    "s_mov_b32 exec_hi, -1\n"
    "v_mfma_f32_16x16x32_bf16 v[10:13], v[42:45], v[184:187], v[10:13]\n"
    "v_mfma_f32_16x16x32_bf16 v[14:17], v[46:49], v[184:187], v[14:17]\n"
    "v_mfma_f32_16x16x32_bf16 v[18:21], v[42:45], v[188:191], v[18:21]\n"
    "v_mfma_f32_16x16x32_bf16 v[22:25], v[46:49], v[188:191], v[22:25]\n"
    "s_waitcnt vmcnt(1)\n"
    "v_max3_u32 v74, v192, v193, v194\n"
    "v_max3_u32 v74, v74, v195, v196\n"
    "v_max3_u32 v74, v74, v197, v198\n"
    "v_max_u32 v74, v74, v199\n"
    "v_cmp_eq_u32 vcc, -1, v74\n"
    "s_cbranch_vccnz 142f\n"
    "143:\n"
    "v_mfma_f32_16x16x32_bf16 v[10:13], v[42:45], v[192:195], v[10:13]\n"
    "v_mfma_f32_16x16x32_bf16 v[14:17], v[46:49], v[192:195], v[14:17]\n"
    "v_mfma_f32_16x16x32_bf16 v[18:21], v[42:45], v[196:199], v[18:21]\n"
    "v_mfma_f32_16x16x32_bf16 v[22:25], v[46:49], v[196:199], v[22:25]\n"
    "s_nop 4\n"
    "v_cvt_pk_bf16_f32 v10, v10, v11\n"
    "v_cvt_pk_bf16_f32 v11, v12, v13\n"
    "ds_write_b64 v58, v[10:11] offset:0\n"
    "v_cvt_pk_bf16_f32 v14, v14, v15\n"
    "v_cvt_pk_bf16_f32 v15, v16, v17\n"
    "ds_write_b64 v58, v[14:15] offset:136\n"
    "v_cvt_pk_bf16_f32 v18, v18, v19\n"
    "v_cvt_pk_bf16_f32 v19, v20, v21\n"
    "ds_write_b64 v58, v[18:19] offset:272\n"
    "v_cvt_pk_bf16_f32 v22, v22, v23\n"
    "v_cvt_pk_bf16_f32 v23, v24, v25\n"
    "ds_write_b64 v58, v[22:23] offset:408\n"
    "s_waitcnt lgkmcnt(0)\n"
    "s_barrier\n"
    "ds_read2_b64 v[10:13], v59 offset0:0 offset1:1\n"
    "ds_read2_b64 v[14:17], v59 offset0:136 offset1:137\n"
    "s_waitcnt lgkmcnt(1)\n"
    "global_store_dwordx4 v60, v[10:13], s[80:81] sc1\n"
    "s_waitcnt lgkmcnt(0)\n"
    "global_store_dwordx4 v61, v[14:17], s[80:81] sc1\n"
    "s_mov_b32 m0, s86\n"
    "s_branch 99f\n"
    "122:\n"
    "s_cmp_eq_u32 s92, 0\n"
    "s_cbranch_scc1 123b\n"
    "s_sub_u32 s92, s92, 1\n"
    "s_sleep 4\n"
    "v_mov_b32 v158, 0\n"
    "global_load_dword v158, v158, s[48:49] sc1\n"
    "s_waitcnt vmcnt(0)\n"
    "v_cmp_gt_u32 vcc, 0x80, v158\n"
    "s_cbranch_vccnz 122b\n"
    "s_branch 123b\n"
    "128:\n"
    "s_cmp_eq_u32 s92, 0\n"
    "s_cbranch_scc1 129b\n"
    "s_sub_u32 s92, s92, 1\n"
    "s_waitcnt lgkmcnt(0)\n"
    "s_branch 124b\n"
    "134:\n"
    "s_cmp_eq_u32 s92, 0\n"
    "s_cbranch_scc1 135b\n"
    "s_sub_u32 s92, s92, 1\n"
    "s_sleep 2\n"
    "s_sub_u32 s91, s68, 1\n"
    "s_and_b32 s91, s91, 7\n"
    "s_lshl_b32 s91, s91, 11\n"
    "s_add_u32 s42, s52, s91\n"
    "s_addc_u32 s43, s53, 0\n"
    "global_load_dwordx4 v[112:115], v6, s[42:43] sc1\n"
    "global_load_dwordx4 v[116:119], v6, s[42:43] offset:1024 sc1\n"
    "s_sub_u32 s91, s68, 2\n"
    "s_and_b32 s91, s91, 7\n"
    "s_lshl_b32 s91, s91, 11\n"
    "s_add_u32 s42, s52, s91\n"
    "s_addc_u32 s43, s53, 0\n"
    "global_load_dwordx4 v[120:123], v6, s[42:43] sc1\n"
    "global_load_dwordx4 v[124:127], v6, s[42:43] offset:1024 sc1\n"
    "s_sub_u32 s91, s68, 3\n"
    "s_and_b32 s91, s91, 7\n"
    "s_lshl_b32 s91, s91, 11\n"
    "s_add_u32 s42, s52, s91\n"
    "s_addc_u32 s43, s53, 0\n"
    "global_load_dwordx4 v[128:131], v6, s[42:43] sc1\n"
    "global_load_dwordx4 v[132:135], v6, s[42:43] offset:1024 sc1\n"
    "s_sub_u32 s91, s68, 4\n"
    "s_and_b32 s91, s91, 7\n"
    "s_lshl_b32 s91, s91, 11\n"
    "s_add_u32 s42, s52, s91\n"
    "s_addc_u32 s43, s53, 0\n"
    "global_load_dwordx4 v[136:139], v6, s[42:43] sc1\n"
    "global_load_dwordx4 v[140:143], v6, s[42:43] offset:1024 sc1\n"
    "s_sub_u32 s91, s68, 5\n"
    "s_and_b32 s91, s91, 7\n"
    "s_lshl_b32 s91, s91, 11\n"
    "s_add_u32 s42, s52, s91\n"
    "s_addc_u32 s43, s53, 0\n"
    "global_load_dwordx4 v[144:147], v6, s[42:43] sc1\n"
    "global_load_dwordx4 v[148:151], v6, s[42:43] offset:1024 sc1\n"
    "s_sub_u32 s91, s68, 6\n"
    "s_and_b32 s91, s91, 7\n"
    "s_lshl_b32 s91, s91, 11\n"
    "s_add_u32 s42, s52, s91\n"
    "s_addc_u32 s43, s53, 0\n"
    "global_load_dwordx4 v[184:187], v6, s[42:43] sc1\n"
    "global_load_dwordx4 v[188:191], v6, s[42:43] offset:1024 sc1\n"
    "s_sub_u32 s91, s68, 7\n"
    "s_and_b32 s91, s91, 7\n"
    "s_lshl_b32 s91, s91, 11\n"
    "s_add_u32 s42, s52, s91\n"
    "s_addc_u32 s43, s53, 0\n"
    "global_load_dwordx4 v[192:195], v6, s[42:43] sc1\n"
    "global_load_dwordx4 v[196:199], v6, s[42:43] offset:1024 sc1\n"
    "s_waitcnt vmcnt(0)\n"
    "v_max3_u32 v74, v112, v113, v114\n"
    "v_max3_u32 v74, v74, v115, v116\n"
    "v_max3_u32 v74, v74, v117, v118\n"
    "v_max3_u32 v74, v74, v119, v120\n"
    "v_max3_u32 v74, v74, v121, v122\n"
    "v_max3_u32 v74, v74, v123, v124\n"
    "v_max3_u32 v74, v74, v125, v126\n"
    "v_max_u32 v74, v74, v127\n"
    "v_cmp_eq_u32 vcc, -1, v74\n"
    "s_cbranch_vccnz 134b\n"
    "s_branch 135b\n"
    "136:\n"
    "s_cmp_eq_u32 s92, 0\n"
    "s_cbranch_scc1 137b\n"
    "s_sub_u32 s92, s92, 1\n"
    "s_sleep 2\n"
    "s_sub_u32 s91, s68, 3\n"
    "s_and_b32 s91, s91, 7\n"
    "s_lshl_b32 s91, s91, 11\n"
    "s_add_u32 s42, s52, s91\n"
    "s_addc_u32 s43, s53, 0\n"
    "global_load_dwordx4 v[128:131], v6, s[42:43] sc1\n"
    "global_load_dwordx4 v[132:135], v6, s[42:43] offset:1024 sc1\n"
    "s_sub_u32 s91, s68, 4\n"
    "s_and_b32 s91, s91, 7\n"
    "s_lshl_b32 s91, s91, 11\n"
    "s_add_u32 s42, s52, s91\n"
    "s_addc_u32 s43, s53, 0\n"
    "global_load_dwordx4 v[136:139], v6, s[42:43] sc1\n"
    "global_load_dwordx4 v[140:143], v6, s[42:43] offset:1024 sc1\n"
    "s_sub_u32 s91, s68, 5\n"
    "s_and_b32 s91, s91, 7\n"
    "s_lshl_b32 s91, s91, 11\n"
    "s_add_u32 s42, s52, s91\n"
    "s_addc_u32 s43, s53, 0\n"
    "global_load_dwordx4 v[144:147], v6, s[42:43] sc1\n"
    "global_load_dwordx4 v[148:151], v6, s[42:43] offset:1024 sc1\n"
    "s_sub_u32 s91, s68, 6\n"
    "s_and_b32 s91, s91, 7\n"
    "s_lshl_b32 s91, s91, 11\n"
    "s_add_u32 s42, s52, s91\n"
    "s_addc_u32 s43, s53, 0\n"
    "global_load_dwordx4 v[184:187], v6, s[42:43] sc1\n"
    "global_load_dwordx4 v[188:191], v6, s[42:43] offset:1024 sc1\n"
    "s_sub_u32 s91, s68, 7\n"
    "s_and_b32 s91, s91, 7\n"
    "s_lshl_b32 s91, s91, 11\n"
    "s_add_u32 s42, s52, s91\n"
    "s_addc_u32 s43, s53, 0\n"
    "global_load_dwordx4 v[192:195], v6, s[42:43] sc1\n"
    "global_load_dwordx4 v[196:199], v6, s[42:43] offset:1024 sc1\n"
    "s_waitcnt vmcnt(0)\n"
    "v_max3_u32 v74, v128, v129, v130\n"
    "v_max3_u32 v74, v74, v131, v132\n"
    "v_max3_u32 v74, v74, v133, v134\n"
    "v_max3_u32 v74, v74, v135, v136\n"
    "v_max3_u32 v74, v74, v137, v138\n"
    "v_max3_u32 v74, v74, v139, v140\n"
    "v_max3_u32 v74, v74, v141, v142\n"
    "v_max_u32 v74, v74, v143\n"
    "v_cmp_eq_u32 vcc, -1, v74\n"
    "s_cbranch_vccnz 136b\n"
    "s_branch 137b\n"
    "138:\n"
    "s_cmp_eq_u32 s92, 0\n"
    "s_cbranch_scc1 139b\n"
    "s_sub_u32 s92, s92, 1\n"
    "s_sleep 2\n"
    "global_load_dwordx4 v[152:155], v164, s[46:47] sc1\n"
    "global_load_dwordx4 v[156:159], v165, s[46:47] sc1\n"
    "s_sub_u32 s91, s68, 5\n"
    "s_and_b32 s91, s91, 7\n"
    "s_lshl_b32 s91, s91, 11\n"
    "s_add_u32 s42, s52, s91\n"
    "s_addc_u32 s43, s53, 0\n"
    "global_load_dwordx4 v[144:147], v6, s[42:43] sc1\n"
    "global_load_dwordx4 v[148:151], v6, s[42:43] offset:1024 sc1\n"
    "s_sub_u32 s91, s68, 6\n"
    "s_and_b32 s91, s91, 7\n"
    "s_lshl_b32 s91, s91, 11\n"
    "s_add_u32 s42, s52, s91\n"
    "s_addc_u32 s43, s53, 0\n"
    "global_load_dwordx4 v[184:187], v6, s[42:43] sc1\n"
    "global_load_dwordx4 v[188:191], v6, s[42:43] offset:1024 sc1\n"
    "s_sub_u32 s91, s68, 7\n"
    "s_and_b32 s91, s91, 7\n"
    "s_lshl_b32 s91, s91, 11\n"
    "s_add_u32 s42, s52, s91\n"
    "s_addc_u32 s43, s53, 0\n"
    "global_load_dwordx4 v[192:195], v6, s[42:43] sc1\n"
    "global_load_dwordx4 v[196:199], v6, s[42:43] offset:1024 sc1\n"
    "s_waitcnt vmcnt(0)\n"
    "v_max3_u32 v74, v152, v153, v154\n"
    "v_max3_u32 v74, v74, v155, v156\n"
    "v_max3_u32 v74, v74, v157, v158\n"
    "v_max3_u32 v74, v74, v159, v144\n"
    "v_max3_u32 v74, v74, v145, v146\n"
    "v_max3_u32 v74, v74, v147, v148\n"
    "v_max3_u32 v74, v74, v149, v150\n"
    "v_max_u32 v74, v74, v151\n"
    "v_cmp_eq_u32 vcc, -1, v74\n"
    "s_cbranch_vccnz 138b\n"
    "s_branch 139b\n"
    "140:\n"
    "s_cmp_eq_u32 s92, 0\n"
    "s_cbranch_scc1 141b\n"
    "s_sub_u32 s92, s92, 1\n"
    "s_sleep 2\n"
    "s_sub_u32 s91, s68, 6\n"
    "s_and_b32 s91, s91, 7\n"
    "s_lshl_b32 s91, s91, 11\n"
    "s_add_u32 s42, s52, s91\n"
    "s_addc_u32 s43, s53, 0\n"
    "global_load_dwordx4 v[184:187], v6, s[42:43] sc1\n"
    "global_load_dwordx4 v[188:191], v6, s[42:43] offset:1024 sc1\n"
    "s_sub_u32 s91, s68, 7\n"
    "s_and_b32 s91, s91, 7\n"
    "s_lshl_b32 s91, s91, 11\n"
    "s_add_u32 s42, s52, s91\n"
    "s_addc_u32 s43, s53, 0\n"
    "global_load_dwordx4 v[192:195], v6, s[42:43] sc1\n"
    "global_load_dwordx4 v[196:199], v6, s[42:43] offset:1024 sc1\n"
    "s_waitcnt vmcnt(0)\n"
    "v_max3_u32 v74, v184, v185, v186\n"
    "v_max3_u32 v74, v74, v187, v188\n"
    "v_max3_u32 v74, v74, v189, v190\n"
    "v_max_u32 v74, v74, v191\n"
    "v_cmp_eq_u32 vcc, -1, v74\n"
    "s_cbranch_vccnz 140b\n"
    "s_branch 141b\n"
    "142:\n"
    "s_cmp_eq_u32 s92, 0\n"
    "s_cbranch_scc1 143b\n"
    "s_sub_u32 s92, s92, 1\n"
    "s_sleep 2\n"
    "s_sub_u32 s91, s68, 7\n"
    "s_and_b32 s91, s91, 7\n"
    "s_lshl_b32 s91, s91, 11\n"
    "s_add_u32 s42, s52, s91\n"
    "s_addc_u32 s43, s53, 0\n"
    "global_load_dwordx4 v[192:195], v6, s[42:43] sc1\n"
    "global_load_dwordx4 v[196:199], v6, s[42:43] offset:1024 sc1\n"
    "s_waitcnt vmcnt(0)\n"
    "v_max3_u32 v74, v192, v193, v194\n"
    "v_max3_u32 v74, v74, v195, v196\n"
    "v_max3_u32 v74, v74, v197, v198\n"
    "v_max_u32 v74, v74, v199\n"
    "v_cmp_eq_u32 vcc, -1, v74\n"
    "s_cbranch_vccnz 142b\n"
    "s_branch 143b\n"
    "99:\n"
    : "+{v[2:9]}"(vin), "+{v[38:53]}"(qa), "+{v[54:61]}"(qb), "+{v[62:93]}"(oa), "+{v[94:125]}"(ob), "+{s[36:51]}"(sa), "+{s[52:67]}"(sb), "+{s[68:83]}"(sc)
    :
    : "v10", "v11", "v12", "v13", "v14", "v15", "v16", "v17", "v18", "v19", "v20", "v21", "v22", "v23", "v24", "v25", "v26", "v27", "v28", "v29", "v30", "v31", "v32", "v33", "v34", "v35", "v36", "v37", "v126", "v127", "v128", "v129", "v130", "v131", "v132", "v133", "v134", "v135", "v136", "v137", "v138", "v139", "v140", "v141", "v142", "v143", "v144", "v145", "v146", "v147", "v148", "v149", "v150", "v151", "v152", "v153", "v154", "v155", "v156", "v157", "v158", "v159", "v160", "v161", "v162", "v163", "v164", "v165", "v166", "v167", "v168", "v169", "v170", "v171", "v172", "v173", "v174", "v175", "v176", "v177", "v178", "v179", "v180", "v181", "v182", "v183", "v184", "v185", "v186", "v187", "v188", "v189", "v190", "v191", "v192", "v193", "v194", "v195", "v196", "v197", "v198", "v199", "v200", "v201", "v202", "v203", "v204", "v205", "v206", "v207", "v208", "v209", "v210", "v211", "v212", "v213", "v214", "v215", "v216", "v217", "v218", "v219", "v220", "v221", "v222", "v223", "v224", "v225", "v226", "v227", "v228", "v229", "v230", "v231", "v232", "v233", "v234", "v235", "v236", "v237", "v238", "v239", "v240", "v241", "v242", "v243", "v244", "v245", "v246", "v247", "v248", "v249", "v250", "v251", "v252", "v253", "v254", "v255", "a0", "a1", "a2", "a3", "a4", "a5", "a6", "a7", "a8", "a9", "a10", "a11", "a12", "a13", "a14", "a15", "a16", "a17", "a18", "a19", "a20", "a21", "a22", "a23", "a24", "a25", "a26", "a27", "a28", "a29", "a30", "a31", "a32", "a33", "a34", "a35", "a36", "a37", "a38", "a39", "a40", "a41", "a42", "a43", "a44", "a45", "a46", "a47", "a48", "a49", "a50", "a51", "a52", "a53", "a54", "a55", "a56", "a57", "a58", "a59", "a60", "a61", "a62", "a63", "a64", "a65", "a66", "a67", "a68", "a69", "a70", "a71", "a72", "a73", "a74", "a75", "a76", "a77", "a78", "a79", "a80", "a81", "a82", "a83", "a84", "a85", "a86", "a87", "a88", "a89", "a90", "a91", "a92", "a93", "a94", "a95", "a96", "a97", "a98", "a99", "a100", "a101", "a102", "a103", "a104", "a105", "a106", "a107", "a108", "a109", "a110", "a111", "a112", "a113", "a114", "a115", "a116", "a117", "a118", "a119", "a120", "a121", "a122", "a123", "a124", "a125", "a126", "a127", "a128", "a129", "a130", "a131", "a132", "a133", "a134", "a135", "a136", "a137", "a138", "a139", "a140", "a141", "a142", "a143", "a144", "a145", "a146", "a147", "a148", "a149", "a150", "a151", "a152", "a153", "a154", "a155", "a156", "a157", "a158", "a159", "a160", "a161", "a162", "a163", "a164", "a165", "a166", "a167", "a168", "a169", "a170", "a171", "a172", "a173", "a174", "a175", "a176", "a177", "a178", "a179", "a180", "a181", "a182", "a183", "a184", "a185", "a186", "a187", "a188", "a189", "a190", "a191", "a192", "a193", "a194", "a195", "a196", "a197", "a198", "a199", "a200", "a201", "a202", "a203", "a204", "a205", "a206", "a207", "a208", "a209", "a210", "a211", "a212", "a213", "a214", "a215", "a216", "a217", "a218", "a219", "a220", "a221", "a222", "a223", "a224", "a225", "a226", "a227", "a228", "a229", "a230", "a231", "a232", "a233", "a234", "a235", "a236", "a237", "a238", "a239", "a240", "a241", "a242", "a243", "a244", "a245", "a246", "a247", "a248", "a249", "a250", "a251", "a252", "a253", "a254", "a255", "s84", "s85", "s86", "s87", "s88", "s89", "s90", "s91", "s92", "s93", "s94", "s95", "vcc", "scc", "memory");
}

extern "C" void kernel_launch(void* const* d_in, const int* in_sizes, int n_in, void* d_out, int out_size, void* d_ws,
                              size_t ws_size, hipStream_t stream) {
  const u16* x = (const u16*)d_in[0];
  const u16* wq = (const u16*)d_in[1];
  const u16* wk = (const u16*)d_in[2];
  const u16* wv = (const u16*)d_in[3];
  const u16* wo = (const u16*)d_in[4];
  const u16* w1 = (const u16*)d_in[6];
  const u16* b1 = (const u16*)d_in[7];
  const u16* w2 = (const u16*)d_in[8];
  const u16* b2 = (const u16*)d_in[9];
  unsigned char* ws = (unsigned char*)d_ws;
  (void)hipFuncSetAttribute((const void*)k_qkv, hipFuncAttributeMaxDynamicSharedMemorySize, K1_LDS);
  (void)hipFuncSetAttribute((const void*)k_main, hipFuncAttributeMaxDynamicSharedMemorySize, 163840);
  k_qkv<<<256, 512, K1_LDS, stream>>>(x, wq, wk, wv, wo, ws, w1, w2, b1, b2, (const u16*)d_out);
  k_main<<<256, 256, 163840, stream>>>(x, w1, b1, w2, b2, ws, (u16*)d_out);
}
```
